```python
import torch

def _softmax(x, axis=-1):
    return torch.softmax(x, dim=axis)

def _gelu(x):
    return torch.nn.functional.gelu(x, approximate='tanh')

def _silu(x):
    return torch.nn.functional.silu(x)

def _sigmoid(x):
    return torch.sigmoid(x)

def _layernorm(x, axis=-1, eps=1e-05):
    m = x.mean(dim=axis, keepdim=True)
    v = x.var(dim=axis, keepdim=True, unbiased=False)
    return (x - m) / torch.sqrt(v + eps)

def _scatter_add(tgt, idx, src, axis=0):
    return tgt.clone().index_add_(axis, idx, src)

def _segment_sum(src, seg, n):
    out = torch.zeros((n,) + src.shape[1:], dtype=src.dtype)
    return out.index_add_(0, seg, src)

def _pad(x, pw):
    flat = tuple((v for pair in reversed(pw) for v in pair))
    return torch.nn.functional.pad(x, flat)
kernel_name = 'adaLN_cond__ssm_conv_s4__dense_activation_silu__prompt'

def setup_inputs():

    _w = lambda a: (lambda t: t.to(torch.bfloat16) if t.is_floating_point() else t)(torch.as_tensor(a))

    def _A_setup():
        B, L, D, N = (1, 2048, 448, 256)
        A_re = -(lambda _x: torch.exp(torch.as_tensor(_x)))((lambda *s: torch.randn(*s, dtype=torch.bfloat16) * 3.2)(D, N))
        A_im = torch.pi * (lambda *s: torch.randn(*s, dtype=torch.bfloat16) * 0.05)(D, N)
        B_re = (lambda *s: torch.randn(*s, dtype=torch.bfloat16) * 0.1)(D, N)
        B_im = (lambda *s: torch.randn(*s, dtype=torch.bfloat16) * 0.1)(D, N)
        C_re = (lambda *s: torch.randn(*s, dtype=torch.bfloat16) * 0.1)(D, N)
        C_im = (lambda *s: torch.randn(*s, dtype=torch.bfloat16) * 0.1)(D, N)
        log_dt = (lambda *s: torch.randn(*s, dtype=torch.bfloat16) * 0.05)(D) * 0.5 - 4.0
        Dskip = (lambda *s: torch.randn(*s, dtype=torch.bfloat16) * 0.05)(D)
        return {'u': _w((lambda *s: torch.randn(*s, dtype=torch.bfloat16) * 0.05)(B, L, D)), 'A_re': _w(A_re), 'A_im': _w(A_im), 'B_re': _w(B_re), 'B_im': _w(B_im), 'C_re': _w(C_re), 'C_im': _w(C_im), 'log_dt': _w(log_dt), 'Dskip': _w(Dskip)}
    _A_in = _A_setup()

    def _B_setup():
        B, S, D, F = (1, 2048, 448, 1792)
        return {'x': _w((lambda *s: torch.randn(*s, dtype=torch.bfloat16) * 0.05)(B, S, D)), 'w1': _w((lambda *s: torch.randn(*s, dtype=torch.bfloat16) * 3.2)(D, F) * D ** (-0.5)), 'w2': _w((lambda *s: torch.randn(*s, dtype=torch.bfloat16) * 0.4)(F, D) * F ** (-0.5))}
    _B_in = _B_setup()
    return {'A_u': _A_in['u'], 'A_A_re': _A_in['A_re'], 'A_A_im': _A_in['A_im'], 'A_B_re': _A_in['B_re'], 'A_B_im': _A_in['B_im'], 'A_C_re': _A_in['C_re'], 'A_C_im': _A_in['C_im'], 'A_log_dt': _A_in['log_dt'], 'A_Dskip': _A_in['Dskip'], 'B_x': _B_in['x'], 'B_w1': _B_in['w1'], 'B_w2': _B_in['w2'], 'c': _w((lambda *s: torch.randn(*s, dtype=torch.bfloat16) * 0.2)(1, 448) * 8.0), 'w_mod': _w((lambda *s: torch.randn(*s, dtype=torch.bfloat16) * 0.2)(448, 6 * 448) * 448 ** (-0.5) * 8.0)}

def reference(A_u, A_A_re, A_A_im, A_B_re, A_B_im, A_C_re, A_C_im, A_log_dt, A_Dskip, B_x, B_w1, B_w2, c, w_mod):

    def _A_ref(u, A_re, A_im, B_re, B_im, C_re, C_im, log_dt, Dskip):
        Bsz, L, D = u.shape
        dt = torch.exp(log_dt)[:, None]
        A = A_re + 1j * A_im
        Bc = B_re + 1j * B_im
        Cc = C_re + 1j * C_im
        logdA = dt * A
        dB = dt * Bc
        l = (lambda x, dt: x.to(dt))(torch.arange(L), A_re.dtype)
        P = torch.exp(logdA[:, :, None] * l[None, None, :])
        K = torch.einsum('dn,dnl,dn->dl', Cc, P, dB).real
        L2 = 2 * L
        Kp = _pad(K, ((0, 0), (0, L)))
        up = (lambda x, *d: x.permute(*d))(u, 0, 2, 1)
        up = _pad(up, ((0, 0), (0, 0), (0, L)))
        Kf = (lambda x, axes=(-2, -1): torch.fft.rfft2(x.float(), dim=axes))(Kp[:, None, :], axes=(-2, -1))
        Uf = (lambda x, axes=(-2, -1): torch.fft.rfft2(x.float(), dim=axes))(up[:, :, None, :], axes=(-2, -1))
        Yf = Uf * Kf[None, :, :, :]
        y = (lambda x, s, axes=(-2, -1): torch.fft.irfft2(x, s=s, dim=axes).to(torch.bfloat16))(Yf, (1, L2), axes=(-2, -1))[:, :, 0, :L]
        y = (lambda x, *d: x.permute(*d))(y, 0, 2, 1) + u * Dskip[None, None, :]
        return y

    def _B_ref(x, w1, w2):
        h = torch.einsum('bsd,df->bsf', x, w1)
        h = _silu(h)
        out = torch.einsum('bsf,fd->bsd', h, w2)
        return out
    x = A_u
    mod = (_silu(c) @ w_mod).reshape(c.shape[0], 6, 1, -1)
    sa, ca, ga, sb, cb, gb = (mod[:, 0].clone(), mod[:, 1].clone(), mod[:, 2].clone(), mod[:, 3].clone(), mod[:, 4].clone(), mod[:, 5].clone())
    h = _layernorm(x, axis=-1) * (1 + ca) + sa
    x = x + ga * _A_ref(h, A_A_re, A_A_im, A_B_re, A_B_im, A_C_re, A_C_im, A_log_dt, A_Dskip)
    h = _layernorm(x, axis=-1) * (1 + cb) + sb
    x = x + gb * _B_ref(h, B_w1, B_w2)
    return x
```

```cpp
#include <hip/hip_runtime.h>
#include <hip/hip_bf16.h>
#include <stdint.h>

typedef unsigned short u16;
constexpr int L = 2048, D = 448, N = 256, F = 1792;

__device__ __forceinline__ float bf2f(u16 v) { return __uint_as_float(((unsigned)v) << 16); }
__device__ __forceinline__ u16 f2bf(float f) { __bf16 b = (__bf16)f; return __builtin_bit_cast(u16, b); }
__device__ __forceinline__ float rbf(float f) { return bf2f(f2bf(f)); }

__global__ void k_mod(const u16* c, const u16* wmod, float* mod) {
  int j = blockIdx.x * blockDim.x + threadIdx.x;
  if (j >= 6 * D) return;
  float acc = 0.f;
  for (int i = 0; i < D; ++i) {
    float cv = bf2f(c[i]);
    float s = rbf(cv / (1.f + __expf(-cv)));
    acc += s * bf2f(wmod[i * 6 * D + j]);
  }
  mod[j] = rbf(acc);
}

__global__ void k_ln(const u16* x, const float* shift, const float* scale, u16* h) {
  int row = blockIdx.x; int lane = threadIdx.x;
  float v[7]; float s = 0.f;
  for (int i = 0; i < 7; ++i) { v[i] = bf2f(x[row * D + lane + 64 * i]); s += v[i]; }
  for (int o = 32; o; o >>= 1) s += __shfl_xor(s, o);
  float mean = s / D; float q = 0.f;
  for (int i = 0; i < 7; ++i) { float t = v[i] - mean; q += t * t; }
  for (int o = 32; o; o >>= 1) q += __shfl_xor(q, o);
  float m = rbf(mean), var = rbf(q / D);
  float sd = rbf(sqrtf(rbf(var + 1e-5f)));
  for (int i = 0; i < 7; ++i) {
    int d = lane + 64 * i;
    float z = rbf(rbf(v[i] - m) / sd);
    float r = rbf(rbf(z * rbf(1.f + scale[d])) + shift[d]);
    h[row * D + d] = f2bf(r);
  }
}

__global__ void k_kgen(const u16* Are, const u16* Aim, const u16* Bre, const u16* Bim, const u16* Cre, const u16* Cim, const u16* logdt, float* K) {
  int d = blockIdx.y; int l = blockIdx.x * blockDim.x + threadIdx.x;
  float dt = rbf(expf(bf2f(logdt[d])));
  float lb = rbf((float)l);
  float acc = 0.f;
  for (int n = 0; n < N; ++n) {
    int i = d * N + n;
    float ar = dt * bf2f(Are[i]), ai = dt * bf2f(Aim[i]);
    float br = dt * bf2f(Bre[i]), bi = dt * bf2f(Bim[i]);
    float cr = bf2f(Cre[i]), ci = bf2f(Cim[i]);
    float e = expf(ar * lb); float sn, cs; sincosf(ai * lb, &sn, &cs);
    float pr = e * cs, pi = e * sn;
    float wr = cr * br - ci * bi, wi = cr * bi + ci * br;
    acc += wr * pr - wi * pi;
  }
  K[d * L + l] = acc;
}

__global__ void k_conv(const u16* x, const u16* h, const float* K, const u16* Dskip, const float* ga, u16* x1) {
  int l = blockIdx.x; int d = threadIdx.x;
  float acc = 0.f;
  for (int j = 0; j <= l; ++j) acc += K[d * L + j] * bf2f(h[(l - j) * D + d]);
  float y = rbf(rbf(acc) + rbf(bf2f(h[l * D + d]) * bf2f(Dskip[d])));
  x1[l * D + d] = f2bf(bf2f(x[l * D + d]) + rbf(ga[d] * y));
}

__global__ void k_g1(const u16* h, const u16* w1, u16* H) {
  int l = blockIdx.y; int f = blockIdx.x * blockDim.x + threadIdx.x;
  float acc = 0.f;
  for (int d = 0; d < D; ++d) acc += bf2f(h[l * D + d]) * bf2f(w1[d * F + f]);
  float a = rbf(acc);
  H[l * F + f] = f2bf(a / (1.f + __expf(-a)));
}

__global__ void k_g2(const u16* H, const u16* w2, const u16* x1, const float* gb, u16* out) {
  int l = blockIdx.x; int d = threadIdx.x;
  float acc = 0.f;
  for (int f = 0; f < F; ++f) acc += bf2f(H[l * F + f]) * bf2f(w2[f * D + d]);
  out[l * D + d] = f2bf(bf2f(x1[l * D + d]) + rbf(gb[d] * rbf(acc)));
}

extern "C" void kernel_launch(void* const* d_in, const int* in_sizes, int n_in,
                              void* d_out, int out_size, void* d_ws, size_t ws_size,
                              hipStream_t stream) {
  const u16* x = (const u16*)d_in[0];
  const u16 *Are = (const u16*)d_in[1], *Aim = (const u16*)d_in[2], *Bre = (const u16*)d_in[3], *Bim = (const u16*)d_in[4];
  const u16 *Cre = (const u16*)d_in[5], *Cim = (const u16*)d_in[6], *logdt = (const u16*)d_in[7], *Dskip = (const u16*)d_in[8];
  const u16 *w1 = (const u16*)d_in[10], *w2 = (const u16*)d_in[11], *c = (const u16*)d_in[12], *wmod = (const u16*)d_in[13];
  char* ws = (char*)d_ws;
  float* mod = (float*)ws;
  u16* h = (u16*)(ws + (1 << 20));
  float* K = (float*)(ws + (4 << 20));
  u16* x1 = (u16*)(ws + (8 << 20));
  u16* h2 = (u16*)(ws + (12 << 20));
  u16* H = (u16*)(ws + (16 << 20));
  k_mod<<<(6 * D + 63) / 64, 64, 0, stream>>>(c, wmod, mod);
  k_ln<<<L, 64, 0, stream>>>(x, mod + 0 * D, mod + 1 * D, h);
  k_kgen<<<dim3(L / 256, D), 256, 0, stream>>>(Are, Aim, Bre, Bim, Cre, Cim, logdt, K);
  k_conv<<<L, D, 0, stream>>>(x, h, K, Dskip, mod + 2 * D, x1);
  k_ln<<<L, 64, 0, stream>>>(x1, mod + 3 * D, mod + 4 * D, h2);
  k_g1<<<dim3(F / 256, L), 256, 0, stream>>>(h2, w1, H);
  k_g2<<<L, D, 0, stream>>>(H, w2, x1, mod + 5 * D, (u16*)d_out);
}
```

```cpp
#include <hip/hip_runtime.h>
#include <stdint.h>

typedef unsigned short u16;
typedef unsigned u32;
typedef unsigned long long u64;
typedef u32 v4u __attribute__((ext_vector_type(4)));
typedef u32 v2u __attribute__((ext_vector_type(2)));
typedef float v4f __attribute__((ext_vector_type(4)));
typedef float v2f __attribute__((ext_vector_type(2)));
typedef float v16f __attribute__((ext_vector_type(16)));
typedef __bf16 bf8 __attribute__((ext_vector_type(8)));
typedef __bf16 bf2 __attribute__((ext_vector_type(2)));
typedef short s4 __attribute__((ext_vector_type(4)));
#define GAS __attribute__((address_space(1)))
#define LAS __attribute__((address_space(3)))
#define DEV __device__ __forceinline__

constexpr int L = 2048, D = 448, F = 1792;

constexpr int OFF_STAT = 4096;
constexpr int OFF_MOD = 20480;
constexpr int OFF_UV = 32768;
constexpr int OFF_WFL = 65536;
constexpr int OFF_XT = 131072;
constexpr int OFF_W1T = 4 << 20;
constexpr int OFF_W2T = 6 << 20;
constexpr int OFF_X1 = 8 << 20;
constexpr int OFF_H = 12 << 20;
constexpr int OFF_DBG = 24 << 20;
constexpr int WS_BYTES = 32 << 20;
constexpr int XTILE = 57344, HTILE = 114688;

constexpr int KG_W = 8704;
constexpr int L_KC = 0, KC_S = 4160, KC_CH = 4 * KC_S;
constexpr int L_CUM = 33280;
constexpr int L_DUMP0 = 49664;
constexpr int L_LNT = 69632;
constexpr int L_STX = 76800;
constexpr int L_SC = 76832;
constexpr int L_KLIN = 76864, KLIN_CH = 2576;
constexpr int L_Z = 82048, Z_CH = 4608;
constexpr int L_XT = 91264;
constexpr int L_DUMP1 = 99456;
constexpr int L_SCAN = 115840;
constexpr int L_XA = 0, L_XB = XTILE;
constexpr int RED_W = 8448, L_REDA = 0, L_REDB = 8 * RED_W;
constexpr int L_MOD = 135168;
constexpr int L_RES = 140544, RES_P = 4352;
constexpr int L_ST = 149248, ST_P = 2560;
constexpr int LDS_BYTES = 154368;
constexpr int L_ASC = 0;
constexpr int L_ARED = 2048;
constexpr int L_AG = 8192;
constexpr int L_ARED2 = 12288;

constexpr u32 SPIN_CAP = 1u << 14;
constexpr int NW2A = 18;

DEV float blo(u32 v) { return __uint_as_float(v << 16); }
DEV float bhi(u32 v) { return __uint_as_float(v & 0xffff0000u); }
DEV u32 pk(float a, float b) { bf2 r = {(__bf16)a, (__bf16)b}; return __builtin_bit_cast(u32, r); }
DEV float rbf(float a) { return blo(pk(a, a)); }
DEV u32 umin(u32 a, u32 b) { return a < b ? a : b; }
DEV u32 umax(u32 a, u32 b) { return a > b ? a : b; }
DEV u32 max4(u32 m, v4u v) { return umax(umax(umax(m, v.x), v.y), umax(v.z, v.w)); }
DEV v4u nosent(v4u v) { v4u r = {umin(v.x, 0xfffffffeu), umin(v.y, 0xfffffffeu), umin(v.z, 0xfffffffeu), umin(v.w, 0xfffffffeu)}; return r; }
DEV v4f mfma16(v4u a, v4u b, v4f c) { return __builtin_amdgcn_mfma_f32_16x16x32_bf16(__builtin_bit_cast(bf8, a), __builtin_bit_cast(bf8, b), c, 0, 0, 0); }
DEV v16f mfma32(v4u a, v4u b, v16f c) { return __builtin_amdgcn_mfma_f32_32x32x16_bf16(__builtin_bit_cast(bf8, a), __builtin_bit_cast(bf8, b), c, 0, 0, 0); }
DEV float dot2(u32 a, u32 b, float c) { return __builtin_amdgcn_fdot2_f32_bf16(__builtin_bit_cast(bf2, a), __builtin_bit_cast(bf2, b), c, false); }
DEV v2u trd(LAS char* p) { return __builtin_bit_cast(v2u, __builtin_amdgcn_ds_read_tr16_b64_v4i16((LAS s4*)p)); }
DEV v4u trfrag(LAS char* p, int second) { v2u a = trd(p), b = trd(p + second); v4u r = {a.x, a.y, b.x, b.y}; return r; }
template <int CTRL, int RM> DEV float dppadd(float v) {
  return v + __builtin_bit_cast(float, __builtin_amdgcn_update_dpp(0, __builtin_bit_cast(int, v), CTRL, RM, 0xf, false));
}
DEV float wave_scan(float v) {
  v = dppadd<0x111, 0xf>(v); v = dppadd<0x112, 0xf>(v); v = dppadd<0x114, 0xf>(v); v = dppadd<0x118, 0xf>(v);
  v = dppadd<0x142, 0xa>(v); v = dppadd<0x143, 0xc>(v);
  return v;
}
DEV float wave_sum(float v) { return __builtin_bit_cast(float, __builtin_amdgcn_readlane(__builtin_bit_cast(int, wave_scan(v)), 63)); }
DEV float swap32_add(float v) { auto r = __builtin_amdgcn_permlane32_swap(__float_as_uint(v), __float_as_uint(v), false, false); return __uint_as_float(r[0]) + __uint_as_float(r[1]); }
DEV float swap16_add(float v) { auto r = __builtin_amdgcn_permlane16_swap(__float_as_uint(v), __float_as_uint(v), false, false); return __uint_as_float(r[0]) + __uint_as_float(r[1]); }

DEV int opq(int v) { asm volatile("" : "+v"(v)); return v; }
#define BAR() asm volatile("s_waitcnt lgkmcnt(0)\n\ts_barrier" ::: "memory")
#define VMW(n) asm volatile("s_waitcnt vmcnt(" #n ")" ::: "memory")
#define MEMB() asm volatile("" ::: "memory")
#define SB() __builtin_amdgcn_sched_barrier(0)
#define LDS128(off) (*(LAS v4u*)(lds + (off)))
#define LDS128F(off) (*(LAS v4f*)(lds + (off)))
#define LDS64(off) (*(LAS v2u*)(lds + (off)))
#define LDS32(off) (*(LAS u32*)(lds + (off)))
#define LDS32F(off) (*(LAS float*)(lds + (off)))
#define LDS16(off) (*(LAS u16*)(lds + (off)))
#define GLD(off) __builtin_amdgcn_raw_buffer_load_b128(rs, (off), 0, 16)
#define GLDS(voff, soff) __builtin_amdgcn_raw_buffer_load_b128(rs, (voff), (soff), 16)
#define GST(v, off) __builtin_amdgcn_raw_buffer_store_b128((v), rs, (off), 0, 16)
#define GSTS(v, voff, soff) __builtin_amdgcn_raw_buffer_store_b128((v), rs, (voff) + (soff), 0, 16)

DEV void dma16(u32 ldsaddr, const char* gbase, u32 voff) {
  asm volatile("s_nop 4\n\ts_mov_b32 m0, %0\n\ts_nop 0\n\tglobal_load_lds_dwordx4 %1, %2" :: "s"(ldsaddr), "v"(voff), "s"(gbase) : "memory");
}
DEV void dma16_dev(u32 ldsaddr, const char* gbase, u32 voff) {
  asm volatile("s_nop 4\n\ts_mov_b32 m0, %0\n\ts_nop 0\n\tglobal_load_lds_dwordx4 %1, %2 sc1" :: "s"(ldsaddr), "v"(voff), "s"(gbase) : "memory");
}

#define TS(i)

struct cf { float r, i; };
DEV cf cmul(cf a, cf b) { cf c; c.r = a.r * b.r - a.i * b.i; c.i = a.r * b.i + a.i * b.r; return c; }
DEV cf cexpx(float er, float ei, float x) {
  float e = __builtin_amdgcn_exp2f(er * x), ph = __builtin_amdgcn_fractf(ei * x);
  cf c; c.r = e * __builtin_amdgcn_cosf(ph); c.i = e * __builtin_amdgcn_sinf(ph); return c;
}

DEV void ln1_publish(LAS char* lds, __amdgpu_buffer_rsrc_t rs, v4u xr, int b, int tid, int lane, int wave, u32 tagL, u32 tagH) {
  float v[8] = {blo(xr.x), bhi(xr.x), blo(xr.y), bhi(xr.y), blo(xr.z), bhi(xr.z), blo(xr.w), bhi(xr.w)};
  if (lane < 56) {
    const int o = L_LNT + ((wave >> 1) * 224 + 4 * lane) * 8 + (wave & 1) * 4;
    LDS32(o) = xr.x; LDS32(o + 8) = xr.y; LDS32(o + 16) = xr.z; LDS32(o + 24) = xr.w;
  }
  float s = ((v[0] + v[1]) + (v[2] + v[3])) + ((v[4] + v[5]) + (v[6] + v[7]));
  const float mean = wave_sum(s) * (1.f / D);
  float q = 0.f;
#pragma unroll
  for (int i = 0; i < 8; ++i) { float t = v[i] - mean; q += t * t; }
  if (lane >= 56) q = 0.f;
  const float var = rbf(wave_sum(q) * (1.f / D));
  const float sd = __builtin_amdgcn_sqrtf(rbf(var + 1e-5f));
  if (lane == 0) LDS32(L_STX + wave * 4) = pk(mean, sd);
  BAR();
  {
    v2u g = LDS64(L_LNT + ((tid & 3) * 224 + (tid >> 2)) * 8);
    v4u o = {tagL, g.x, g.y, tagH};
    GST(o, OFF_XT + (tid >> 2) * 16384 + (4 * b + (tid & 3)) * 16);
  }
  if (wave < 6) {
    v2u g = LDS64(L_LNT + ((tid & 3) * 224 + 128 + (tid >> 2)) * 8);
    v4u o = {tagL, g.x, g.y, tagH};
    GST(o, OFF_XT + (128 + (tid >> 2)) * 16384 + (4 * b + (tid & 3)) * 16);
  } else if (wave == 7 && lane < 4) {
    v2u g = LDS64(L_STX + lane * 8);
    v4u o = {tagL, g.x, g.y, tagH};
    GST(o, OFF_STAT + (4 * b + lane) * 16);
  }
}

DEV bool tagok(v4u g, u32 tagL, u32 tagH) { return g.x == tagL && g.w == tagH; }

DEV void conv_epi(LAS char* lds, __amdgpu_buffer_rsrc_t rs, int dump, int ch, int tl, int t, int d, float dk, int lane) {
  const int c = lane & 15, g = lane >> 4;
  const v4f p0 = LDS128F(dump + ((ch * 2 + 0) * 4 + tl) * 1024 + lane * 16), p1 = LDS128F(dump + ((ch * 2 + 1) * 4 + tl) * 1024 + lane * 16);
  const int l0 = 256 * t + 16 * c + 4 * g;
  const v4f ck = LDS128F(L_CUM + ch * 8192 + l0 * 4);
  const v2u zz = LDS64(L_Z + ch * Z_CH + 480 + l0 * 2), xx = LDS64(L_XT + ch * 4096 + l0 * 2);
  const float sa = blo(LDS16(L_MOD + (0 * D + d) * 2)), ca = blo(LDS16(L_MOD + (1 * D + d) * 2));
  const float ga = blo(LDS16(L_MOD + (2 * D + d) * 2)), cb = blo(LDS16(L_MOD + (4 * D + d) * 2));
  const float g1 = rbf(1.f + ca), g2 = rbf(1.f + cb);
  const float a1 = ga * g1, a2 = ga * sa, a3 = a1 * dk, a4 = a2 * dk;
  const float z4[4] = {blo(zz.x), bhi(zz.x), blo(zz.y), bhi(zz.y)}, x4[4] = {blo(xx.x), bhi(xx.x), blo(xx.y), bhi(xx.y)};
  float o[4];
#pragma unroll
  for (int e = 0; e < 4; ++e) o[e] = x4[e] + (a1 * (p0[e] + p1[e]) + (a2 * ck[e] + (a3 * z4[e] + a4)));
  const u32 X0 = pk(o[0], o[1]), X1 = pk(o[2], o[3]);
  const u32 S0 = pk(blo(X0) * g2, bhi(X0) * g2), S1 = pk(blo(X1) * g2, bhi(X1) * g2);
  auto r0 = __builtin_amdgcn_permlane16_swap(X0, S0, false, false);
  auto r1 = __builtin_amdgcn_permlane16_swap(X1, S1, false, false);
  v4u v = {r0[0], r1[0], r0[1], r1[1]};
  const int off = OFF_X1 + (8 * t + (c >> 1)) * XTILE + ((g & 1) ? 0 : XTILE / 2) + d * 64 + (c & 1) * 32 + (g >> 1) * 16;
  GST(nosent(v), off);
}

__global__ void __launch_bounds__(512, 2) k_all(const u16* __restrict__ x, const u16* __restrict__ Are, const u16* __restrict__ Aim,
    const u16* __restrict__ Bre, const u16* __restrict__ Bim, const u16* __restrict__ Cre, const u16* __restrict__ Cim,
    const u16* __restrict__ logdt, const u16* __restrict__ Dskip, const u16* __restrict__ w1, const u16* __restrict__ w2,
    const u16* __restrict__ cvec, const u16* __restrict__ wmod, u16* __restrict__ out, char* ws) {
  __shared__ __attribute__((aligned(16))) char smem[LDS_BYTES];
  LAS char* lds = (LAS char*)smem;
  const u32 lds0 = (u32)(size_t)lds;
  const int tid = threadIdx.x, lane = tid & 63, wave = __builtin_amdgcn_readfirstlane(tid >> 6);
  const int b = blockIdx.x, xcd = b & 7, j = b >> 3;
  TS(0);
  const __amdgpu_buffer_rsrc_t rs = __builtin_amdgcn_make_buffer_rsrc(ws, 0, WS_BYTES, 0x00020000);
  const u64 T = *(const GAS u64*)ws + 0x9E3779B97F4A7C15ull;
  const u32 tagL = (u32)T, tagH = (u32)(T >> 32);

  v4u xr = {0, 0, 0, 0};
  if (lane < 56) xr = *(const GAS v4u*)(x + (8 * b + wave) * D + lane * 8);

  if (j >= 28) {
    const int a = (j - 28) * 8 + xcd;
    u32 cv = 0;
    if (tid < D) cv = cvec[tid];
    v4u wm[14], wu[7];
    const int rg = 4 * wave + (lane >> 4);
    if (a < 21) {
#pragma unroll
      for (int i = 0; i < 14; ++i) wm[i] = *(const GAS v4u*)(wmod + (rg + 32 * i) * (6 * D) + 128 * a + (lane & 15) * 8);
    }
    const int ru = 8 * wave + (lane >> 3);
    if (a >= 4) {
#pragma unroll
      for (int i = 0; i < 7; ++i) wu[i] = *(const GAS v4u*)(w1 + (ru + 64 * i) * F + 64 * (a - 4) + (lane & 7) * 8);
    }
    SB();
    {
      const float cf_ = blo(cv);
      if (tid < D) LDS32F(L_ASC + tid * 4) = rbf(cf_ / (1.f + __expf(-cf_)));
    }
    ln1_publish(lds, rs, xr, b, tid, lane, wave, tagL, tagH);
    if (a < 21) {
      float acc[8] = {0, 0, 0, 0, 0, 0, 0, 0};
#pragma unroll
      for (int i = 0; i < 14; ++i) {
        const float s = LDS32F(L_ASC + (rg + 32 * i) * 4);
        acc[0] += s * blo(wm[i].x); acc[1] += s * bhi(wm[i].x); acc[2] += s * blo(wm[i].y); acc[3] += s * bhi(wm[i].y);
        acc[4] += s * blo(wm[i].z); acc[5] += s * bhi(wm[i].z); acc[6] += s * blo(wm[i].w); acc[7] += s * bhi(wm[i].w);
      }
#pragma unroll
      for (int e = 0; e < 8; ++e) acc[e] = swap32_add(swap16_add(acc[e]));
      if (lane < 16) {
        v4f lo = {acc[0], acc[1], acc[2], acc[3]}, hi = {acc[4], acc[5], acc[6], acc[7]};
        LDS128F(L_ARED + wave * 512 + lane * 32) = lo; LDS128F(L_ARED + wave * 512 + lane * 32 + 16) = hi;
      }
      BAR();
      if (tid < 32) {
        v4f s = LDS128F(L_ARED + tid * 16);
#pragma unroll
        for (int w = 1; w < 8; ++w) s += LDS128F(L_ARED + w * 512 + tid * 16);
        v4u o = {tagL, pk(s[0], s[1]), pk(s[2], s[3]), tagH};
        GST(o, OFF_MOD + (32 * a + tid) * 16);
      }
    }
    if (a >= 4) {
      if (wave < 4) {
        const int gi = 336 + (tid < 224 ? tid : 0);
        v4u g;
        _Pragma("unroll 1") for (u32 sp = 0; sp < SPIN_CAP; ++sp) {
          MEMB();
          g = GLD(OFF_MOD + gi * 16);
          if (__all(tagok(g, tagL, tagH))) break;
          __builtin_amdgcn_s_sleep(8);
        }
        if (tid < 224) {
          v4f f = {blo(g.y), bhi(g.y), blo(g.z), bhi(g.z)};
          if (tid >= 112) { f[0] = rbf(1.f + f[0]); f[1] = rbf(1.f + f[1]); f[2] = rbf(1.f + f[2]); f[3] = rbf(1.f + f[3]); }
          LDS128F(L_AG + tid * 16) = f;
        }
      }
      BAR();
      float au[8] = {0, 0, 0, 0, 0, 0, 0, 0}, av[8] = {0, 0, 0, 0, 0, 0, 0, 0};
#pragma unroll
      for (int i = 0; i < 7; ++i) {
        const float sb = LDS32F(L_AG + (ru + 64 * i) * 4), g2 = LDS32F(L_AG + (D + ru + 64 * i) * 4);
        const float w[8] = {blo(wu[i].x), bhi(wu[i].x), blo(wu[i].y), bhi(wu[i].y), blo(wu[i].z), bhi(wu[i].z), blo(wu[i].w), bhi(wu[i].w)};
#pragma unroll
        for (int e = 0; e < 8; ++e) { au[e] += g2 * w[e]; av[e] += sb * w[e]; }
      }
#pragma unroll
      for (int e = 0; e < 8; ++e) {
        au[e] = swap32_add(swap16_add(dppadd<0x128, 0xf>(au[e])));
        av[e] = swap32_add(swap16_add(dppadd<0x128, 0xf>(av[e])));
      }
      if (lane < 8) {
        v4f u0 = {au[0], av[0], au[1], av[1]}, u1 = {au[2], av[2], au[3], av[3]}, u2 = {au[4], av[4], au[5], av[5]}, u3 = {au[6], av[6], au[7], av[7]};
        const int o = L_ARED2 + wave * 512 + lane * 64;
        LDS128F(o) = u0; LDS128F(o + 16) = u1; LDS128F(o + 32) = u2; LDS128F(o + 48) = u3;
      }
      BAR();
      if (tid < 64) {
        v2f s = *(LAS v2f*)(lds + L_ARED2 + tid * 8);
#pragma unroll
        for (int w = 1; w < 8; ++w) s += *(LAS v2f*)(lds + L_ARED2 + w * 512 + tid * 8);
        v4u o = {tagL, __float_as_uint(s[0]), __float_as_uint(s[1]), tagH};
        GST(o, OFF_UV + (64 * (a - 4) + tid) * 16);
      }
    }
    return;
  }

  const int p = xcd * 28 + j;
  const int grp = xcd * 4 + j / 7;
  const int sl = j % 7;
  const int chw = wave >> 2;
  const v4u SENT = {0xffffffffu, 0xffffffffu, 0xffffffffu, 0xffffffffu};

  const int pi = (2 * p + chw) * 256 + (tid & 255);
  const u32 pAr = Are[pi], pAi = Aim[pi], pBr = Bre[pi], pBi = Bim[pi], pCr = Cre[pi], pCi = Cim[pi];
  const u32 pLd = logdt[2 * p + chw];
  const u32 dk2 = *(const GAS u32*)(Dskip + 2 * p);
  const int unit = p * 7 + wave;
  const bool isw1 = unit < 784;
  int wt_dst = 0;
  if (wave < 7) {
    const char* src; int rstride;
    if (isw1) {
      const int kap = unit % 28, fg = unit / 28;
      src = (const char*)w1 + (16 * kap) * (F * 2) + fg * 128; rstride = F * 2;
      wt_dst = OFF_W1T + (((fg >> 2) * 8 + 2 * (fg & 3)) * 28 + kap) * 1024;
    } else {
      const int v = unit - 784, kk = v / 7, s2 = v % 7;
      src = (const char*)w2 + (16 * kk) * (D * 2) + s2 * 128; rstride = D * 2;
      wt_dst = OFF_W2T + (((s2 * 8 + kk / 14) * 14 + kk % 14) * 2) * 1024;
    }
    const u32 vo = (lane >> 3) * rstride + (lane & 7) * 16;
    dma16(lds0 + wave * KG_W, src, vo);
    dma16(lds0 + wave * KG_W + 1024, src + 8 * rstride, vo);
  }
  SB();
  {
    const int i0 = tid;
    GST(SENT, OFF_X1 + (i0 >> 3) * (XTILE / 2) + p * 128 + (i0 & 7) * 16);
    GST(SENT, OFF_X1 + (64 + (i0 >> 3)) * (XTILE / 2) + p * 128 + (i0 & 7) * 16);
    const int ho = OFF_H + grp * HTILE + (16 * sl + 2 * wave) * 1024;
    GSTS(SENT, lane * 16, ho); GSTS(SENT, lane * 16 + 1024, ho);
    GSTS(SENT, lane * 16, ho + 32 * HTILE); GSTS(SENT, lane * 16 + 1024, ho + 32 * HTILE);
  }
  SB();
  ln1_publish(lds, rs, xr, b, tid, lane, wave, tagL, tagH);
  TS(1);

  const int h = lane >> 5, trq = (lane & 15) >> 2, trc = ((lane >> 4) & 1) * 32 + (lane & 3) * 8;
  if (wave < 7) {
    asm volatile("s_waitcnt vmcnt(7)" ::: "memory");
    const int r0 = isw1 ? 8 * h + trq : 4 * h + trq, r1 = isw1 ? 512 : 1024;
    LAS char* pb = lds + wave * KG_W + r0 * 128 + trc;
    v2u a0 = trd(pb), a1 = trd(pb + r1), b0 = trd(pb + 64), b1 = trd(pb + 64 + r1);
    v4u f0 = {a0.x, a0.y, a1.x, a1.y}, f1 = {b0.x, b0.y, b1.x, b1.y};
    GSTS(f0, lane * 16, wt_dst);
    GSTS(f1, lane * 16, wt_dst + (isw1 ? 28 * 1024 : 1024));
  }
  MEMB();

  {
    const float dt = rbf(expf(blo(pLd)));
    const float er = (dt * blo(pAr)) * 1.4426950408889634f, ei = (dt * blo(pAi)) * 0.15915494309189535f;
    cf dB = {dt * blo(pBr), dt * blo(pBi)}, Cc = {blo(pCr), blo(pCi)};
    cf X = cmul(Cc, dB);
    const int wo = wave * KG_W + lane * 4;
    const cf e1 = cexpx(er, ei, 1.f), e16 = cexpx(er, ei, 16.f), e512 = cexpx(er, ei, 512.f);
    cf Y = {1.f, 0.f}, Y4, Y8, Y12;
    LDS32(wo) = 0x00003f80u;
    Y = e1;
#pragma unroll
    for (int jj = 1; jj < 16; ++jj) {
      LDS32(wo + jj * 272) = pk(Y.r, Y.i);
      if (jj == 4) Y4 = Y;
      if (jj == 8) Y8 = Y;
      if (jj == 12) Y12 = Y;
      if (jj < 15) Y = cmul(Y, e1);
    }
    cf t;
    LDS32(wo + 16 * 272) = pk(e512.r, e512.i);
    t = cmul(e512, Y4);  LDS32(wo + 17 * 272) = pk(t.r, t.i);
    t = cmul(e512, Y8);  LDS32(wo + 18 * 272) = pk(t.r, t.i);
    t = cmul(e512, Y12); LDS32(wo + 19 * 272) = pk(t.r, t.i);
    const cf e1024 = cmul(e512, e512), e1536 = cmul(e1024, e512), e2048 = cmul(e1024, e1024);
    LDS32(wo + 20 * 272) = pk(e1024.r, e1024.i);
    t = cmul(e1024, Y8); LDS32(wo + 21 * 272) = pk(t.r, t.i);
    LDS32(wo + 22 * 272) = pk(e1536.r, e1536.i);
    t = cmul(e1536, Y8); LDS32(wo + 23 * 272) = pk(t.r, t.i);
    LDS32(wo + 24 * 272) = pk(e2048.r, e2048.i);
    const int r = lane & 31;
    v4u yf[8];
#pragma unroll
    for (int m = 0; m < 8; ++m) yf[m] = LDS128(wave * KG_W + (r < 24 ? r : 24) * 272 + 32 * m + 16 * h);
#pragma unroll
    for (int i = 0; i < 32; ++i) {
      LDS32(wo + i * 272) = pk(X.r, -X.i);
      if (i < 31) X = cmul(X, e16);
    }
    v16f acc = {0, 0, 0, 0, 0, 0, 0, 0, 0, 0, 0, 0, 0, 0, 0, 0};
#pragma unroll
    for (int m = 0; m < 8; ++m) acc = mfma32(LDS128(wave * KG_W + r * 272 + 32 * m + 16 * h), yf[m], acc);
#pragma unroll
    for (int q = 0; q < 4; ++q) {
      v4f o = {acc[4 * q], acc[4 * q + 1], acc[4 * q + 2], acc[4 * q + 3]};
      LDS128F(wave * KG_W + 144 * r + 32 * q + 16 * h) = o;
    }
  }
  TS(2);
  VMW(0);
  BAR();
  if (tid == 0) { v2u f = {tagL, tagH}; __builtin_amdgcn_raw_buffer_store_b64(f, rs, OFF_WFL + p * 128, 0, 16); }
  TS(3);
  if (tid < 400) {
    const int ch = tid >= 200, rem = tid - 200 * ch, jj = rem >> 3, iq = rem & 7;
    const int o = ch * 4 * KG_W + 144 * jj + 16 * iq;
    const v4f s = (LDS128F(o) + LDS128F(o + KG_W)) + (LDS128F(o + 2 * KG_W) + LDS128F(o + 3 * KG_W));
    int cj, sh, base;
    if (jj < 16) { cj = jj; sh = iq >= 4; base = sh * 128; }
    else if (jj < 20) { cj = 512 + 4 * (jj - 16); sh = 2; base = 256; }
    else { cj = 1024 + 512 * ((jj - 20) >> 1) + 8 * (jj & 1); sh = 3; base = 384; }
    const int k0 = base + ((64 * iq + cj) >> sh), dk = 16 >> sh;
    const bool okj = jj < 16 ? (iq < 4 || !(jj & 1)) : jj < 24;
    const int ko = L_KLIN + ch * KLIN_CH + 4 * k0;
    if (okj || iq == 0) LDS32F(ko) = s[0];
    if (okj) { LDS32F(ko + 4 * dk) = s[1]; LDS32F(ko + 8 * dk) = s[2]; LDS32F(ko + 12 * dk) = s[3]; }
  } else if (tid < 460) {
    const int i = tid - 400;
    const v4u z0 = {0, 0, 0, 0};
    LDS128(L_Z + (i >= 30) * (Z_CH - 480) + i * 16) = z0;
  }
  BAR();
  float cum[2][4];
  {
    int kap[7];
#pragma unroll
    for (int e = 0; e < 7; ++e) {
      const int l = 4 * tid + e - 3;
      const int kb = (int)(pk((float)l, (float)l) & 0xffffu) - 0x4280;
      kap[e] = l < 128 ? (l < 0 ? 0 : l) : kb;
    }
#pragma unroll
    for (int ch = 0; ch < 2; ++ch) {
      float k[7];
#pragma unroll
      for (int e = 0; e < 7; ++e) k[e] = LDS32F(L_KLIN + ch * KLIN_CH + 4 * kap[e]);
      if (tid == 0) { k[0] = 0.f; k[1] = 0.f; k[2] = 0.f; }
      const u32 d10 = pk(k[1], k[0]), d21 = pk(k[2], k[1]), d32 = pk(k[3], k[2]), d43 = pk(k[4], k[3]), d54 = pk(k[5], k[4]), d65 = pk(k[6], k[5]);
      const int o = L_KC + ch * KC_CH + (511 - tid) * 8;
      v2u c0 = {d32, d10}, c1 = {d43, d21}, c2 = {d54, d32}, c3 = {d65, d43};
      LDS64(o) = c0; LDS64(o + KC_S) = c1; LDS64(o + 2 * KC_S) = c2; LDS64(o + 3 * KC_S) = c3;
      cum[ch][0] = k[3]; cum[ch][1] = k[3] + k[4]; cum[ch][2] = cum[ch][1] + k[5]; cum[ch][3] = cum[ch][2] + k[6];
      const float inc = wave_scan(cum[ch][3]), exc = inc - cum[ch][3];
      if (lane == 63) LDS32F(L_SCAN + ch * 32 + wave * 4) = inc;
#pragma unroll
      for (int e = 0; e < 4; ++e) cum[ch][e] += exc;
    }
    if (tid < 24) { const v2u z0 = {0, 0}; LDS64(L_KC + (tid / 3) * KC_S + 4096 + (tid % 3) * 8) = z0; }
  }
  BAR();
#pragma unroll
  for (int ch = 0; ch < 2; ++ch) {
    const v4f s0 = LDS128F(L_SCAN + ch * 32), s1 = LDS128F(L_SCAN + ch * 32 + 16);
    float o = 0.f;
#pragma unroll
    for (int w = 0; w < 7; ++w) o += w < wave ? (w < 4 ? s0[w] : s1[w - 4]) : 0.f;
    v4f c = {cum[ch][0] + o, cum[ch][1] + o, cum[ch][2] + o, cum[ch][3] + o};
    LDS128F(L_CUM + ch * 8192 + tid * 16) = c;
  }
  TS(4);

  {
    v4u gx0, gx1, gs0, gs1;
    _Pragma("unroll 1") for (u32 sp = 0; sp < SPIN_CAP; ++sp) {
      MEMB();
      gx0 = GLDS(tid * 16, OFF_XT + p * 16384); gx1 = GLDS(tid * 16, OFF_XT + p * 16384 + 8192);
      gs0 = GLDS(tid * 16, OFF_STAT); gs1 = GLDS(tid * 16, OFF_STAT + 8192);
      if (__all(tagok(gx0, tagL, tagH) && tagok(gx1, tagL, tagH) && tagok(gs0, tagL, tagH) && tagok(gs1, tagL, tagH))) break;
      __builtin_amdgcn_s_sleep(4);
    }
#pragma unroll
    for (int q = 0; q < 2; ++q) {
      const v4u gx = q ? gx1 : gx0, gs = q ? gs1 : gs0;
      const float m0 = blo(gs.y), r0 = __builtin_amdgcn_rcpf(bhi(gs.y)), m1 = blo(gs.z), r1 = __builtin_amdgcn_rcpf(bhi(gs.z));
      const int lo = (q * 512 + tid) * 4;
      LDS32(L_Z + 480 + lo) = pk((blo(gx.y) - m0) * r0, (blo(gx.z) - m1) * r1);
      LDS32(L_Z + Z_CH + 480 + lo) = pk((bhi(gx.y) - m0) * r0, (bhi(gx.z) - m1) * r1);
      LDS32(L_XT + lo) = (gx.y & 0xffffu) | (gx.z << 16);
      LDS32(L_XT + 4096 + lo) = (gx.y >> 16) | (gx.z & 0xffff0000u);
    }
  }
  BAR();
  TS(5);
  if (b == 0 && tid == 0) { v2u e = {tagL, tagH}; __builtin_amdgcn_raw_buffer_store_b64(e, rs, 0, 0, 16); }

  v4u w1f[28];
  const int w1o = OFF_W1T + (sl * 8 + wave) * 28 * 1024;
  if (wave < 4) {
    const int ch = wave >> 1, kh = wave & 1, r = lane & 15, g = lane >> 4;
    LAS char* pa = lds + L_KC + ch * KC_CH + (r & 3) * KC_S + 4104 - 8 * (r >> 2) - 16 * g - 256 * kh - 64 * 59;
    LAS char* pbz = lds + L_Z + ch * Z_CH + 496 + 32 * r - 16 * g - 256 * kh - 192;
    auto lda = [&](int a, int rho) { const int o = 64 * (59 - 8 * a - rho); v2u u = *(LAS v2u*)(pa + o), v = *(LAS v2u*)(pa + o + 8); v4u f = {u.x, u.y, v.x, v.y}; return f; };
    auto ldb = [&](int dl, int rho) { return *(LAS v4u*)(pbz + 64 * (8 * dl + 3 - rho)); };
    v4f acc[4];
#pragma unroll
    for (int t = 0; t < 4; ++t) acc[t] = (v4f){0, 0, 0, 0};
#pragma unroll
    for (int rho = 0; rho < 4; ++rho) {
      v4u B[4];
#pragma unroll
      for (int dl = 0; dl < 4; ++dl) B[dl] = ldb(dl, rho);
#pragma unroll
      for (int a = 0; a < 4; ++a) {
        const v4u A = lda(a, rho);
#pragma unroll
        for (int dl = 3 - a; dl >= 0; --dl) acc[a + dl] = mfma16(A, B[dl], acc[a + dl]);
      }
    }
#pragma unroll
    for (int t = 0; t < 4; ++t) LDS128F(L_DUMP0 + (wave * 4 + t) * 1024 + lane * 16) = acc[t];
    BAR();
    TS(6);
    { const int l16 = opq(lane * 16);
#pragma unroll
      for (int i = 0; i < 28; ++i) { w1f[i] = GLDS(l16 + (i & 3) * 1024, w1o + (i >> 2) * 4096); SB(); } }
    SB();
#pragma unroll
    for (int t = 0; t < 4; ++t) acc[t] = (v4f){0, 0, 0, 0};
#pragma unroll
    for (int rho = 0; rho < 4; ++rho) {
      v4u B[8];
#pragma unroll
      for (int dl = 7; dl >= 0; --dl) B[dl] = ldb(dl, rho);
#pragma unroll
      for (int a = 0; a < 8; ++a) {
        const v4u A = lda(a, rho);
#pragma unroll
        for (int dl = 7 - a; dl >= 0 && dl >= 4 - a; --dl) acc[a + dl - 4] = mfma16(A, B[dl], acc[a + dl - 4]);
      }
    }
#pragma unroll
    for (int t = 0; t < 4; ++t) LDS128F(L_DUMP1 + (wave * 4 + t) * 1024 + lane * 16) = acc[t];
    BAR();
  } else {
    const int t4 = tid - 256;
    v4u m0, m1, m2; v2u fl;
    _Pragma("unroll 1") for (u32 sp = 0; sp < SPIN_CAP; ++sp) {
      MEMB();
      m0 = GLD(OFF_MOD + t4 * 16); m1 = GLD(OFF_MOD + (256 + t4) * 16); m2 = GLD(OFF_MOD + (512 + (t4 < 160 ? t4 : 0)) * 16);
      fl = __builtin_amdgcn_raw_buffer_load_b64(rs, OFF_WFL + (t4 < 224 ? t4 : 0) * 128, 0, 16);
      if (__all(tagok(m0, tagL, tagH) && tagok(m1, tagL, tagH) && tagok(m2, tagL, tagH) && fl.x == tagL && fl.y == tagH)) break;
      __builtin_amdgcn_s_sleep(4);
    }
    { v2u u0 = {m0.y, m0.z}, u1 = {m1.y, m1.z}, u2 = {m2.y, m2.z};
      LDS64(L_MOD + t4 * 8) = u0; LDS64(L_MOD + (256 + t4) * 8) = u1;
      if (t4 < 160) LDS64(L_MOD + (512 + t4) * 8) = u2; }
    BAR();
    TS(6);
    const int ch = (wave >> 1) & 1, t0 = (wave & 1) * 2;
    const float dk = ch ? bhi(dk2) : blo(dk2);
    conv_epi(lds, rs, L_DUMP0, ch, t0, t0, 2 * p + ch, dk, lane);
    conv_epi(lds, rs, L_DUMP0, ch, t0 + 1, t0 + 1, 2 * p + ch, dk, lane);
    SB();
    TS(7);
    { const int l16 = opq(lane * 16);
#pragma unroll
      for (int i = 0; i < 28; ++i) { w1f[i] = GLDS(l16 + (i & 3) * 1024, w1o + (i >> 2) * 4096); SB(); } }
    BAR();
  }
  TS(8);
  conv_epi(lds, rs, L_DUMP1, chw, wave & 3, 4 + (wave & 3), 2 * p + chw, chw ? bhi(dk2) : blo(dk2), lane);
  BAR();
  TS(9);

  const char* xg = ws + OFF_X1 + grp * XTILE + wave * 1024;
  auto stage_x = [&](int ldst, const char* src) {
#pragma unroll
    for (int i = 0; i < 7; ++i) {
      int ii = i + sl; ii = ii >= 7 ? ii - 7 : ii;
      dma16_dev(lds0 + ldst + ii * 8192 + wave * 1024, src + ii * 8192, lane * 16);
    }
  };
  auto check_x = [&](int ldst) {
    u32 m = 0;
#pragma unroll
    for (int i = 0; i < 7; ++i) m = max4(m, LDS128(ldst + i * 8192 + tid * 16));
    return !__any(m == 0xffffffffu);
  };
  v4u uv = GLD(OFF_UV + (256 * sl + 32 * wave + (lane & 31)) * 16);
  stage_x(L_XA, xg);
  v4u w2f[28];
  const int w2o = OFF_W2T + (sl * 8 + wave) * 28 * 1024;
  { const int l16 = opq(lane * 16);
#pragma unroll
    for (int i = 0; i < NW2A; ++i) { w2f[i] = GLDS(l16 + (i & 3) * 1024, w2o + (i >> 2) * 4096); SB(); } }
  SB();
  asm volatile("s_waitcnt vmcnt(%0)" :: "i"(NW2A) : "memory");
  asm volatile("" : "+v"(uv));
  _Pragma("unroll 1") for (u32 sp = 0; sp < SPIN_CAP && !check_x(L_XA); ++sp) { __builtin_amdgcn_s_sleep(4); stage_x(L_XA, xg); VMW(0); }
  _Pragma("unroll 1") for (u32 sp = 0; sp < SPIN_CAP && !__all(tagok(uv, tagL, tagH)); ++sp) {
    __builtin_amdgcn_s_sleep(4); MEMB();
    uv = GLD(OFF_UV + (256 * sl + 32 * wave + (lane & 31)) * 16);
  }
  v4u uvf;
  {
    const float u = __uint_as_float(uv.y), v = __uint_as_float(uv.z), uh = rbf(u), vh = rbf(v);
    uvf = (v4u){h ? 0u : pk(uh, u - uh), h ? 0u : pk(vh, v - vh), 0u, 0u};
  }
  BAR();
  TS(10);
  stage_x(L_XB, xg + 32 * XTILE);

  const int trb = (8 * h + trq) * 64 + trc;
  auto gemm1 = [&](int lx, int pass) {
    float s = 0.f, q = 0.f;
#pragma unroll
    for (int i = 0; i < 4; ++i) {
      if (i < 3 || wave < 4) {
        const v4u f = trfrag(lds + lx + XTILE / 2 + trb + (wave + 8 * i) * 1024, 256);
        s = dot2(f.x, 0x3f803f80u, s); q = dot2(f.x, f.x, q); s = dot2(f.y, 0x3f803f80u, s); q = dot2(f.y, f.y, q);
        s = dot2(f.z, 0x3f803f80u, s); q = dot2(f.z, f.z, q); s = dot2(f.w, 0x3f803f80u, s); q = dot2(f.w, f.w, q);
      }
    }
    s = swap32_add(s); q = swap32_add(q);
    if (!h) { v2f o = {s, q}; *(LAS v2f*)(lds + L_ST + pass * ST_P + lane * 80 + wave * 8) = o; }
    LDS64(L_RES + pass * RES_P + (2 * wave + h) * 272 + (lane & 31) * 8) = trd(lds + lx + XTILE / 2 + (64 * sl + 8 * wave + 4 * h + trq) * 64 + trc);
    v16f acc = {0, 0, 0, 0, 0, 0, 0, 0, 0, 0, 0, 0, 0, 0, 0, 0};
#pragma unroll
    for (int k = 0; k < 28; ++k) acc = mfma32(w1f[k], trfrag(lds + lx + trb + k * 1024, 256), acc);
    BAR();
    const v4f a0 = LDS128F(L_ST + pass * ST_P + (lane & 31) * 80 + h * 32), a1 = LDS128F(L_ST + pass * ST_P + (lane & 31) * 80 + h * 32 + 16);
    const float mean = swap32_add((a0[0] + a0[2]) + (a1[0] + a1[2])) * (1.f / D);
    const float ex2 = swap32_add((a0[1] + a0[3]) + (a1[1] + a1[3])) * (1.f / D);
    const float var = rbf(fmaxf(ex2 - mean * mean, 0.f));
    const float sd = rbf(__builtin_amdgcn_sqrtf(rbf(var + 1e-5f)));
    const v4u bs = {h ? 0u : pk(-mean, -mean), h ? 0u : pk(sd, sd), 0u, 0u};
    acc = mfma32(uvf, bs, acc);
    const float rr = __builtin_amdgcn_rcpf(sd), rl = rr * -1.4426950408889634f;
    u32 hp[8];
#pragma unroll
    for (int e = 0; e < 8; ++e) {
      const float x0 = acc[2 * e], x1 = acc[2 * e + 1];
      const float o0 = (x0 * rr) * __builtin_amdgcn_rcpf(1.f + __builtin_amdgcn_exp2f(x0 * rl));
      const float o1 = (x1 * rr) * __builtin_amdgcn_rcpf(1.f + __builtin_amdgcn_exp2f(x1 * rl));
      hp[e] = pk(o0, o1);
    }
    const int ho = OFF_H + (grp + 32 * pass) * HTILE + (16 * sl + 2 * wave) * 1024;
    v4u h0 = {hp[0], hp[1], hp[2], hp[3]}, h1 = {hp[4], hp[5], hp[6], hp[7]};
    const int l16 = opq(lane * 16);
    GSTS(nosent(h0), l16, ho); GSTS(nosent(h1), l16 + 1024, ho);
  };
  gemm1(L_XA, 0);
  TS(11);
  VMW(2);
  _Pragma("unroll 1") for (u32 sp = 0; sp < SPIN_CAP && !check_x(L_XB); ++sp) { __builtin_amdgcn_s_sleep(4); stage_x(L_XB, xg + 32 * XTILE); VMW(0); }
  BAR();
  TS(12);
  gemm1(L_XB, 1);
  TS(13);
  SB();

  const int hoA = OFF_H + grp * HTILE + wave * 14 * 1024, hoB = hoA + 32 * HTILE;
  const v2u gbv = LDS64(L_MOD + (5 * D + 64 * sl + 4 * (tid & 15)) * 2);
  auto put_red = [&](int lr, const v16f& a0, const v16f& a1) {
#pragma unroll
    for (int q = 0; q < 4; ++q) {
      v4f o0 = {a0[4 * q], a0[4 * q + 1], a0[4 * q + 2], a0[4 * q + 3]}, o1 = {a1[4 * q], a1[4 * q + 1], a1[4 * q + 2], a1[4 * q + 3]};
      LDS128F(lr + wave * RED_W + (2 * q + h) * 528 + (lane & 31) * 16) = o0;
      LDS128F(lr + wave * RED_W + (8 + 2 * q + h) * 528 + (lane & 31) * 16) = o1;
    }
  };
  auto finish = [&](int lr, int pass) {
    const int dq = tid & 15, l = tid >> 4;
    v4f s = LDS128F(lr + dq * 528 + l * 16);
#pragma unroll
    for (int w = 1; w < 8; ++w) s += LDS128F(lr + w * RED_W + dq * 528 + l * 16);
    const v2u rv = LDS64(L_RES + pass * RES_P + dq * 272 + l * 8);
    v2u o = {pk(blo(rv.x) + blo(gbv.x) * s[0], bhi(rv.x) + bhi(gbv.x) * s[1]), pk(blo(rv.y) + blo(gbv.y) * s[2], bhi(rv.y) + bhi(gbv.y) * s[3])};
    *(GAS v2u*)(out + (pass * 1024 + grp * 32 + l) * D + 64 * sl + 4 * dq) = o;
  };
  v4u hB[14];
  v16f c0, c1;
  const v16f zero16 = {0, 0, 0, 0, 0, 0, 0, 0, 0, 0, 0, 0, 0, 0, 0, 0};
  auto redo = [&](int ho) {
    _Pragma("unroll 1") for (int i = 0; i < 14; ++i) {
      _Pragma("unroll 1") for (u32 sp = 0; sp < SPIN_CAP; ++sp) {
        MEMB();
        const v4u t = GLDS(lane * 16, ho + i * 1024);
        if (!__any(max4(0, t) == 0xffffffffu)) break;
        __builtin_amdgcn_s_sleep(4);
      }
    }
    MEMB();
    c0 = zero16; c1 = zero16;
#pragma unroll
    for (int i = 0; i < 14; ++i) {
      const v4u t = GLDS(lane * 16, ho + i * 1024);
      c0 = mfma32(w2f[2 * i], t, c0); c1 = mfma32(w2f[2 * i + 1], t, c1);
      SB();
    }
  };
  {
    v4u hA[14];
    const int l16 = opq(lane * 16);
#pragma unroll
    for (int i = 0; i < NW2A / 2; ++i) { hA[i] = GLDS(l16 + (i & 3) * 1024, hoA + (i >> 2) * 4096); SB(); }
#pragma unroll
    for (int i = NW2A / 2; i < 14; ++i) {
      w2f[2 * i] = GLDS(l16 + ((2 * i) & 3) * 1024, w2o + ((2 * i) >> 2) * 4096); SB();
      w2f[2 * i + 1] = GLDS(l16 + ((2 * i + 1) & 3) * 1024, w2o + ((2 * i + 1) >> 2) * 4096); SB();
      hA[i] = GLDS(l16 + (i & 3) * 1024, hoA + (i >> 2) * 4096); SB();
    }
    c0 = zero16; c1 = zero16;
    u32 m = 0;
#pragma unroll
    for (int i = 0; i < 14; ++i) {
      c0 = mfma32(w2f[2 * i], hA[i], c0); c1 = mfma32(w2f[2 * i + 1], hA[i], c1);
      m = max4(m, hA[i]);
      SB();
      hB[i] = GLDS(l16 + (i & 3) * 1024, hoB + (i >> 2) * 4096);
      SB();
    }
    if (__any(m == 0xffffffffu)) redo(hoA);
  }
  TS(14);
  put_red(L_REDA, c0, c1);
  BAR();
  finish(L_REDA, 0);
  TS(15);
  {
    c0 = zero16; c1 = zero16;
    u32 m = 0;
#pragma unroll
    for (int i = 0; i < 14; ++i) {
      c0 = mfma32(w2f[2 * i], hB[i], c0); c1 = mfma32(w2f[2 * i + 1], hB[i], c1);
      m = max4(m, hB[i]);
    }
    if (__any(m == 0xffffffffu)) redo(hoB);
  }
  TS(16);
  put_red(L_REDB, c0, c1);
  BAR();
  finish(L_REDB, 1);
  TS(17);
}

extern "C" void kernel_launch(void* const* d_in, const int* in_sizes, int n_in, void* d_out, int out_size, void* d_ws, size_t ws_size,
                              hipStream_t stream) {
  k_all<<<256, 512, 0, stream>>>((const u16*)d_in[0], (const u16*)d_in[1], (const u16*)d_in[2], (const u16*)d_in[3], (const u16*)d_in[4],
                                 (const u16*)d_in[5], (const u16*)d_in[6], (const u16*)d_in[7], (const u16*)d_in[8], (const u16*)d_in[10],
                                 (const u16*)d_in[11], (const u16*)d_in[12], (const u16*)d_in[13], (u16*)d_out, (char*)d_ws);
}
```

```cpp
#include <hip/hip_runtime.h>
#include <stdint.h>

typedef unsigned short u16;
typedef unsigned u32;
typedef unsigned long long u64;
typedef u32 v4u __attribute__((ext_vector_type(4)));
typedef u32 v2u __attribute__((ext_vector_type(2)));
typedef float v4f __attribute__((ext_vector_type(4)));
typedef float v2f __attribute__((ext_vector_type(2)));
typedef float v16f __attribute__((ext_vector_type(16)));
typedef __bf16 bf8 __attribute__((ext_vector_type(8)));
typedef __bf16 bf2 __attribute__((ext_vector_type(2)));
typedef short s4 __attribute__((ext_vector_type(4)));
#define GAS __attribute__((address_space(1)))
#define LAS __attribute__((address_space(3)))
#define DEV __device__ __forceinline__

constexpr int D = 448, F = 1792;

constexpr int OFF_STAT = 4096;
constexpr int OFF_MOD = 20480;
constexpr int OFF_UV = 32768;
constexpr int OFF_CFL = 61440;
constexpr int OFF_WFL = 65536;
constexpr int OFF_XT = 131072;
constexpr int OFF_W1T = 4 << 20;
constexpr int OFF_W2T = 6 << 20;
constexpr int OFF_X1 = 8 << 20;
constexpr int OFF_H = 12 << 20;
constexpr int OFF_DBG = 24 << 20;
constexpr int WS_BYTES = 32 << 20;
constexpr int XTILE = 57344, XHALF = 28672, HTILE = 114688;

constexpr int KG_W = 8704;
constexpr int L_KC = 0, KC_S = 4160, KC_CH = 4 * KC_S;
constexpr int L_DUMP0 = 33280;
constexpr int L_LNT = 69632;
constexpr int L_STX = 76800;
constexpr int L_KLIN = 76864, KLIN_CH = 2576;
constexpr int L_Z = 82048, Z_CH = 4608;
constexpr int L_XT = 91264;
constexpr int L_DUMP1 = 100480;
constexpr int L_XA = 0, L_XB = XTILE;
constexpr int RED_W = 8448, L_REDA = 0, L_REDB = 8 * RED_W;
constexpr int L_RES = 135168, RES_P = 4352;
constexpr int L_ST = 143872, ST_P = 1536;
constexpr int L_PF = 146944;
constexpr int LDS_BYTES = 147200;
constexpr int L_AR0 = 0, L_AR1 = 81920;
constexpr int L_ASC = 57344;
constexpr int L_ARED = 58368;
constexpr int L_AG = 60416;
constexpr int L_ARED2 = 62464;

constexpr u32 SPIN_CAP = 1u << 14;
constexpr u32 SENTW = 0xffbfffbfu;
#define CODE_PF 30720

DEV float blo(u32 v) { return __uint_as_float(v << 16); }
DEV float bhi(u32 v) { return __uint_as_float(v & 0xffff0000u); }
DEV v2f unpk(u32 v) { v2f r = {blo(v), bhi(v)}; return r; }
DEV u32 pk(float a, float b) { bf2 r = {(__bf16)a, (__bf16)b}; return __builtin_bit_cast(u32, r); }
DEV u32 pk(v2f a) { return pk(a.x, a.y); }
DEV float rbf(float a) { return blo(pk(a, a)); }
DEV u32 umax(u32 a, u32 b) { return a > b ? a : b; }
DEV u32 max4(u32 m, v4u v) { return umax(umax(umax(umax(m, v.x), v.y), v.z), v.w); }
DEV v2f fma2(v2f a, v2f b, v2f c) { return __builtin_elementwise_fma(a, b, c); }
DEV v2f bc(float a) { v2f r = {a, a}; return r; }
DEV v4f mfma16(v4u a, v4u b, v4f c) { return __builtin_amdgcn_mfma_f32_16x16x32_bf16(__builtin_bit_cast(bf8, a), __builtin_bit_cast(bf8, b), c, 0, 0, 0); }
DEV v16f mfma32(v4u a, v4u b, v16f c) { return __builtin_amdgcn_mfma_f32_32x32x16_bf16(__builtin_bit_cast(bf8, a), __builtin_bit_cast(bf8, b), c, 0, 0, 0); }
DEV float dot2(u32 a, u32 b, float c) { return __builtin_amdgcn_fdot2_f32_bf16(__builtin_bit_cast(bf2, a), __builtin_bit_cast(bf2, b), c, false); }
DEV void sumsq(v4u f, float& s, float& q) {
  s = dot2(f.x, 0x3f803f80u, s); q = dot2(f.x, f.x, q); s = dot2(f.y, 0x3f803f80u, s); q = dot2(f.y, f.y, q);
  s = dot2(f.z, 0x3f803f80u, s); q = dot2(f.z, f.z, q); s = dot2(f.w, 0x3f803f80u, s); q = dot2(f.w, f.w, q);
}
DEV v2u trd(LAS char* p) { return __builtin_bit_cast(v2u, __builtin_amdgcn_ds_read_tr16_b64_v4i16((LAS s4*)p)); }
DEV v4u trfrag(LAS char* p) { v2u a = trd(p), b = trd(p + 256); v4u r = {a.x, a.y, b.x, b.y}; return r; }
template <int CTRL, int RM> DEV float dppadd(float v) {
  return v + __builtin_bit_cast(float, __builtin_amdgcn_update_dpp(0, __builtin_bit_cast(int, v), CTRL, RM, 0xf, RM == 0xf));
}
DEV float rdl(float v, int l) { return __builtin_bit_cast(float, __builtin_amdgcn_readlane(__builtin_bit_cast(int, v), l)); }
DEV float swap32_add(float v) { auto r = __builtin_amdgcn_permlane32_swap(__float_as_uint(v), __float_as_uint(v), false, false); return __uint_as_float(r[0]) + __uint_as_float(r[1]); }
DEV float swap16_add(float v) { auto r = __builtin_amdgcn_permlane16_swap(__float_as_uint(v), __float_as_uint(v), false, false); return __uint_as_float(r[0]) + __uint_as_float(r[1]); }
DEV int opq(int v) { asm volatile("" : "+v"(v)); return v; }
DEV v2f cmul(v2f a, v2f w, v2f wn) { return fma2(a.yx, wn, a * w.xx); }
DEV v2f cneg(v2f w) { v2f r = {-w.y, w.y}; return r; }
DEV v2f cexpx(float er, float ei, float x) {
  const float e = __builtin_amdgcn_exp2f(er * x), ph = __builtin_amdgcn_fractf(ei * x);
  v2f c = {__builtin_amdgcn_cosf(ph), __builtin_amdgcn_sinf(ph)};
  return c * bc(e);
}

#define BAR() asm volatile("s_waitcnt lgkmcnt(0)\n\ts_barrier" ::: "memory")
#define VMW(n) asm volatile("s_waitcnt vmcnt(" #n ")" ::: "memory")
#define LGK0() __builtin_amdgcn_s_waitcnt(0xC07F)
#define MEMB() asm volatile("" ::: "memory")
#define SB() __builtin_amdgcn_sched_barrier(0)
#define SPIN _Pragma("unroll 1") for (u32 sp = 0; sp < SPIN_CAP; ++sp)
#define LDS128(off) (*(LAS v4u*)(lds + (off)))
#define LDS128F(off) (*(LAS v4f*)(lds + (off)))
#define LDS64(off) (*(LAS v2u*)(lds + (off)))
#define LDS64F(off) (*(LAS v2f*)(lds + (off)))
#define LDS32(off) (*(LAS u32*)(lds + (off)))
#define LDS32F(off) (*(LAS float*)(lds + (off)))
#define GLD(off) __builtin_amdgcn_raw_buffer_load_b128(rs, (off), 0, 16)
#define GLDS(voff, soff) __builtin_amdgcn_raw_buffer_load_b128(rs, (voff), (soff), 16)
#define GST(v, off) __builtin_amdgcn_raw_buffer_store_b128((v), rs, (off), 0, 16)

DEV void dma16(u32 ldsaddr, const char* gbase, u32 voff) {
  asm volatile("s_mov_b32 m0, %0\n\ts_nop 0\n\tglobal_load_lds_dwordx4 %1, %2" :: "s"(ldsaddr), "v"(voff), "s"(gbase) : "memory");
}
DEV void dma16_dev(u32 ldsaddr, const char* gbase, u32 voff) {
  asm volatile("s_mov_b32 m0, %0\n\ts_nop 0\n\tglobal_load_lds_dwordx4 %1, %2 sc1" :: "s"(ldsaddr), "v"(voff), "s"(gbase) : "memory");
}
template <int OFF> DEV void st_tid(u32 v) { asm volatile("ds_write_addtid_b32 %0 offset:%1" :: "v"(v), "i"(OFF) : "memory"); }

#define TS(i)
#define TSEND()

DEV bool tagok(v4u g, u32 tagL, u32 tagH) { return g.x == tagL && g.w == tagH; }

DEV void ln1_publish(LAS char* lds, __amdgpu_buffer_rsrc_t rs, v4u xr, u32 b, int tid, int lane, u32 wave, u32 tagL, u32 tagH) {
  if (lane < 56) {
    const int o = L_LNT + ((wave >> 1) * 224 + 4 * lane) * 8 + (wave & 1) * 4;
    LDS32(o) = xr.x; LDS32(o + 8) = xr.y; LDS32(o + 16) = xr.z; LDS32(o + 24) = xr.w;
  }
  float s = 0.f, q = 0.f;
  sumsq(xr, s, q);
  s = dppadd<0x111, 0xf>(s); q = dppadd<0x111, 0xf>(q); s = dppadd<0x112, 0xf>(s); q = dppadd<0x112, 0xf>(q);
  s = dppadd<0x114, 0xf>(s); q = dppadd<0x114, 0xf>(q); s = dppadd<0x118, 0xf>(s); q = dppadd<0x118, 0xf>(q);
  s = swap32_add(swap16_add(s)); q = swap32_add(swap16_add(q));
  const float mean = s * (1.f / D), var = rbf(fmaxf(q * (1.f / D) - mean * mean, 0.f));
  const float sd = __builtin_amdgcn_sqrtf(rbf(var + 1e-5f));
  if (lane == 15) LDS32(L_STX + wave * 4) = pk(mean, sd);
  BAR();
  const int lo = L_LNT + ((tid & 3) * 224 + (tid >> 2)) * 8, go = OFF_XT + (tid >> 2) * 16384 + (4 * b + (tid & 3)) * 16;
  {
    v2u g = LDS64(lo);
    v4u o = {tagL, g.x, g.y, tagH};
    GST(o, go);
  }
  if (wave < 6) {
    v2u g = LDS64(lo + 1024);
    v4u o = {tagL, g.x, g.y, tagH};
    GST(o, go + 128 * 16384);
  } else if (wave == 7 && lane < 4) {
    v2u g = LDS64(L_STX + lane * 8);
    v4u o = {tagL, g.x, g.y, tagH};
    GST(o, OFF_STAT + (4 * b + lane) * 16);
  }
}

DEV void conv_epi(LAS char* lds, __amdgpu_buffer_rsrc_t rs, int dump, int lz, int goff, float ga, float gd, float g2) {
  const v4f p0 = LDS128F(dump), p1 = LDS128F(dump + 4096);
  const v2u hh = LDS64(L_Z + 480 + lz), xx = LDS64(L_XT + lz);
  const v2f o0 = fma2(bc(ga), p0.xy + p1.xy, fma2(bc(gd), unpk(hh.x), unpk(xx.x)));
  const v2f o1 = fma2(bc(ga), p0.zw + p1.zw, fma2(bc(gd), unpk(hh.y), unpk(xx.y)));
  const u32 X0 = pk(o0), X1 = pk(o1);
  const u32 S0 = pk(unpk(X0) * bc(g2)), S1 = pk(unpk(X1) * bc(g2));
  auto r0 = __builtin_amdgcn_permlane16_swap(X0, S0, false, false);
  auto r1 = __builtin_amdgcn_permlane16_swap(X1, S1, false, false);
  v4u v = {r0[0], r1[0], r0[1], r1[1]};
  GST(v, goff);
}

__global__ void __launch_bounds__(512, 2) k_all(const u16* __restrict__ x, const u16* __restrict__ Are, const u16* __restrict__ Aim,
    const u16* __restrict__ Bre, const u16* __restrict__ Bim, const u16* __restrict__ Cre, const u16* __restrict__ Cim,
    const u16* __restrict__ logdt, const u16* __restrict__ Dskip, const u16* __restrict__ w1, const u16* __restrict__ w2,
    const u16* __restrict__ cvec, const u16* __restrict__ wmod, u16* __restrict__ out, char* ws) {
  __shared__ __attribute__((aligned(16))) char smem[LDS_BYTES];
  LAS char* lds = (LAS char*)smem;
  const u32 lds0 = (u32)(size_t)lds;
  u64 pc;
  asm volatile("s_getpc_b64 %0" : "=s"(pc));
  asm volatile("" :: "s"(x), "s"(Are), "s"(Aim), "s"(Bre), "s"(Bim), "s"(Cre), "s"(Cim), "s"(logdt), "s"(Dskip), "s"(w1), "s"(w2), "s"(cvec), "s"(wmod), "s"(out), "s"(ws));
  const int tid = threadIdx.x, lane = tid & 63;
  const u32 wave = __builtin_amdgcn_readfirstlane(tid >> 6);
  const u32 b = blockIdx.x, xcd = b & 7, j = b >> 3;
  TS(0);
  const __amdgpu_buffer_rsrc_t rs = __builtin_amdgcn_make_buffer_rsrc(ws, 0, WS_BYTES, 0x00020000);
  const v4u xr = __builtin_amdgcn_raw_buffer_load_b128(__builtin_amdgcn_make_buffer_rsrc((void*)(x + (8 * b + wave) * D), 0, 2 * D, 0x00020000), lane * 16, 0, 0);
  const v2u e0 = __builtin_amdgcn_raw_buffer_load_b64(rs, 0, 0, 0);
  SB();
  const v4u SENT = {SENTW, SENTW, SENTW, SENTW};
  auto gettag = [&](u32& tl, u32& th) {
    const u64 T = (((u64)__builtin_amdgcn_readfirstlane(e0.y) << 32) | __builtin_amdgcn_readfirstlane(e0.x)) + 0x9E3779B97F4A7C15ull;
    tl = (u32)T; th = (u32)(T >> 32);
  };
  u32 tagL, tagH;

  if (j >= 28) {
    const u32 a = (j - 28) * 8 + xcd;
    const int h = lane >> 5, trq = (lane & 15) >> 2, trc = ((lane >> 4) & 1) * 32 + (lane & 3) * 8;
    const u32 wv = wave < 7 ? wave : 0;
    auto stage = [&](u32 ldst, const char* src, u32 rstride) {
      if (wave < 7) {
        const u32 vo = ((lane >> 3) + 64 * wave) * rstride + (lane & 7) * 16;
#pragma unroll
        for (int i = 0; i < 8; ++i) dma16(lds0 + ldst + wave * 8192 + i * 1024, src + i * 8 * rstride, vo);
      }
    };
    auto gemv = [&](u32 lsrc, int atab, float& r0, float& r1) {
      v16f c0 = {0, 0, 0, 0, 0, 0, 0, 0, 0, 0, 0, 0, 0, 0, 0, 0}, c1 = c0;
      LAS char* pb = lds + opq(lsrc + wv * 8192 + (8 * h + trq) * 128 + trc);
#pragma unroll
      for (int k = 0; k < 4; ++k) {
        const v4u af = LDS128(atab + wv * 128 + k * 32 + h * 16);
        const v2u b0 = trd(pb + k * 2048), b1 = trd(pb + k * 2048 + 512), b2 = trd(pb + k * 2048 + 64), b3 = trd(pb + k * 2048 + 576);
        c0 = mfma32(af, (v4u){b0.x, b0.y, b1.x, b1.y}, c0); c1 = mfma32(af, (v4u){b2.x, b2.y, b3.x, b3.y}, c1);
      }
      r0 = h ? c1[0] : c0[0]; r1 = h ? c1[1] : c0[1];
    };
    const u32 cv = cvec[64 * wv + lane];
    u32 lc = a < 21 ? a : (a < 28 ? a + 7 : a - 7);
    stage(L_AR0, (const char*)wmod + lc * 128, 6 * D * 2);
    SB();
    {
      const float cf_ = blo(cv);
      *(LAS u16*)(lds + L_ASC + (64 * wv + lane) * 2) = (u16)pk(cf_ * __builtin_amdgcn_rcpf(1.f + __expf(-cf_)), 0.f);
    }
    gettag(tagL, tagH);
    TS(1);
    ln1_publish(lds, rs, xr, b, tid, lane, wave, tagL, tagH);
    TS(2);
    VMW(1);
    if (a >= 4) stage(L_AR1, (const char*)w1 + (a - 4) * 128, F * 2);
    _Pragma("unroll 1") for (int round = 0;; ++round) {
      float r0, r1;
      gemv(L_AR0, L_ASC, r0, r1);
      LDS32F(L_ARED + wave * 256 + lane * 4) = r0;
      BAR();
      const bool more = round == 0 && a < 10;
      if (more) stage(L_AR0, (const char*)wmod + (a < 3 ? 25 + a : 32 + a) * 128, 6 * D * 2);
      if (tid < 16) {
        v4f s = LDS128F(L_ARED + tid * 16);
#pragma unroll
        for (int w = 1; w < 7; ++w) s += LDS128F(L_ARED + w * 256 + tid * 16);
        v4u o = {tagL, pk(s[0], s[1]), pk(s[2], s[3]), tagH};
        GST(o, OFF_MOD + (16 * lc + tid) * 16);
      }
      if (!more) break;
      lc = a < 3 ? 25 + a : 32 + a;
      VMW(0);
      BAR();
    }
    TS(3);
    const int hc = OFF_H + a * HTILE + tid * 16;
#pragma unroll
    for (int i = 0; i < 8; ++i) GST(SENT, hc + i * 8192);
    SB();
    TS(4);
    if (a >= 4) {
      if (wave < 4) {
        const int gi = 336 + (tid < 224 ? tid : 0);
        v4u g;
        SPIN {
          MEMB();
          g = GLD(OFF_MOD + gi * 16);
          if (__all(tagok(g, tagL, tagH))) break;
          __builtin_amdgcn_s_sleep(8);
        }
        v2u f = {g.y, g.z};
        if (tid >= 112) { f.x = pk(unpk(f.x) + bc(1.f)); f.y = pk(unpk(f.y) + bc(1.f)); }
        if (tid < 224) LDS64(L_AG + tid * 8) = f;
      }
      VMW(0);
      BAR();
      TS(5);
      float r0, r1;
      gemv(L_AR1, L_AG + ((lane & 1) ? 0 : 2 * D), r0, r1);
      { v2f o = {r0, r1}; LDS64F(L_ARED2 + wave * 512 + lane * 8) = o; }
      BAR();
      if (tid < 64) {
        v2f s = LDS64F(L_ARED2 + tid * 8);
#pragma unroll
        for (int w = 1; w < 7; ++w) s += LDS64F(L_ARED2 + w * 512 + tid * 8);
        v4u o = {tagL, __float_as_uint(s[0]), __float_as_uint(s[1]), tagH};
        GST(o, OFF_UV + (64 * (a - 4) + tid) * 16);
      }
    }
    SB();
    TS(6);
#pragma unroll
    for (int i = 8; i < 14; ++i) GST(SENT, hc + i * 8192);
#pragma unroll
    for (int i = 0; i < 14; ++i) GST(SENT, hc + 32 * HTILE + i * 8192);
    VMW(0);
    BAR();
    if (tid == 0) { v4u f = {tagL, 0, 0, tagH}; GST(f, OFF_CFL + a * 128); }
    TS(29); TSEND();
    return;
  }

  const u32 p = xcd * 28 + j;
  const u32 jq = (j * 37) >> 8;
  const u32 grp = xcd * 4 + jq, sl = j - 7 * jq;
  const u32 chw = wave >> 2;

  const u32 pi = ((2 * p + chw) * 256 + (tid & 255)) * 2;
  auto ldp = [&](const u16* q) { return (u32)*(const GAS u16*)((const GAS char*)q + pi); };
  const u32 pAr = ldp(Are), pAi = ldp(Aim), pBr = ldp(Bre), pBi = ldp(Bim), pCr = ldp(Cre), pCi = ldp(Cim);
  const u32 pLd2 = *(const GAS u32*)(logdt + 2 * p), dk2 = *(const GAS u32*)(Dskip + 2 * p);
  SB();
  const bool isw1 = xcd < 4;
  u32 wt_dst = 0;
  if (wave < 7) {
    const char* src; u32 rstride;
    if (isw1) {
      const u32 kap = p >> 2, fg = (p & 3) * 7 + wave;
      src = (const char*)w1 + kap * (16 * F * 2) + fg * 128; rstride = F * 2;
      wt_dst = OFF_W1T + (fg * 56 + kap) * 1024;
    } else {
      const u32 kk = p - 112;
      src = (const char*)w2 + kk * (16 * D * 2) + wave * 128; rstride = D * 2;
      wt_dst = OFF_W2T + (wave * 224 + 2 * kk) * 1024;
    }
    const u32 vo = (lane >> 3) * rstride + (lane & 7) * 16;
    dma16(lds0 + wave * KG_W, src, vo);
    dma16(lds0 + wave * KG_W + 1024, src + 8 * rstride, vo);
  } else if (CODE_PF) {
    u32 vo = (j & 3) * 8192 + lane * 128; vo = vo < CODE_PF ? vo : 0;
    asm volatile("s_mov_b32 m0, %0\n\ts_nop 0\n\tglobal_load_lds_dword %1, %2" :: "s"(lds0 + L_PF), "v"(vo), "s"(pc & ~255ull) : "memory");
  }
  SB();
  {
    const int o = OFF_X1 + (tid >> 3) * XHALF + p * 128 + (tid & 7) * 16;
    GST(SENT, o); GST(SENT, o + 64 * XHALF);
  }
  SB();
  int kap[7];
#pragma unroll
  for (int e = 0; e < 7; ++e) {
    const int l = 4 * tid + e - 3;
    const int kb = (int)(pk((float)l, (float)l) & 0xffffu) - 0x4280;
    kap[e] = opq(L_KLIN + 4 * (l < 128 ? (l < 0 ? 643 : l) : kb));
  }
  int rd_src, rd_dst, rd_dk;
  {
    const int ch = tid >= 200, rem = tid - 200 * ch, jj = rem >> 3, iq = rem & 7;
    int cj, sh, base;
    if (jj < 16) { cj = jj; sh = iq >= 4; base = sh * 128; }
    else if (jj < 20) { cj = 512 + 4 * (jj - 16); sh = 2; base = 256; }
    else { cj = 1024 + 512 * ((jj - 20) >> 1) + 8 * (jj & 1); sh = 3; base = 384; }
    const bool okj = jj < 16 ? (iq < 4 || !(jj & 1)) : jj < 24;
    rd_src = opq(ch * 4 * KG_W + 144 * jj + 16 * iq);
    rd_dst = opq(L_KLIN + ch * KLIN_CH + 4 * ((okj || iq == 0) ? base + ((64 * iq + cj) >> sh) : 641));
    rd_dk = opq(okj ? 64 >> sh : 0);
    if (jj == 24 && iq == 0) rd_dk = -1;
  }
  SB();
  gettag(tagL, tagH);
  ln1_publish(lds, rs, xr, b, tid, lane, wave, tagL, tagH);
  TS(1);

  const int h = lane >> 5, trq = (lane & 15) >> 2, trc = ((lane >> 4) & 1) * 32 + (lane & 3) * 8;
  v4u wt0, wt1;
  if (wave < 7) {
    VMW(3);
    const int r0 = isw1 ? 8 * h + trq : 4 * h + trq, r1 = isw1 ? 512 : 1024;
    LAS char* pb = lds + wave * KG_W + r0 * 128 + trc;
    v2u a0 = trd(pb), a1 = trd(pb + r1), b0 = trd(pb + 64), b1 = trd(pb + 64 + r1);
    wt0 = (v4u){a0.x, a0.y, a1.x, a1.y}; wt1 = (v4u){b0.x, b0.y, b1.x, b1.y};
  }
  MEMB();

  {
    const float dt = rbf(expf(chw ? bhi(pLd2) : blo(pLd2)));
    const float er = (dt * blo(pAr)) * 1.4426950408889634f, ei = (dt * blo(pAi)) * 0.15915494309189535f;
    const v2f dB = {dt * blo(pBr), dt * blo(pBi)}, Cc = {blo(pCr), blo(pCi)};
    v2f X = cmul(Cc, dB, cneg(dB));
    X.y = -X.y;
    const v2f e1 = cexpx(er, ei, 1.f), e16 = cexpx(er, -ei, 16.f), e512 = cexpx(er, ei, 512.f);
    const v2f e1n = cneg(e1), e16n = cneg(e16), e512n = cneg(e512);
    asm volatile("s_mov_b32 m0, %0\n\ts_nop 0" :: "s"(lds0 + wave * KG_W) : "memory");
    st_tid<0>(0x00003f80u);
    v2f Y = e1, Y4, Y8, Y12;
#define YROW(jj) st_tid<jj * 272>(pk(Y)); if (jj == 4) Y4 = Y; if (jj == 8) Y8 = Y; if (jj == 12) Y12 = Y; if (jj < 15) Y = cmul(Y, e1, e1n);
    YROW(1) YROW(2) YROW(3) YROW(4) YROW(5) YROW(6) YROW(7) YROW(8) YROW(9) YROW(10) YROW(11) YROW(12) YROW(13) YROW(14) YROW(15)
    st_tid<16 * 272>(pk(e512));
    st_tid<17 * 272>(pk(cmul(Y4, e512, e512n)));
    Y = cmul(Y8, e512, e512n);  st_tid<18 * 272>(pk(Y));
    st_tid<19 * 272>(pk(cmul(Y12, e512, e512n)));
    v2f E = cmul(e512, e512, e512n); st_tid<20 * 272>(pk(E));
    Y = cmul(Y, e512, e512n);   st_tid<21 * 272>(pk(Y));
    E = cmul(E, e512, e512n);   st_tid<22 * 272>(pk(E));
    Y = cmul(Y, e512, e512n);   st_tid<23 * 272>(pk(Y));
    E = cmul(E, e512, e512n);   st_tid<24 * 272>(pk(E));
    if (wave < 7) {
      GST(wt0, lane * 16 + wt_dst);
      GST(wt1, lane * 16 + wt_dst + (isw1 ? 28 * 1024 : 1024));
    }
    const int r = lane & 31;
    v4u yf[8];
    LAS char* py = lds + wave * KG_W + (r < 24 ? r : 24) * 272 + 16 * h;
    LAS char* px = lds + wave * KG_W + r * 272 + 16 * h;
#pragma unroll
    for (int m = 0; m < 8; ++m) yf[m] = *(LAS v4u*)(py + 32 * m);
#define XROW(i) st_tid<(i) * 272>(pk(X)); if ((i) < 31) X = cmul(X, e16, e16n);
#define XROW4(i) XROW(i) XROW(i + 1) XROW(i + 2) XROW(i + 3)
    XROW4(0) XROW4(4) XROW4(8) XROW4(12) XROW4(16) XROW4(20) XROW4(24) XROW4(28)
    v16f acc = {0, 0, 0, 0, 0, 0, 0, 0, 0, 0, 0, 0, 0, 0, 0, 0};
    v4u xf[8];
#pragma unroll
    for (int m = 0; m < 8; ++m) xf[m] = *(LAS v4u*)(px + 32 * m);
#pragma unroll
    for (int m = 0; m < 8; ++m) acc = mfma32(xf[m], yf[m], acc);
#pragma unroll
    for (int q = 0; q < 4; ++q) {
      v4f o = {acc[4 * q], acc[4 * q + 1], acc[4 * q + 2], acc[4 * q + 3]};
      LDS128F(wave * KG_W + 144 * r + 32 * q + 16 * h) = o;
    }
  }
  TS(2);
  BAR();
  TS(3);
  if (wave == 7) {
    const v4u z0 = {0, 0, 0, 0};
    if (lane < 60) LDS128(L_Z + (lane >= 30) * (Z_CH - 480) + lane * 16) = z0;
    if (lane < 2) LDS32(L_KLIN + lane * KLIN_CH + 4 * 643) = 0;
  } else if (tid < 400) {
    const v4f s = (LDS128F(rd_src) + LDS128F(rd_src + KG_W)) + (LDS128F(rd_src + 2 * KG_W) + LDS128F(rd_src + 3 * KG_W));
    LDS32F(rd_dst) = s[0];
    if (rd_dk >= 0) { LDS32F(rd_dst + rd_dk) = s[1]; LDS32F(rd_dst + 2 * rd_dk) = s[2]; LDS32F(rd_dst + 3 * rd_dk) = s[3]; }
  }
  VMW(0);
  BAR();
  TS(19);
  if (tid == 448) { v4u f = {tagL, 0, 0, tagH}; GST(f, OFF_WFL + p * 128); }
#pragma unroll
  for (int ch = 0; ch < 2; ++ch) {
    float k[7];
#pragma unroll
    for (int e = 0; e < 7; ++e) k[e] = LDS32F(kap[e] + ch * KLIN_CH);
    const u32 d10 = pk(k[1], k[0]), d21 = pk(k[2], k[1]), d32 = pk(k[3], k[2]), d43 = pk(k[4], k[3]), d54 = pk(k[5], k[4]), d65 = pk(k[6], k[5]);
    const int o = L_KC + ch * KC_CH + (511 - tid) * 8;
    v2u c0 = {d32, d10}, c1 = {d43, d21}, c2 = {d54, d32}, c3 = {d65, d43};
    LDS64(o) = c0; LDS64(o + KC_S) = c1; LDS64(o + 2 * KC_S) = c2; LDS64(o + 3 * KC_S) = c3;
  }
  if (tid < 32) { const v2u z0 = {0, 0}; LDS64(L_KC + (tid >> 2) * KC_S + 4096 + (tid & 3) * 8) = z0; }
  TS(4);

  float ga_[2], gd_[2], g2_[2];
  {
    v4u gx0, gx1, gs0, gs1, gm;
    const int l5 = lane & 31;
    const int mo = l5 < 4 ? OFF_MOD + ((l5 == 3 ? 4 : l5) * 112 + (p >> 1)) * 16 : OFF_WFL + (wave * 28 + l5 - 4) * 128;
    SPIN {
      MEMB();
      gx0 = GLDS(tid * 16, OFF_XT + p * 16384); gx1 = GLDS(tid * 16, OFF_XT + p * 16384 + 8192);
      gs0 = GLDS(tid * 16, OFF_STAT); gs1 = GLDS(tid * 16, OFF_STAT + 8192);
      gm = GLD(mo);
      const u32 bad = ((gx0.x ^ tagL) | (gx1.x ^ tagL) | (gs0.x ^ tagL)) | ((gs1.x ^ tagL) | (gm.x ^ tagL) | (gx0.w ^ tagH)) |
                      ((gx1.w ^ tagH) | (gs0.w ^ tagH) | (gs1.w ^ tagH)) | (gm.w ^ tagH);
      if (!__any(bad != 0)) break;
      __builtin_amdgcn_s_sleep(4);
    }
    const int mv = (p & 1) ? gm.z : gm.y;
    const u32 sa2 = __builtin_amdgcn_readlane(mv, 0), ca2 = __builtin_amdgcn_readlane(mv, 1), ga2 = __builtin_amdgcn_readlane(mv, 2), cb2 = __builtin_amdgcn_readlane(mv, 3);
    const v2f sa = unpk(sa2), g1 = unpk(pk(unpk(ca2) + bc(1.f))), g2 = unpk(pk(unpk(cb2) + bc(1.f))), ga = unpk(ga2), gd = ga * unpk(dk2);
    ga_[0] = ga.x; ga_[1] = ga.y; gd_[0] = gd.x; gd_[1] = gd.y; g2_[0] = g2.x; g2_[1] = g2.y;
#pragma unroll
    for (int q = 0; q < 2; ++q) {
      const v4u gx = q ? gx1 : gx0, gs = q ? gs1 : gs0;
      const v2f h0 = fma2((unpk(gx.y) - bc(blo(gs.y))) * bc(__builtin_amdgcn_rcpf(bhi(gs.y))), g1, sa);
      const v2f h1 = fma2((unpk(gx.z) - bc(blo(gs.z))) * bc(__builtin_amdgcn_rcpf(bhi(gs.z))), g1, sa);
      const int lo = (q * 512 + tid) * 4;
      LDS32(L_Z + 480 + lo) = pk(h0.x, h1.x);
      LDS32(L_Z + Z_CH + 480 + lo) = pk(h0.y, h1.y);
      LDS32(L_XT + lo) = __builtin_amdgcn_perm(gx.z, gx.y, 0x05040100u);
      LDS32(L_XT + Z_CH + lo) = __builtin_amdgcn_perm(gx.z, gx.y, 0x07060302u);
    }
  }
  BAR();
  TS(5);

  v4u w1f[28];
  const int w1o = OFF_W1T + (sl * 8 + wave) * 28 * 1024;
  const int c16 = lane & 15, g4 = lane >> 4;
  const int epi_l = 32 * c16 + 8 * g4;
  const int epi_g = OFF_X1 + (c16 >> 1) * XTILE + ((g4 & 1) ? 0 : XHALF) + p * 128 + (c16 & 1) * 32 + (g4 >> 1) * 16;
  auto ldw1 = [&](int i0, int i1) {
    const int l16 = opq(lane * 16);
#pragma unroll
    for (int i = i0; i < i1; ++i) { w1f[i] = GLDS(l16 + (i & 3) * 1024, w1o + (i >> 2) * 4096); SB(); }
  };
  if (wave >= 4) {
    const int ch = (wave >> 1) & 1, kh = wave & 1;
    LAS char* pa = lds + opq(L_KC + ch * KC_CH + (c16 & 3) * KC_S + 4104 - 8 * (c16 >> 2) - 16 * g4 - 256 * kh - 64 * 59);
    LAS char* pbz = lds + opq(L_Z + ch * Z_CH + 496 + 32 * c16 - 16 * g4 - 256 * kh - 192);
    v4u A[2][8], B[2][8];
    auto rd = [&](int s, int rho, int na, int n) {
      if (n >= 3 * na) return;
      const int i = n / 3, m = n % 3;
      if (m == 0) B[s][na - 1 - i] = *(LAS v4u*)(pbz + 64 * (8 * (na - 1 - i) + 3 - rho));
      else { const v2u u = *(LAS v2u*)(pa + 64 * (59 - 8 * i - rho) + 8 * (m - 1)); if (m == 1) { A[s][i].x = u.x; A[s][i].y = u.y; } else { A[s][i].z = u.x; A[s][i].w = u.y; } }
      SB();
    };
    v4f acc[4];
    __builtin_amdgcn_s_setprio(1);
#pragma unroll
    for (int n = 0; n < 12; ++n) rd(0, 0, 4, n);
#pragma unroll
    for (int t = 0; t < 4; ++t) acc[t] = (v4f){0, 0, 0, 0};
#pragma unroll
    for (int rho = 0; rho < 4; ++rho) {
      LGK0(); SB();
      int n = 0;
#pragma unroll
      for (int a = 0; a < 4; ++a) {
#pragma unroll
        for (int dl = 3 - a; dl >= 0; --dl) {
          acc[a + dl] = mfma16(A[rho & 1][a], B[rho & 1][dl], acc[a + dl]);
          if (rho < 3) { rd((rho + 1) & 1, rho + 1, 4, n); rd((rho + 1) & 1, rho + 1, 4, n + 1); }
          else { rd(0, 0, 8, n); rd(0, 0, 8, n + 1); }
          n += 2; SB();
        }
      }
    }
#pragma unroll
    for (int n = 20; n < 24; ++n) rd(0, 0, 8, n);
#pragma unroll
    for (int t = 0; t < 4; ++t) LDS128F(L_DUMP0 + ((wave - 4) * 4 + t) * 1024 + lane * 16) = acc[t];
    TS(20);
    BAR();
    TS(6);
#pragma unroll
    for (int t = 0; t < 4; ++t) acc[t] = (v4f){0, 0, 0, 0};
#pragma unroll
    for (int rho = 0; rho < 4; ++rho) {
      LGK0(); SB();
      int n = 0, cnt = 0;
#pragma unroll
      for (int a = 0; a < 8; ++a) {
#pragma unroll
        for (int dl = 7 - a; dl >= 0 && dl >= 4 - a; --dl) {
          acc[a + dl - 4] = mfma16(A[rho & 1][a], B[rho & 1][dl], acc[a + dl - 4]);
          if (rho < 3) { rd((rho + 1) & 1, rho + 1, 8, n++); if (cnt < 6) rd((rho + 1) & 1, rho + 1, 8, n++); }
          ++cnt; SB();
        }
      }
    }
#pragma unroll
    for (int t = 0; t < 4; ++t) LDS128F(L_DUMP1 + ((wave - 4) * 4 + t) * 1024 + lane * 16) = acc[t];
    __builtin_amdgcn_s_setprio(0);
    TS(21);
    BAR();
  } else {
    if (b == 0 && tid == 0) { v2u e = {tagL, tagH}; __builtin_amdgcn_raw_buffer_store_b64(e, rs, 0, 0, 16); }
    ldw1(0, 8);
    TS(20);
    BAR();
    TS(6);
    const int ch = wave >> 1, t0 = (wave & 1) * 2;
    const float ga = ch ? ga_[1] : ga_[0], gd = ch ? gd_[1] : gd_[0], g2 = ch ? g2_[1] : g2_[0];
    const int dm = L_DUMP0 + (ch * 8 + t0) * 1024 + lane * 16, lz = ch * Z_CH + t0 * 512 + epi_l, go = epi_g + ch * 64 + t0 * 8 * XTILE;
    conv_epi(lds, rs, dm, lz, go, ga, gd, g2);
    conv_epi(lds, rs, dm + 1024, lz + 512, go + 8 * XTILE, ga, gd, g2);
    SB();
    TS(7);
    ldw1(8, 28);
    TS(21);
    BAR();
  }
  TS(8);
  {
    const int t = wave & 3;
    conv_epi(lds, rs, L_DUMP1 + (chw * 8 + t) * 1024 + lane * 16, chw * Z_CH + (4 + t) * 512 + epi_l, epi_g + chw * 64 + (4 + t) * 8 * XTILE,
             chw ? ga_[1] : ga_[0], chw ? gd_[1] : gd_[0], chw ? g2_[1] : g2_[0]);
  }
  SB();
  TS(9);

  const int uvo = h ? OFF_CFL + grp * 128 : OFF_UV + (256 * sl + 32 * wave + lane) * 16;
  const int gbo = OFF_MOD + (560 + 16 * sl + (tid & 15)) * 16;
  v4u uv = GLD(uvo), gbv = GLD(gbo);
  SB();
  const int trb = (8 * h + trq) * 64 + trc;
  const char* xg = ws + OFF_X1 + grp * XTILE + wave * 7168;
  const u32 xl = lane * 16;
  auto dma_x = [&](int pass, int i) {
    const int half = i >= 7, q = i - 7 * half, o = half * XHALF + (q >> 2) * 4096;
    const char* src = xg + pass * 32 * XTILE + o;
    if ((q & 3) == 0) asm volatile("s_mov_b32 m0, %0\n\ts_nop 0" :: "s"(lds0 + pass * XTILE + wave * 7168 + o) : "memory");
    asm volatile("global_load_lds_dwordx4 %0, %1 offset:%2 sc1" :: "v"(xl), "s"(src), "i"((q & 3) * 1024) : "memory");
  };
  auto accept_x = [&](int pass) {
    const int lx = pass * XTILE + wave * 7168;
    float s, q;
    SPIN {
      u32 m = 0;
      v4u t[7], f[7];
#pragma unroll
      for (int i = 0; i < 7; ++i) t[i] = LDS128(lx + i * 1024 + lane * 16);
#pragma unroll
      for (int i = 0; i < 7; ++i) f[i] = trfrag(lds + lx + XHALF + trb + i * 1024);
#pragma unroll
      for (int i = 0; i < 7; ++i) m = max4(m, t[i]);
      s = 0.f; q = 0.f;
#pragma unroll
      for (int i = 0; i < 7; ++i) sumsq(f[i], s, q);
      if (!__any(m == SENTW || q != q)) break;
      MEMB();
#pragma unroll
      for (int i = 0; i < 7; ++i) m = max4(m, LDS128(lx + XHALF + i * 1024 + lane * 16));
      if (!__any(m == SENTW)) break;
      __builtin_amdgcn_s_sleep(4);
#pragma unroll
      for (int i = 0; i < 14; ++i) dma_x(pass, i);
      VMW(0);
    }
    s = swap32_add(s); q = swap32_add(q);
    if (!h) { v2f o = {s, q}; LDS64F(L_ST + pass * ST_P + lane * 48 + wave * 8) = o; }
  };
  if (wave < 4) {
#pragma unroll
    for (int i = 0; i < 14; ++i) dma_x(0, i);
    VMW(0);
    asm volatile("" : "+v"(gbv));
    accept_x(0);
  } else {
    ldw1(0, 14);
  }
  TS(10);
  BAR();
  TS(11);
  if (wave >= 4) ldw1(14, 28);
  SPIN {
    if (__all(tagok(uv, tagL, tagH) && tagok(gbv, tagL, tagH))) break;
    __builtin_amdgcn_s_sleep(4); MEMB();
    uv = GLD(uvo); gbv = GLD(gbo);
  }
  v4u uvf;
  {
    const float u = __uint_as_float(uv.y), v = __uint_as_float(uv.z), uh = rbf(u), vh = rbf(v);
    uvf = (v4u){h ? 0u : pk(uh, u - uh), h ? 0u : pk(vh, v - vh), 0u, 0u};
  }
  const u32 gb0 = gbv.y, gb1 = gbv.z;

  v4u w2f[28];
  const int w2o = OFF_W2T + (sl * 8 + wave) * 28 * 1024;
  const v16f zero16 = {0, 0, 0, 0, 0, 0, 0, 0, 0, 0, 0, 0, 0, 0, 0, 0};
  auto gemm1 = [&](int pass, auto extra) {
    const int lx = pass * XTILE;
    LAS char* pb = lds + lx + trb;
    const v2u res = trd(lds + lx + XHALF + (64 * sl + 8 * wave + 4 * h + trq) * 64 + trc);
    v4u bf[4];
#pragma unroll
    for (int k = 0; k < 3; ++k) bf[k] = trfrag(pb + k * 1024);
    LDS64(L_RES + pass * RES_P + (2 * wave + h) * 272 + (lane & 31) * 8) = res;
    SB();
    v16f acc = zero16;
#pragma unroll
    for (int k = 0; k < 28; ++k) {
      if (k + 3 < 28) bf[(k + 3) & 3] = trfrag(pb + (k + 3) * 1024);
      acc = mfma32(w1f[k], bf[k & 3], acc);
      extra(k);
      SB();
    }
    return acc;
  };
  auto epi1 = [&](int pass, v16f acc) {
    const v4f a0 = LDS128F(L_ST + pass * ST_P + (lane & 31) * 48 + h * 16);
    const float mean = swap32_add(a0[0] + a0[2]) * (1.f / D);
    const float ex2 = swap32_add(a0[1] + a0[3]) * (1.f / D);
    const float var = rbf(fmaxf(ex2 - mean * mean, 0.f));
    const float sd = rbf(__builtin_amdgcn_sqrtf(rbf(var + 1e-5f)));
    const v4u bs = {h ? 0u : pk(-mean, -mean), h ? 0u : pk(sd, sd), 0u, 0u};
    acc = mfma32(uvf, bs, acc);
    const v2f rl = bc(__builtin_amdgcn_rcpf(sd) * -1.4426950408889634f), sd2 = bc(sd);
    u32 hp[8];
#pragma unroll
    for (int e = 0; e < 8; ++e) {
      const v2f a = {acc[2 * e], acc[2 * e + 1]}, t = a * rl;
      const v2f ex = {__builtin_amdgcn_exp2f(t.x), __builtin_amdgcn_exp2f(t.y)}, dn = fma2(ex, sd2, sd2);
      const v2f rc = {__builtin_amdgcn_rcpf(dn.x), __builtin_amdgcn_rcpf(dn.y)};
      hp[e] = pk(a * rc);
    }
    const int ho = opq(OFF_H + (grp + 32 * pass) * HTILE + (16 * sl + 2 * wave) * 1024 + lane * 16);
    v4u h0 = {hp[0], hp[1], hp[2], hp[3]}, h1 = {hp[4], hp[5], hp[6], hp[7]};
    GST(h0, ho); GST(h1, ho + 1024);
    SB();
  };
  const int l16w = opq(lane * 16);
  auto ldw2 = [&](int i) { w2f[i] = GLDS(l16w + (i & 3) * 1024, w2o + (i >> 2) * 4096); };
  v16f acc;
  if (wave < 4) {
    acc = gemm1(0, [&](int k) { if (!(k & 1)) dma_x(1, k >> 1); });
    TS(12);
    epi1(0, acc);
    VMW(2);
    accept_x(1);
    TS(13);
    BAR();
    acc = gemm1(1, [&](int k) { if (!(k & 1)) ldw2(k >> 1); });
  } else {
    acc = gemm1(0, [&](int k) { if (k >= 14 && !(k & 1)) ldw2((k - 14) >> 1); });
    TS(12);
    BAR();
    epi1(0, acc);
    TS(13);
    acc = gemm1(1, [&](int k) { if (!(k & 3)) ldw2(7 + (k >> 2)); });
  }
  TS(14);
  epi1(1, acc);
#pragma unroll
  for (int i = 14; i < 28; ++i) { ldw2(i); SB(); }
  BAR();
  TS(15);

  const int hoA = OFF_H + grp * HTILE + wave * 14 * 1024;
  auto gemm2 = [&](int ho, int lr) {
    v4u hf[14];
    const int l16 = opq(lane * 16);
#pragma unroll
    for (int i = 0; i < 14; ++i) { hf[i] = GLDS(l16 + (i & 3) * 1024, ho + (i >> 2) * 4096); SB(); }
    __builtin_amdgcn_s_setprio(0);
    v16f c0 = zero16, c1 = zero16;
#pragma unroll
    for (int i = 0; i < 14; ++i) { c0 = mfma32(w2f[2 * i], hf[i], c0); c1 = mfma32(w2f[2 * i + 1], hf[i], c1); SB(); }
    if (__any(c0[0] != c0[0])) {
      _Pragma("unroll 1") for (int i = 0; i < 14; ++i) {
        SPIN {
          MEMB();
          const v4u t = GLDS(lane * 16, ho + i * 1024);
          if (!__any(t.x == SENTW || t.y == SENTW || t.z == SENTW || t.w == SENTW)) break;
          __builtin_amdgcn_s_sleep(4);
        }
      }
      MEMB();
      c0 = zero16; c1 = zero16;
#pragma unroll
      for (int i = 0; i < 14; ++i) {
        const v4u t = GLDS(lane * 16, ho + i * 1024);
        c0 = mfma32(w2f[2 * i], t, c0); c1 = mfma32(w2f[2 * i + 1], t, c1);
        SB();
      }
    }
    const int o = lr + wave * RED_W + h * 528 + (lane & 31) * 16;
#pragma unroll
    for (int q = 0; q < 4; ++q) {
      v4f o0 = {c0[4 * q], c0[4 * q + 1], c0[4 * q + 2], c0[4 * q + 3]}, o1 = {c1[4 * q], c1[4 * q + 1], c1[4 * q + 2], c1[4 * q + 3]};
      LDS128F(o + 2 * q * 528) = o0;
      LDS128F(o + (8 + 2 * q) * 528) = o1;
    }
    SB();
  };
  __builtin_amdgcn_s_setprio(1);
  gemm2(hoA, L_REDA);
  TS(16);
  gemm2(hoA + 32 * HTILE, L_REDB);
  TS(17);
  BAR();
  {
    const int dq = tid & 15, l = tid >> 4;
#pragma unroll
    for (int pass = 0; pass < 2; ++pass) {
      const int lr = pass * L_REDB + dq * 528 + l * 16;
      v4f s = LDS128F(lr);
#pragma unroll
      for (int w = 1; w < 8; ++w) s += LDS128F(lr + w * RED_W);
      const v2u rv = LDS64(L_RES + pass * RES_P + dq * 272 + l * 8);
      v2u o = {pk(fma2(unpk(gb0), s.xy, unpk(rv.x))), pk(fma2(unpk(gb1), s.zw, unpk(rv.y)))};
      *(GAS v2u*)(out + (pass * 1024 + grp * 32 + l) * D + 64 * sl + 4 * dq) = o;
    }
  }
  TS(29); TSEND();
}

extern "C" void kernel_launch(void* const* d_in, const int* in_sizes, int n_in, void* d_out, int out_size, void* d_ws, size_t ws_size,
                              hipStream_t stream) {
  k_all<<<256, 512, 0, stream>>>((const u16*)d_in[0], (const u16*)d_in[1], (const u16*)d_in[2], (const u16*)d_in[3], (const u16*)d_in[4],
                                 (const u16*)d_in[5], (const u16*)d_in[6], (const u16*)d_in[7], (const u16*)d_in[8], (const u16*)d_in[10],
                                 (const u16*)d_in[11], (const u16*)d_in[12], (const u16*)d_in[13], (u16*)d_out, (char*)d_ws);
}
```

```cpp
#define PROBE 1
#define PB 16
#include <hip/hip_runtime.h>
#include <stdint.h>

typedef unsigned short u16;
typedef unsigned u32;
typedef unsigned long long u64;
typedef u32 v4u __attribute__((ext_vector_type(4)));
typedef u32 v2u __attribute__((ext_vector_type(2)));
typedef float v4f __attribute__((ext_vector_type(4)));
typedef float v2f __attribute__((ext_vector_type(2)));
typedef float v16f __attribute__((ext_vector_type(16)));
typedef __bf16 bf8 __attribute__((ext_vector_type(8)));
typedef __bf16 bf2 __attribute__((ext_vector_type(2)));
typedef short s4 __attribute__((ext_vector_type(4)));
#define GAS __attribute__((address_space(1)))
#define LAS __attribute__((address_space(3)))
#define DEV __device__ __forceinline__

constexpr int D = 448, F = 1792;

constexpr int OFF_STAT = 4096;
constexpr int OFF_MOD = 20480;
constexpr int OFF_UV = 32768;
constexpr int OFF_CFL = 61440;
constexpr int OFF_WFL = 65536;
constexpr int OFF_XT = 131072;
constexpr int OFF_W1T = 4 << 20;
constexpr int OFF_W2T = 6 << 20;
constexpr int OFF_X1 = 8 << 20;
constexpr int OFF_H = 12 << 20;
constexpr int OFF_DBG = 24 << 20;
constexpr int WS_BYTES = 32 << 20;
constexpr int XTILE = 57344, XHALF = 28672, HTILE = 114688;

constexpr int KG_W = 8704;
constexpr int L_KC = 0, KC_S = 4160, KC_CH = 4 * KC_S;
constexpr int L_DUMP0 = 33280;
constexpr int L_LNT = 69632;
constexpr int L_STX = 76800;
constexpr int L_KLIN = 76864, KLIN_CH = 2576;
constexpr int L_Z = 82048, Z_CH = 4608;
constexpr int L_XT = 91264;
constexpr int L_DUMP1 = 100480;
constexpr int L_XA = 0, L_XB = XTILE;
constexpr int RED_W = 8448, L_REDA = 0, L_REDB = 8 * RED_W;
constexpr int L_RES = 135168, RES_P = 4352;
constexpr int L_ST = 143872, ST_P = 1536;
constexpr int L_PF = 146944;
constexpr int LDS_BYTES = 147200;
constexpr int L_AR0 = 0, L_AR1 = 81920;
constexpr int L_ASC = 57344;
constexpr int L_ARED = 58368;
constexpr int L_AG = 60416;
constexpr int L_ARED2 = 62464;

constexpr u32 SPIN_CAP = 1u << 14;
constexpr u32 SENTW = 0xffbfffbfu;
#define CODE_PF 20480

DEV float blo(u32 v) { return __uint_as_float(v << 16); }
DEV float bhi(u32 v) { return __uint_as_float(v & 0xffff0000u); }
DEV v2f unpk(u32 v) { v2f r = {blo(v), bhi(v)}; return r; }
DEV u32 pk(float a, float b) { bf2 r = {(__bf16)a, (__bf16)b}; return __builtin_bit_cast(u32, r); }
DEV u32 pk(v2f a) { return pk(a.x, a.y); }
DEV float rbf(float a) { return blo(pk(a, a)); }
DEV u32 umax(u32 a, u32 b) { return a > b ? a : b; }
DEV u32 max4(u32 m, v4u v) { return umax(umax(umax(umax(m, v.x), v.y), v.z), v.w); }
DEV v2f fma2(v2f a, v2f b, v2f c) { return __builtin_elementwise_fma(a, b, c); }
DEV v2f bc(float a) { v2f r = {a, a}; return r; }
DEV v4f mfma16(v4u a, v4u b, v4f c) { return __builtin_amdgcn_mfma_f32_16x16x32_bf16(__builtin_bit_cast(bf8, a), __builtin_bit_cast(bf8, b), c, 0, 0, 0); }
DEV v16f mfma32(v4u a, v4u b, v16f c) { return __builtin_amdgcn_mfma_f32_32x32x16_bf16(__builtin_bit_cast(bf8, a), __builtin_bit_cast(bf8, b), c, 0, 0, 0); }
DEV float dot2(u32 a, u32 b, float c) { return __builtin_amdgcn_fdot2_f32_bf16(__builtin_bit_cast(bf2, a), __builtin_bit_cast(bf2, b), c, false); }
DEV void sumsq(v4u f, float& s, float& q) {
  s = dot2(f.x, 0x3f803f80u, s); q = dot2(f.x, f.x, q); s = dot2(f.y, 0x3f803f80u, s); q = dot2(f.y, f.y, q);
  s = dot2(f.z, 0x3f803f80u, s); q = dot2(f.z, f.z, q); s = dot2(f.w, 0x3f803f80u, s); q = dot2(f.w, f.w, q);
}
DEV v2u trd(LAS char* p) { return __builtin_bit_cast(v2u, __builtin_amdgcn_ds_read_tr16_b64_v4i16((LAS s4*)p)); }
DEV v4u trfrag(LAS char* p) { v2u a = trd(p), b = trd(p + 256); v4u r = {a.x, a.y, b.x, b.y}; return r; }
template <int CTRL, int RM> DEV float dppadd(float v) {
  return v + __builtin_bit_cast(float, __builtin_amdgcn_update_dpp(0, __builtin_bit_cast(int, v), CTRL, RM, 0xf, RM == 0xf));
}
DEV float rdl(float v, int l) { return __builtin_bit_cast(float, __builtin_amdgcn_readlane(__builtin_bit_cast(int, v), l)); }
DEV float swap32_add(float v) { auto r = __builtin_amdgcn_permlane32_swap(__float_as_uint(v), __float_as_uint(v), false, false); return __uint_as_float(r[0]) + __uint_as_float(r[1]); }
DEV float swap16_add(float v) { auto r = __builtin_amdgcn_permlane16_swap(__float_as_uint(v), __float_as_uint(v), false, false); return __uint_as_float(r[0]) + __uint_as_float(r[1]); }
DEV int opq(int v) { asm volatile("" : "+v"(v)); return v; }
DEV v2f cmul(v2f a, v2f w, v2f wn) { return fma2(a.yx, wn, a * w.xx); }
DEV v2f cneg(v2f w) { v2f r = {-w.y, w.y}; return r; }
DEV v2f cexpx(float er, float ei, float x) {
  const float e = __builtin_amdgcn_exp2f(er * x), ph = __builtin_amdgcn_fractf(ei * x);
  v2f c = {__builtin_amdgcn_cosf(ph), __builtin_amdgcn_sinf(ph)};
  return c * bc(e);
}

#define BAR() asm volatile("s_waitcnt lgkmcnt(0)\n\ts_barrier" ::: "memory")
#define VMW(n) asm volatile("s_waitcnt vmcnt(" #n ")" ::: "memory")
#define LGK0() __builtin_amdgcn_s_waitcnt(0xC07F)
#define MEMB() asm volatile("" ::: "memory")
#define SB() __builtin_amdgcn_sched_barrier(0)
#define SPIN _Pragma("unroll 1") for (u32 sp = 0; sp < SPIN_CAP; ++sp)
#define LDS128(off) (*(LAS v4u*)(lds + (off)))
#define LDS128F(off) (*(LAS v4f*)(lds + (off)))
#define LDS64(off) (*(LAS v2u*)(lds + (off)))
#define LDS64F(off) (*(LAS v2f*)(lds + (off)))
#define LDS32(off) (*(LAS u32*)(lds + (off)))
#define LDS32F(off) (*(LAS float*)(lds + (off)))
#define GLD(off) __builtin_amdgcn_raw_buffer_load_b128(rs, (off), 0, 16)
#define GLDS(voff, soff) __builtin_amdgcn_raw_buffer_load_b128(rs, (voff), (soff), 16)
#define GST(v, off) __builtin_amdgcn_raw_buffer_store_b128((v), rs, (off), 0, 16)

DEV void dma16(u32 ldsaddr, const char* gbase, u32 voff) {
  asm volatile("s_mov_b32 m0, %0\n\ts_nop 0\n\tglobal_load_lds_dwordx4 %1, %2" :: "s"(ldsaddr), "v"(voff), "s"(gbase) : "memory");
}
DEV void dma16_dev(u32 ldsaddr, const char* gbase, u32 voff) {
  asm volatile("s_mov_b32 m0, %0\n\ts_nop 0\n\tglobal_load_lds_dwordx4 %1, %2 sc1" :: "s"(ldsaddr), "v"(voff), "s"(gbase) : "memory");
}
template <int OFF> DEV void st_tid(u32 v) { asm volatile("ds_write_addtid_b32 %0 offset:%1" :: "v"(v), "i"(OFF) : "memory"); }

#define TS(i) do { if (i == 0) pr0_ = __builtin_amdgcn_s_memrealtime(); if (i == PROBE) pr1_ = __builtin_amdgcn_s_memrealtime(); } while (0)
#define TSEND() do { if (b == PB && wave == 0) { if (PROBE == 99) { const u64 c = __builtin_amdgcn_s_memtime(); while (__builtin_amdgcn_s_memtime() - c < 200000) {} } \
  else { const u64 c = __builtin_amdgcn_s_memrealtime(), dl = 10 * (pr1_ - pr0_); while (__builtin_amdgcn_s_memrealtime() - c < dl) {} } } } while (0)

DEV bool tagok(v4u g, u32 tagL, u32 tagH) { return g.x == tagL && g.w == tagH; }

DEV void ln1_publish(LAS char* lds, __amdgpu_buffer_rsrc_t rs, v4u xr, u32 b, int tid, int lane, u32 wave, u32 tagL, u32 tagH) {
  if (lane < 56) {
    const int o = L_LNT + ((wave >> 1) * 224 + 4 * lane) * 8 + (wave & 1) * 4;
    LDS32(o) = xr.x; LDS32(o + 8) = xr.y; LDS32(o + 16) = xr.z; LDS32(o + 24) = xr.w;
  }
  float s = 0.f, q = 0.f;
  sumsq(xr, s, q);
  s = dppadd<0x111, 0xf>(s); q = dppadd<0x111, 0xf>(q); s = dppadd<0x112, 0xf>(s); q = dppadd<0x112, 0xf>(q);
  s = dppadd<0x114, 0xf>(s); q = dppadd<0x114, 0xf>(q); s = dppadd<0x118, 0xf>(s); q = dppadd<0x118, 0xf>(q);
  s = swap32_add(swap16_add(s)); q = swap32_add(swap16_add(q));
  const float mean = s * (1.f / D), var = rbf(fmaxf(q * (1.f / D) - mean * mean, 0.f));
  const float sd = __builtin_amdgcn_sqrtf(rbf(var + 1e-5f));
  if (lane == 15) LDS32(L_STX + wave * 4) = pk(mean, sd);
  BAR();
  const int lo = L_LNT + ((tid & 3) * 224 + (tid >> 2)) * 8, go = OFF_XT + (tid >> 2) * 16384 + (4 * b + (tid & 3)) * 16;
  {
    v2u g = LDS64(lo);
    v4u o = {tagL, g.x, g.y, tagH};
    GST(o, go);
  }
  if (wave < 6) {
    v2u g = LDS64(lo + 1024);
    v4u o = {tagL, g.x, g.y, tagH};
    GST(o, go + 128 * 16384);
  } else if (wave == 7 && lane < 4) {
    v2u g = LDS64(L_STX + lane * 8);
    v4u o = {tagL, g.x, g.y, tagH};
    GST(o, OFF_STAT + (4 * b + lane) * 16);
  }
}

DEV void conv_epi(LAS char* lds, __amdgpu_buffer_rsrc_t rs, int dump, int lz, int goff, float ga, float gd, float g2) {
  const v4f p0 = LDS128F(dump), p1 = LDS128F(dump + 4096);
  const v2u hh = LDS64(L_Z + 480 + lz), xx = LDS64(L_XT + lz);
  const v2f o0 = fma2(bc(ga), p0.xy + p1.xy, fma2(bc(gd), unpk(hh.x), unpk(xx.x)));
  const v2f o1 = fma2(bc(ga), p0.zw + p1.zw, fma2(bc(gd), unpk(hh.y), unpk(xx.y)));
  const u32 X0 = pk(o0), X1 = pk(o1);
  const u32 S0 = pk(unpk(X0) * bc(g2)), S1 = pk(unpk(X1) * bc(g2));
  auto r0 = __builtin_amdgcn_permlane16_swap(X0, S0, false, false);
  auto r1 = __builtin_amdgcn_permlane16_swap(X1, S1, false, false);
  v4u v = {r0[0], r1[0], r0[1], r1[1]};
  GST(v, goff);
}

__global__ void __launch_bounds__(512, 2) k_all(const u16* __restrict__ x, const u16* __restrict__ Are, const u16* __restrict__ Aim,
    const u16* __restrict__ Bre, const u16* __restrict__ Bim, const u16* __restrict__ Cre, const u16* __restrict__ Cim,
    const u16* __restrict__ logdt, const u16* __restrict__ Dskip, const u16* __restrict__ w1, const u16* __restrict__ w2,
    const u16* __restrict__ cvec, const u16* __restrict__ wmod, u16* __restrict__ out, char* ws) {
  __shared__ __attribute__((aligned(16))) char smem[LDS_BYTES];
  LAS char* lds = (LAS char*)smem;
  const u32 lds0 = (u32)(size_t)lds;
  u64 pc;
  asm volatile("s_getpc_b64 %0" : "=s"(pc));
  asm volatile("" :: "s"(x), "s"(Are), "s"(Aim), "s"(Bre), "s"(Bim), "s"(Cre), "s"(Cim), "s"(logdt), "s"(Dskip), "s"(w1), "s"(w2), "s"(cvec), "s"(wmod), "s"(out), "s"(ws));
  const int tid = threadIdx.x, lane = tid & 63;
  const u32 wave = __builtin_amdgcn_readfirstlane(tid >> 6);
  const u32 b = blockIdx.x, xcd = b & 7, j = b >> 3;
  u64 pr0_ = 0, pr1_ = 0;
  TS(0);
  const __amdgpu_buffer_rsrc_t rs = __builtin_amdgcn_make_buffer_rsrc(ws, 0, WS_BYTES, 0x00020000);
  const v4u xr = __builtin_amdgcn_raw_buffer_load_b128(__builtin_amdgcn_make_buffer_rsrc((void*)(x + (8 * b + wave) * D), 0, 2 * D, 0x00020000), lane * 16, 0, 0);
  const v2u e0 = __builtin_amdgcn_raw_buffer_load_b64(rs, 0, 0, 0);
  SB();
  const v4u SENT = {SENTW, SENTW, SENTW, SENTW};
  auto gettag = [&](u32& tl, u32& th) {
    const u64 T = (((u64)__builtin_amdgcn_readfirstlane(e0.y) << 32) | __builtin_amdgcn_readfirstlane(e0.x)) + 0x9E3779B97F4A7C15ull;
    tl = (u32)T; th = (u32)(T >> 32);
  };
  u32 tagL, tagH;

  if (j >= 28) {
    const u32 a = (j - 28) * 8 + xcd;
    const int h = lane >> 5, trq = (lane & 15) >> 2, trc = ((lane >> 4) & 1) * 32 + (lane & 3) * 8;
    const u32 wv = wave < 7 ? wave : 0;
    auto stage = [&](u32 ldst, const char* src, u32 rstride) {
      if (wave < 7) {
        const u32 vo = ((lane >> 3) + 64 * wave) * rstride + (lane & 7) * 16;
#pragma unroll
        for (int i = 0; i < 8; ++i) dma16(lds0 + ldst + wave * 8192 + i * 1024, src + i * 8 * rstride, vo);
      }
    };
    auto gemv = [&](u32 lsrc, int atab, float& r0, float& r1) {
      v16f c0 = {0, 0, 0, 0, 0, 0, 0, 0, 0, 0, 0, 0, 0, 0, 0, 0}, c1 = c0;
      LAS char* pb = lds + opq(lsrc + wv * 8192 + (8 * h + trq) * 128 + trc);
#pragma unroll
      for (int k = 0; k < 4; ++k) {
        const v4u af = LDS128(atab + wv * 128 + k * 32 + h * 16);
        const v2u b0 = trd(pb + k * 2048), b1 = trd(pb + k * 2048 + 512), b2 = trd(pb + k * 2048 + 64), b3 = trd(pb + k * 2048 + 576);
        c0 = mfma32(af, (v4u){b0.x, b0.y, b1.x, b1.y}, c0); c1 = mfma32(af, (v4u){b2.x, b2.y, b3.x, b3.y}, c1);
      }
      r0 = h ? c1[0] : c0[0]; r1 = h ? c1[1] : c0[1];
    };
    const u32 cv = cvec[64 * wv + lane];
    u32 lc = a < 21 ? a : (a < 28 ? a + 7 : a - 7);
    stage(L_AR0, (const char*)wmod + lc * 128, 6 * D * 2);
    SB();
    {
      const float cf_ = blo(cv);
      *(LAS u16*)(lds + L_ASC + (64 * wv + lane) * 2) = (u16)pk(cf_ * __builtin_amdgcn_rcpf(1.f + __expf(-cf_)), 0.f);
    }
    gettag(tagL, tagH);
    TS(1);
    ln1_publish(lds, rs, xr, b, tid, lane, wave, tagL, tagH);
    TS(2);
    VMW(1);
    if (a >= 4) stage(L_AR1, (const char*)w1 + (a - 4) * 128, F * 2);
    _Pragma("unroll 1") for (int round = 0;; ++round) {
      float r0, r1;
      gemv(L_AR0, L_ASC, r0, r1);
      LDS32F(L_ARED + wave * 256 + lane * 4) = r0;
      BAR();
      const bool more = round == 0 && a < 10;
      if (more) stage(L_AR0, (const char*)wmod + (a < 3 ? 25 + a : 32 + a) * 128, 6 * D * 2);
      if (tid < 16) {
        v4f s = LDS128F(L_ARED + tid * 16);
#pragma unroll
        for (int w = 1; w < 7; ++w) s += LDS128F(L_ARED + w * 256 + tid * 16);
        v4u o = {tagL, pk(s[0], s[1]), pk(s[2], s[3]), tagH};
        GST(o, OFF_MOD + (16 * lc + tid) * 16);
      }
      if (!more) break;
      lc = a < 3 ? 25 + a : 32 + a;
      VMW(0);
      BAR();
    }
    TS(3);
    const int hc = OFF_H + a * HTILE + tid * 16;
#pragma unroll
    for (int i = 0; i < 8; ++i) GST(SENT, hc + i * 8192);
    SB();
    TS(4);
    if (a >= 4) {
      if (wave < 4) {
        const int gi = 336 + (tid < 224 ? tid : 0);
        v4u g;
        SPIN {
          MEMB();
          g = GLD(OFF_MOD + gi * 16);
          if (__all(tagok(g, tagL, tagH))) break;
          __builtin_amdgcn_s_sleep(8);
        }
        v2u f = {g.y, g.z};
        if (tid >= 112) { f.x = pk(unpk(f.x) + bc(1.f)); f.y = pk(unpk(f.y) + bc(1.f)); }
        if (tid < 224) LDS64(L_AG + tid * 8) = f;
      }
      VMW(0);
      BAR();
      TS(5);
      float r0, r1;
      gemv(L_AR1, L_AG + ((lane & 1) ? 0 : 2 * D), r0, r1);
      { v2f o = {r0, r1}; LDS64F(L_ARED2 + wave * 512 + lane * 8) = o; }
      BAR();
      if (tid < 64) {
        v2f s = LDS64F(L_ARED2 + tid * 8);
#pragma unroll
        for (int w = 1; w < 7; ++w) s += LDS64F(L_ARED2 + w * 512 + tid * 8);
        v4u o = {tagL, __float_as_uint(s[0]), __float_as_uint(s[1]), tagH};
        GST(o, OFF_UV + (64 * (a - 4) + tid) * 16);
      }
    }
    SB();
    TS(6);
#pragma unroll
    for (int i = 8; i < 14; ++i) GST(SENT, hc + i * 8192);
#pragma unroll
    for (int i = 0; i < 14; ++i) GST(SENT, hc + 32 * HTILE + i * 8192);
    VMW(0);
    BAR();
    if (tid == 0) { v4u f = {tagL, 0, 0, tagH}; GST(f, OFF_CFL + a * 128); }
    TS(29); TSEND();
    return;
  }

  const u32 p = xcd * 28 + j;
  const u32 jq = (j * 37) >> 8;
  const u32 grp = xcd * 4 + jq, sl = j - 7 * jq;
  const u32 chw = wave >> 2;

  const u32 pi = ((2 * p + chw) * 256 + (tid & 255)) * 2;
  auto ldp = [&](const u16* q) { return (u32)*(const GAS u16*)((const GAS char*)q + pi); };
  const u32 pAr = ldp(Are), pAi = ldp(Aim), pBr = ldp(Bre), pBi = ldp(Bim), pCr = ldp(Cre), pCi = ldp(Cim);
  const u32 pLd2 = *(const GAS u32*)(logdt + 2 * p), dk2 = *(const GAS u32*)(Dskip + 2 * p);
  SB();
  const bool isw1 = xcd < 4;
  u32 wt_dst = 0;
  if (wave < 7) {
    const char* src; u32 rstride;
    if (isw1) {
      const u32 kap = p >> 2, fg = (p & 3) * 7 + wave;
      src = (const char*)w1 + kap * (16 * F * 2) + fg * 128; rstride = F * 2;
      wt_dst = OFF_W1T + (fg * 56 + kap) * 1024;
    } else {
      const u32 kk = p - 112;
      src = (const char*)w2 + kk * (16 * D * 2) + wave * 128; rstride = D * 2;
      wt_dst = OFF_W2T + (wave * 224 + 2 * kk) * 1024;
    }
    const u32 vo = (lane >> 3) * rstride + (lane & 7) * 16;
    dma16(lds0 + wave * KG_W, src, vo);
    dma16(lds0 + wave * KG_W + 1024, src + 8 * rstride, vo);
  } else if (CODE_PF) {
    u32 vo = (j & 3) * 8192 + lane * 128; vo = vo < CODE_PF ? vo : 0;
    asm volatile("s_mov_b32 m0, %0\n\ts_nop 0\n\tglobal_load_lds_dword %1, %2" :: "s"(lds0 + L_PF), "v"(vo), "s"(pc & ~255ull) : "memory");
  }
  SB();
  {
    const int o = OFF_X1 + (tid >> 3) * XHALF + p * 128 + (tid & 7) * 16;
    GST(SENT, o); GST(SENT, o + 64 * XHALF);
  }
  SB();
  int kap[7];
#pragma unroll
  for (int e = 0; e < 7; ++e) {
    const int l = 4 * tid + e - 3;
    const int kb = (int)(pk((float)l, (float)l) & 0xffffu) - 0x4280;
    kap[e] = opq(L_KLIN + 4 * (l < 128 ? (l < 0 ? 643 : l) : kb));
  }
  int rd_src, rd_dst, rd_dk;
  {
    const int ch = tid >= 200, rem = tid - 200 * ch, jj = rem >> 3, iq = rem & 7;
    int cj, sh, base;
    if (jj < 16) { cj = jj; sh = iq >= 4; base = sh * 128; }
    else if (jj < 20) { cj = 512 + 4 * (jj - 16); sh = 2; base = 256; }
    else { cj = 1024 + 512 * ((jj - 20) >> 1) + 8 * (jj & 1); sh = 3; base = 384; }
    const bool okj = jj < 16 ? (iq < 4 || !(jj & 1)) : jj < 24;
    rd_src = opq(ch * 4 * KG_W + 144 * jj + 16 * iq);
    rd_dst = opq(L_KLIN + ch * KLIN_CH + 4 * ((okj || iq == 0) ? base + ((64 * iq + cj) >> sh) : 641));
    rd_dk = opq(okj ? 64 >> sh : 0);
    if (jj == 24 && iq == 0) rd_dk = -1;
  }
  SB();
  gettag(tagL, tagH);
  ln1_publish(lds, rs, xr, b, tid, lane, wave, tagL, tagH);
  TS(1);

  const int h = lane >> 5, trq = (lane & 15) >> 2, trc = ((lane >> 4) & 1) * 32 + (lane & 3) * 8;
  v4u wt0, wt1;
  if (wave < 7) {
    VMW(3);
    const int r0 = isw1 ? 8 * h + trq : 4 * h + trq, r1 = isw1 ? 512 : 1024;
    LAS char* pb = lds + wave * KG_W + r0 * 128 + trc;
    v2u a0 = trd(pb), a1 = trd(pb + r1), b0 = trd(pb + 64), b1 = trd(pb + 64 + r1);
    wt0 = (v4u){a0.x, a0.y, a1.x, a1.y}; wt1 = (v4u){b0.x, b0.y, b1.x, b1.y};
  }
  MEMB();

  {
    const float dt = rbf(expf(chw ? bhi(pLd2) : blo(pLd2)));
    const float er = (dt * blo(pAr)) * 1.4426950408889634f, ei = (dt * blo(pAi)) * 0.15915494309189535f;
    const v2f dB = {dt * blo(pBr), dt * blo(pBi)}, Cc = {blo(pCr), blo(pCi)};
    v2f X = cmul(Cc, dB, cneg(dB));
    X.y = -X.y;
    const v2f e1 = cexpx(er, ei, 1.f), e16 = cexpx(er, -ei, 16.f), e512 = cexpx(er, ei, 512.f);
    const v2f e1n = cneg(e1), e16n = cneg(e16), e512n = cneg(e512);
    asm volatile("s_mov_b32 m0, %0\n\ts_nop 0" :: "s"(lds0 + wave * KG_W) : "memory");
    st_tid<0>(0x00003f80u);
    v2f Y = e1, Y4, Y8, Y12;
#define YROW(jj) st_tid<jj * 272>(pk(Y)); if (jj == 4) Y4 = Y; if (jj == 8) Y8 = Y; if (jj == 12) Y12 = Y; if (jj < 15) Y = cmul(Y, e1, e1n);
    YROW(1) YROW(2) YROW(3) YROW(4) YROW(5) YROW(6) YROW(7) YROW(8) YROW(9) YROW(10) YROW(11) YROW(12) YROW(13) YROW(14) YROW(15)
    st_tid<16 * 272>(pk(e512));
    st_tid<17 * 272>(pk(cmul(Y4, e512, e512n)));
    Y = cmul(Y8, e512, e512n);  st_tid<18 * 272>(pk(Y));
    st_tid<19 * 272>(pk(cmul(Y12, e512, e512n)));
    v2f E = cmul(e512, e512, e512n); st_tid<20 * 272>(pk(E));
    Y = cmul(Y, e512, e512n);   st_tid<21 * 272>(pk(Y));
    E = cmul(E, e512, e512n);   st_tid<22 * 272>(pk(E));
    Y = cmul(Y, e512, e512n);   st_tid<23 * 272>(pk(Y));
    E = cmul(E, e512, e512n);   st_tid<24 * 272>(pk(E));
    if (wave < 7) {
      GST(wt0, lane * 16 + wt_dst);
      GST(wt1, lane * 16 + wt_dst + (isw1 ? 28 * 1024 : 1024));
    }
    const int r = lane & 31;
    v4u yf[8];
    LAS char* py = lds + wave * KG_W + (r < 24 ? r : 24) * 272 + 16 * h;
    LAS char* px = lds + wave * KG_W + r * 272 + 16 * h;
#pragma unroll
    for (int m = 0; m < 8; ++m) yf[m] = *(LAS v4u*)(py + 32 * m);
#define XROW(i) st_tid<(i) * 272>(pk(X)); if ((i) < 31) X = cmul(X, e16, e16n);
#define XROW4(i) XROW(i) XROW(i + 1) XROW(i + 2) XROW(i + 3)
    XROW4(0) XROW4(4) XROW4(8) XROW4(12) XROW4(16) XROW4(20) XROW4(24) XROW4(28)
    v16f acc = {0, 0, 0, 0, 0, 0, 0, 0, 0, 0, 0, 0, 0, 0, 0, 0};
    v4u xf[8];
#pragma unroll
    for (int m = 0; m < 8; ++m) xf[m] = *(LAS v4u*)(px + 32 * m);
#pragma unroll
    for (int m = 0; m < 8; ++m) acc = mfma32(xf[m], yf[m], acc);
#pragma unroll
    for (int q = 0; q < 4; ++q) {
      v4f o = {acc[4 * q], acc[4 * q + 1], acc[4 * q + 2], acc[4 * q + 3]};
      LDS128F(wave * KG_W + 144 * r + 32 * q + 16 * h) = o;
    }
  }
  TS(2);
  BAR();
  TS(3);
  if (wave == 7) {
    const v4u z0 = {0, 0, 0, 0};
    if (lane < 60) LDS128(L_Z + (lane >= 30) * (Z_CH - 480) + lane * 16) = z0;
    if (lane < 2) LDS32(L_KLIN + lane * KLIN_CH + 4 * 643) = 0;
  } else if (tid < 400) {
    const v4f s = (LDS128F(rd_src) + LDS128F(rd_src + KG_W)) + (LDS128F(rd_src + 2 * KG_W) + LDS128F(rd_src + 3 * KG_W));
    LDS32F(rd_dst) = s[0];
    if (rd_dk >= 0) { LDS32F(rd_dst + rd_dk) = s[1]; LDS32F(rd_dst + 2 * rd_dk) = s[2]; LDS32F(rd_dst + 3 * rd_dk) = s[3]; }
  }
  VMW(0);
  BAR();
  TS(19);
  if (tid == 448) { v4u f = {tagL, 0, 0, tagH}; GST(f, OFF_WFL + p * 128); }
#pragma unroll
  for (int ch = 0; ch < 2; ++ch) {
    float k[7];
#pragma unroll
    for (int e = 0; e < 7; ++e) k[e] = LDS32F(kap[e] + ch * KLIN_CH);
    const u32 d10 = pk(k[1], k[0]), d21 = pk(k[2], k[1]), d32 = pk(k[3], k[2]), d43 = pk(k[4], k[3]), d54 = pk(k[5], k[4]), d65 = pk(k[6], k[5]);
    const int o = L_KC + ch * KC_CH + (511 - tid) * 8;
    v2u c0 = {d32, d10}, c1 = {d43, d21}, c2 = {d54, d32}, c3 = {d65, d43};
    LDS64(o) = c0; LDS64(o + KC_S) = c1; LDS64(o + 2 * KC_S) = c2; LDS64(o + 3 * KC_S) = c3;
  }
  if (tid < 32) { const v2u z0 = {0, 0}; LDS64(L_KC + (tid >> 2) * KC_S + 4096 + (tid & 3) * 8) = z0; }
  TS(4);

  float ga_[2], gd_[2], g2_[2];
  {
    v4u gx0, gx1, gs0, gs1, gm;
    const int l5 = lane & 31;
    const int mo = l5 < 4 ? OFF_MOD + ((l5 == 3 ? 4 : l5) * 112 + (p >> 1)) * 16 : OFF_WFL + (wave * 28 + l5 - 4) * 128;
    SPIN {
      MEMB();
      gx0 = GLDS(tid * 16, OFF_XT + p * 16384); gx1 = GLDS(tid * 16, OFF_XT + p * 16384 + 8192);
      gs0 = GLDS(tid * 16, OFF_STAT); gs1 = GLDS(tid * 16, OFF_STAT + 8192);
      gm = GLD(mo);
      const u32 bad = ((gx0.x ^ tagL) | (gx1.x ^ tagL) | (gs0.x ^ tagL)) | ((gs1.x ^ tagL) | (gm.x ^ tagL) | (gx0.w ^ tagH)) |
                      ((gx1.w ^ tagH) | (gs0.w ^ tagH) | (gs1.w ^ tagH)) | (gm.w ^ tagH);
      if (!__any(bad != 0)) break;
      __builtin_amdgcn_s_sleep(4);
    }
    const int mv = (p & 1) ? gm.z : gm.y;
    const u32 sa2 = __builtin_amdgcn_readlane(mv, 0), ca2 = __builtin_amdgcn_readlane(mv, 1), ga2 = __builtin_amdgcn_readlane(mv, 2), cb2 = __builtin_amdgcn_readlane(mv, 3);
    const v2f sa = unpk(sa2), g1 = unpk(pk(unpk(ca2) + bc(1.f))), g2 = unpk(pk(unpk(cb2) + bc(1.f))), ga = unpk(ga2), gd = ga * unpk(dk2);
    ga_[0] = ga.x; ga_[1] = ga.y; gd_[0] = gd.x; gd_[1] = gd.y; g2_[0] = g2.x; g2_[1] = g2.y;
#pragma unroll
    for (int q = 0; q < 2; ++q) {
      const v4u gx = q ? gx1 : gx0, gs = q ? gs1 : gs0;
      const v2f h0 = fma2((unpk(gx.y) - bc(blo(gs.y))) * bc(__builtin_amdgcn_rcpf(bhi(gs.y))), g1, sa);
      const v2f h1 = fma2((unpk(gx.z) - bc(blo(gs.z))) * bc(__builtin_amdgcn_rcpf(bhi(gs.z))), g1, sa);
      const int lo = (q * 512 + tid) * 4;
      LDS32(L_Z + 480 + lo) = pk(h0.x, h1.x);
      LDS32(L_Z + Z_CH + 480 + lo) = pk(h0.y, h1.y);
      LDS32(L_XT + lo) = __builtin_amdgcn_perm(gx.z, gx.y, 0x05040100u);
      LDS32(L_XT + Z_CH + lo) = __builtin_amdgcn_perm(gx.z, gx.y, 0x07060302u);
    }
  }
  BAR();
  TS(5);

  v4u w1f[28];
  const int w1o = OFF_W1T + (sl * 8 + wave) * 28 * 1024;
  const int c16 = lane & 15, g4 = lane >> 4;
  const int epi_l = 32 * c16 + 8 * g4;
  const int epi_g = OFF_X1 + (c16 >> 1) * XTILE + ((g4 & 1) ? 0 : XHALF) + p * 128 + (c16 & 1) * 32 + (g4 >> 1) * 16;
  auto ldw1 = [&](int i0, int i1) {
    const int l16 = opq(lane * 16);
#pragma unroll
    for (int i = i0; i < i1; ++i) { w1f[i] = GLDS(l16 + (i & 3) * 1024, w1o + (i >> 2) * 4096); SB(); }
  };
  if (wave >= 4) {
    const int ch = (wave >> 1) & 1, kh = wave & 1;
    LAS char* pa = lds + opq(L_KC + ch * KC_CH + (c16 & 3) * KC_S + 4104 - 8 * (c16 >> 2) - 16 * g4 - 256 * kh - 64 * 59);
    LAS char* pbz = lds + opq(L_Z + ch * Z_CH + 496 + 32 * c16 - 16 * g4 - 256 * kh - 192);
    v4u A[2][8], B[2][8];
    auto rd = [&](int s, int rho, int na, int n) {
      if (n >= 3 * na) return;
      const int i = n / 3, m = n % 3;
      if (m == 0) B[s][na - 1 - i] = *(LAS v4u*)(pbz + 64 * (8 * (na - 1 - i) + 3 - rho));
      else { const v2u u = *(LAS v2u*)(pa + 64 * (59 - 8 * i - rho) + 8 * (m - 1)); if (m == 1) { A[s][i].x = u.x; A[s][i].y = u.y; } else { A[s][i].z = u.x; A[s][i].w = u.y; } }
      SB();
    };
    v4f acc[4];
    __builtin_amdgcn_s_setprio(1);
#pragma unroll
    for (int n = 0; n < 12; ++n) rd(0, 0, 4, n);
#pragma unroll
    for (int t = 0; t < 4; ++t) acc[t] = (v4f){0, 0, 0, 0};
#pragma unroll
    for (int rho = 0; rho < 4; ++rho) {
      LGK0(); SB();
      int n = 0;
#pragma unroll
      for (int a = 0; a < 4; ++a) {
#pragma unroll
        for (int dl = 3 - a; dl >= 0; --dl) {
          acc[a + dl] = mfma16(A[rho & 1][a], B[rho & 1][dl], acc[a + dl]);
          if (rho < 3) { rd((rho + 1) & 1, rho + 1, 4, n); rd((rho + 1) & 1, rho + 1, 4, n + 1); }
          else { rd(0, 0, 8, n); rd(0, 0, 8, n + 1); }
          n += 2; SB();
        }
      }
    }
#pragma unroll
    for (int n = 20; n < 24; ++n) rd(0, 0, 8, n);
#pragma unroll
    for (int t = 0; t < 4; ++t) LDS128F(L_DUMP0 + ((wave - 4) * 4 + t) * 1024 + lane * 16) = acc[t];
    TS(20);
    BAR();
    TS(6);
#pragma unroll
    for (int t = 0; t < 4; ++t) acc[t] = (v4f){0, 0, 0, 0};
#pragma unroll
    for (int rho = 0; rho < 4; ++rho) {
      LGK0(); SB();
      int n = 0, cnt = 0;
#pragma unroll
      for (int a = 0; a < 8; ++a) {
#pragma unroll
        for (int dl = 7 - a; dl >= 0 && dl >= 4 - a; --dl) {
          acc[a + dl - 4] = mfma16(A[rho & 1][a], B[rho & 1][dl], acc[a + dl - 4]);
          if (rho < 3) { rd((rho + 1) & 1, rho + 1, 8, n++); if (cnt < 6) rd((rho + 1) & 1, rho + 1, 8, n++); }
          ++cnt; SB();
        }
      }
    }
#pragma unroll
    for (int t = 0; t < 4; ++t) LDS128F(L_DUMP1 + ((wave - 4) * 4 + t) * 1024 + lane * 16) = acc[t];
    __builtin_amdgcn_s_setprio(0);
    TS(21);
    BAR();
  } else {
    if (b == 0 && tid == 0) { v2u e = {tagL, tagH}; __builtin_amdgcn_raw_buffer_store_b64(e, rs, 0, 0, 16); }
    ldw1(0, 8);
    TS(20);
    BAR();
    TS(6);
    const int ch = wave >> 1, t0 = (wave & 1) * 2;
    const float ga = ch ? ga_[1] : ga_[0], gd = ch ? gd_[1] : gd_[0], g2 = ch ? g2_[1] : g2_[0];
    const int dm = L_DUMP0 + (ch * 8 + t0) * 1024 + lane * 16, lz = ch * Z_CH + t0 * 512 + epi_l, go = epi_g + ch * 64 + t0 * 8 * XTILE;
    conv_epi(lds, rs, dm, lz, go, ga, gd, g2);
    conv_epi(lds, rs, dm + 1024, lz + 512, go + 8 * XTILE, ga, gd, g2);
    SB();
    TS(7);
    ldw1(8, 28);
    TS(21);
    BAR();
  }
  TS(8);
  {
    const int t = wave & 3;
    conv_epi(lds, rs, L_DUMP1 + (chw * 8 + t) * 1024 + lane * 16, chw * Z_CH + (4 + t) * 512 + epi_l, epi_g + chw * 64 + (4 + t) * 8 * XTILE,
             chw ? ga_[1] : ga_[0], chw ? gd_[1] : gd_[0], chw ? g2_[1] : g2_[0]);
  }
  SB();
  TS(9);

  const int uvo = h ? OFF_CFL + grp * 128 : OFF_UV + (256 * sl + 32 * wave + lane) * 16;
  const int gbo = OFF_MOD + (560 + 16 * sl + (tid & 15)) * 16;
  v4u uv = GLD(uvo), gbv = GLD(gbo);
  SB();
  const int trb = (8 * h + trq) * 64 + trc;
  const char* xg = ws + OFF_X1 + grp * XTILE + wave * 7168;
  const u32 xl = lane * 16;
  auto dma_x = [&](int pass, int i) {
    const int half = i >= 7, q = i - 7 * half, o = half * XHALF + (q >> 2) * 4096;
    const char* src = xg + pass * 32 * XTILE + o;
    if ((q & 3) == 0) asm volatile("s_mov_b32 m0, %0\n\ts_nop 0" :: "s"(lds0 + pass * XTILE + wave * 7168 + o) : "memory");
    asm volatile("global_load_lds_dwordx4 %0, %1 offset:%2 sc1" :: "v"(xl), "s"(src), "i"((q & 3) * 1024) : "memory");
  };
  auto accept_x = [&](int pass) {
    const int lx = pass * XTILE + wave * 7168;
    float s, q;
    SPIN {
      u32 m = 0;
      v4u t[7], f[7];
#pragma unroll
      for (int i = 0; i < 7; ++i) t[i] = LDS128(lx + i * 1024 + lane * 16);
#pragma unroll
      for (int i = 0; i < 7; ++i) f[i] = trfrag(lds + lx + XHALF + trb + i * 1024);
#pragma unroll
      for (int i = 0; i < 7; ++i) m = max4(m, t[i]);
      s = 0.f; q = 0.f;
#pragma unroll
      for (int i = 0; i < 7; ++i) sumsq(f[i], s, q);
      if (!__any(m == SENTW || q != q)) break;
      MEMB();
#pragma unroll
      for (int i = 0; i < 7; ++i) m = max4(m, LDS128(lx + XHALF + i * 1024 + lane * 16));
      if (!__any(m == SENTW)) break;
      __builtin_amdgcn_s_sleep(4);
#pragma unroll
      for (int i = 0; i < 14; ++i) dma_x(pass, i);
      VMW(0);
    }
    s = swap32_add(s); q = swap32_add(q);
    if (!h) { v2f o = {s, q}; LDS64F(L_ST + pass * ST_P + lane * 48 + wave * 8) = o; }
  };
  if (wave < 4) {
#pragma unroll
    for (int i = 0; i < 14; ++i) dma_x(0, i);
    VMW(0);
    asm volatile("" : "+v"(gbv));
    accept_x(0);
  } else {
    ldw1(0, 14);
  }
  TS(10);
  BAR();
  TS(11);
  if (wave >= 4) ldw1(14, 28);
  SPIN {
    if (__all(tagok(uv, tagL, tagH) && tagok(gbv, tagL, tagH))) break;
    __builtin_amdgcn_s_sleep(4); MEMB();
    uv = GLD(uvo); gbv = GLD(gbo);
  }
  v4u uvf;
  {
    const float u = __uint_as_float(uv.y), v = __uint_as_float(uv.z), uh = rbf(u), vh = rbf(v);
    uvf = (v4u){h ? 0u : pk(uh, u - uh), h ? 0u : pk(vh, v - vh), 0u, 0u};
  }
  const u32 gb0 = gbv.y, gb1 = gbv.z;

  v4u w2f[28];
  const int w2o = OFF_W2T + (sl * 8 + wave) * 28 * 1024;
  const v16f zero16 = {0, 0, 0, 0, 0, 0, 0, 0, 0, 0, 0, 0, 0, 0, 0, 0};
  auto gemm1 = [&](int pass, auto extra) {
    const int lx = pass * XTILE;
    LAS char* pb = lds + lx + trb;
    const v2u res = trd(lds + lx + XHALF + (64 * sl + 8 * wave + 4 * h + trq) * 64 + trc);
    v4u bf[4];
#pragma unroll
    for (int k = 0; k < 3; ++k) bf[k] = trfrag(pb + k * 1024);
    LDS64(L_RES + pass * RES_P + (2 * wave + h) * 272 + (lane & 31) * 8) = res;
    SB();
    v16f acc = zero16;
#pragma unroll
    for (int k = 0; k < 28; ++k) {
      if (k + 3 < 28) bf[(k + 3) & 3] = trfrag(pb + (k + 3) * 1024);
      acc = mfma32(w1f[k], bf[k & 3], acc);
      extra(k);
      SB();
    }
    return acc;
  };
  auto epi1 = [&](int pass, v16f acc) {
    const v4f a0 = LDS128F(L_ST + pass * ST_P + (lane & 31) * 48 + h * 16);
    const float mean = swap32_add(a0[0] + a0[2]) * (1.f / D);
    const float ex2 = swap32_add(a0[1] + a0[3]) * (1.f / D);
    const float var = rbf(fmaxf(ex2 - mean * mean, 0.f));
    const float sd = rbf(__builtin_amdgcn_sqrtf(rbf(var + 1e-5f)));
    const v4u bs = {h ? 0u : pk(-mean, -mean), h ? 0u : pk(sd, sd), 0u, 0u};
    acc = mfma32(uvf, bs, acc);
    const v2f rl = bc(__builtin_amdgcn_rcpf(sd) * -1.4426950408889634f), sd2 = bc(sd);
    u32 hp[8];
#pragma unroll
    for (int e = 0; e < 8; ++e) {
      const v2f a = {acc[2 * e], acc[2 * e + 1]}, t = a * rl;
      const v2f ex = {__builtin_amdgcn_exp2f(t.x), __builtin_amdgcn_exp2f(t.y)}, dn = fma2(ex, sd2, sd2);
      const v2f rc = {__builtin_amdgcn_rcpf(dn.x), __builtin_amdgcn_rcpf(dn.y)};
      hp[e] = pk(a * rc);
    }
    const int ho = opq(OFF_H + (grp + 32 * pass) * HTILE + (16 * sl + 2 * wave) * 1024 + lane * 16);
    v4u h0 = {hp[0], hp[1], hp[2], hp[3]}, h1 = {hp[4], hp[5], hp[6], hp[7]};
    GST(h0, ho); GST(h1, ho + 1024);
    SB();
  };
  const int l16w = opq(lane * 16);
  auto ldw2 = [&](int i) { w2f[i] = GLDS(l16w + (i & 3) * 1024, w2o + (i >> 2) * 4096); };
  v16f acc;
  if (wave < 4) {
    acc = gemm1(0, [&](int k) { if (!(k & 1)) dma_x(1, k >> 1); });
    TS(12);
    epi1(0, acc);
    VMW(2);
    accept_x(1);
    TS(13);
    BAR();
    acc = gemm1(1, [&](int k) { if (!(k & 1)) ldw2(k >> 1); });
  } else {
    acc = gemm1(0, [&](int k) { if (k >= 14 && !(k & 1)) ldw2((k - 14) >> 1); });
    TS(12);
    BAR();
    epi1(0, acc);
    TS(13);
    acc = gemm1(1, [&](int k) { if (!(k & 3)) ldw2(7 + (k >> 2)); });
  }
  TS(14);
  epi1(1, acc);
#pragma unroll
  for (int i = 14; i < 28; ++i) { ldw2(i); SB(); }
  BAR();
  TS(15);

  const int hoA = OFF_H + grp * HTILE + wave * 14 * 1024;
  auto gemm2 = [&](int ho, int lr) {
    v4u hf[14];
    const int l16 = opq(lane * 16);
#pragma unroll
    for (int i = 0; i < 14; ++i) { hf[i] = GLDS(l16 + (i & 3) * 1024, ho + (i >> 2) * 4096); SB(); }
    __builtin_amdgcn_s_setprio(0);
    v16f c0 = zero16, c1 = zero16;
#pragma unroll
    for (int i = 0; i < 14; ++i) { c0 = mfma32(w2f[2 * i], hf[i], c0); c1 = mfma32(w2f[2 * i + 1], hf[i], c1); SB(); }
    if (__any(c0[0] != c0[0])) {
      _Pragma("unroll 1") for (int i = 0; i < 14; ++i) {
        SPIN {
          MEMB();
          const v4u t = GLDS(lane * 16, ho + i * 1024);
          if (!__any(t.x == SENTW || t.y == SENTW || t.z == SENTW || t.w == SENTW)) break;
          __builtin_amdgcn_s_sleep(4);
        }
      }
      MEMB();
      c0 = zero16; c1 = zero16;
#pragma unroll
      for (int i = 0; i < 14; ++i) {
        const v4u t = GLDS(lane * 16, ho + i * 1024);
        c0 = mfma32(w2f[2 * i], t, c0); c1 = mfma32(w2f[2 * i + 1], t, c1);
        SB();
      }
    }
    const int o = lr + wave * RED_W + h * 528 + (lane & 31) * 16;
#pragma unroll
    for (int q = 0; q < 4; ++q) {
      v4f o0 = {c0[4 * q], c0[4 * q + 1], c0[4 * q + 2], c0[4 * q + 3]}, o1 = {c1[4 * q], c1[4 * q + 1], c1[4 * q + 2], c1[4 * q + 3]};
      LDS128F(o + 2 * q * 528) = o0;
      LDS128F(o + (8 + 2 * q) * 528) = o1;
    }
    SB();
  };
  __builtin_amdgcn_s_setprio(1);
  gemm2(hoA, L_REDA);
  TS(16);
  gemm2(hoA + 32 * HTILE, L_REDB);
  TS(17);
  BAR();
  {
    const int dq = tid & 15, l = tid >> 4;
#pragma unroll
    for (int pass = 0; pass < 2; ++pass) {
      const int lr = pass * L_REDB + dq * 528 + l * 16;
      v4f s = LDS128F(lr);
#pragma unroll
      for (int w = 1; w < 8; ++w) s += LDS128F(lr + w * RED_W);
      const v2u rv = LDS64(L_RES + pass * RES_P + dq * 272 + l * 8);
      v2u o = {pk(fma2(unpk(gb0), s.xy, unpk(rv.x))), pk(fma2(unpk(gb1), s.zw, unpk(rv.y)))};
      *(GAS v2u*)(out + (pass * 1024 + grp * 32 + l) * D + 64 * sl + 4 * dq) = o;
    }
  }
  TS(29); TSEND();
}

extern "C" void kernel_launch(void* const* d_in, const int* in_sizes, int n_in, void* d_out, int out_size, void* d_ws, size_t ws_size,
                              hipStream_t stream) {
  k_all<<<256, 512, 0, stream>>>((const u16*)d_in[0], (const u16*)d_in[1], (const u16*)d_in[2], (const u16*)d_in[3], (const u16*)d_in[4],
                                 (const u16*)d_in[5], (const u16*)d_in[6], (const u16*)d_in[7], (const u16*)d_in[8], (const u16*)d_in[10],
                                 (const u16*)d_in[11], (const u16*)d_in[12], (const u16*)d_in[13], (u16*)d_out, (char*)d_ws);
}
```

```cpp
#define PROBE 97
#define PB 16
#include <hip/hip_runtime.h>
#include <stdint.h>

typedef unsigned short u16;
typedef unsigned u32;
typedef unsigned long long u64;
typedef u32 v4u __attribute__((ext_vector_type(4)));
typedef u32 v2u __attribute__((ext_vector_type(2)));
typedef float v4f __attribute__((ext_vector_type(4)));
typedef float v2f __attribute__((ext_vector_type(2)));
typedef float v16f __attribute__((ext_vector_type(16)));
typedef __bf16 bf8 __attribute__((ext_vector_type(8)));
typedef __bf16 bf2 __attribute__((ext_vector_type(2)));
typedef short s4 __attribute__((ext_vector_type(4)));
#define GAS __attribute__((address_space(1)))
#define LAS __attribute__((address_space(3)))
#define DEV __device__ __forceinline__

constexpr int D = 448, F = 1792;

constexpr int OFF_STAT = 4096;
constexpr int OFF_MOD = 20480;
constexpr int OFF_UV = 32768;
constexpr int OFF_CFL = 61440;
constexpr int OFF_WFL = 65536;
constexpr int OFF_XT = 131072;
constexpr int OFF_W1T = 4 << 20;
constexpr int OFF_W2T = 6 << 20;
constexpr int OFF_X1 = 8 << 20;
constexpr int OFF_H = 12 << 20;
constexpr int OFF_DBG = 24 << 20;
constexpr int WS_BYTES = 32 << 20;
constexpr int XTILE = 57344, XHALF = 28672, HTILE = 114688;

constexpr int KG_W = 8704;
constexpr int L_KC = 0, KC_S = 4160, KC_CH = 4 * KC_S;
constexpr int L_DUMP0 = 33280;
constexpr int L_LNT = 69632;
constexpr int L_STX = 76800;
constexpr int L_KLIN = 76864, KLIN_CH = 2576;
constexpr int L_Z = 82048, Z_CH = 4608;
constexpr int L_XT = 91264;
constexpr int L_DUMP1 = 100480;
constexpr int L_XA = 0, L_XB = XTILE;
constexpr int RED_W = 8448, L_REDA = 0, L_REDB = 8 * RED_W;
constexpr int L_RES = 135168, RES_P = 4352;
constexpr int L_ST = 143872, ST_P = 1536;
constexpr int L_PF = 146944;
constexpr int LDS_BYTES = 147200;
constexpr int L_AR0 = 0, L_AR1 = 81920;
constexpr int L_ASC = 57344;
constexpr int L_ARED = 58368;
constexpr int L_AG = 60416;
constexpr int L_ARED2 = 62464;

constexpr u32 SPIN_CAP = 1u << 14;
constexpr u32 SENTW = 0xffbfffbfu;
#define CODE_PF 20480

DEV float blo(u32 v) { return __uint_as_float(v << 16); }
DEV float bhi(u32 v) { return __uint_as_float(v & 0xffff0000u); }
DEV v2f unpk(u32 v) { v2f r = {blo(v), bhi(v)}; return r; }
DEV u32 pk(float a, float b) { bf2 r = {(__bf16)a, (__bf16)b}; return __builtin_bit_cast(u32, r); }
DEV u32 pk(v2f a) { return pk(a.x, a.y); }
DEV float rbf(float a) { return blo(pk(a, a)); }
DEV u32 umax(u32 a, u32 b) { return a > b ? a : b; }
DEV u32 max4(u32 m, v4u v) { return umax(umax(umax(umax(m, v.x), v.y), v.z), v.w); }
DEV v2f fma2(v2f a, v2f b, v2f c) { return __builtin_elementwise_fma(a, b, c); }
DEV v2f bc(float a) { v2f r = {a, a}; return r; }
DEV v4f mfma16(v4u a, v4u b, v4f c) { return __builtin_amdgcn_mfma_f32_16x16x32_bf16(__builtin_bit_cast(bf8, a), __builtin_bit_cast(bf8, b), c, 0, 0, 0); }
DEV v16f mfma32(v4u a, v4u b, v16f c) { return __builtin_amdgcn_mfma_f32_32x32x16_bf16(__builtin_bit_cast(bf8, a), __builtin_bit_cast(bf8, b), c, 0, 0, 0); }
DEV float dot2(u32 a, u32 b, float c) { return __builtin_amdgcn_fdot2_f32_bf16(__builtin_bit_cast(bf2, a), __builtin_bit_cast(bf2, b), c, false); }
DEV void sumsq(v4u f, float& s, float& q) {
  s = dot2(f.x, 0x3f803f80u, s); q = dot2(f.x, f.x, q); s = dot2(f.y, 0x3f803f80u, s); q = dot2(f.y, f.y, q);
  s = dot2(f.z, 0x3f803f80u, s); q = dot2(f.z, f.z, q); s = dot2(f.w, 0x3f803f80u, s); q = dot2(f.w, f.w, q);
}
DEV v2u trd(LAS char* p) { return __builtin_bit_cast(v2u, __builtin_amdgcn_ds_read_tr16_b64_v4i16((LAS s4*)p)); }
DEV v4u trfrag(LAS char* p) { v2u a = trd(p), b = trd(p + 256); v4u r = {a.x, a.y, b.x, b.y}; return r; }
template <int CTRL, int RM> DEV float dppadd(float v) {
  return v + __builtin_bit_cast(float, __builtin_amdgcn_update_dpp(0, __builtin_bit_cast(int, v), CTRL, RM, 0xf, RM == 0xf));
}
DEV float rdl(float v, int l) { return __builtin_bit_cast(float, __builtin_amdgcn_readlane(__builtin_bit_cast(int, v), l)); }
DEV float swap32_add(float v) { auto r = __builtin_amdgcn_permlane32_swap(__float_as_uint(v), __float_as_uint(v), false, false); return __uint_as_float(r[0]) + __uint_as_float(r[1]); }
DEV float swap16_add(float v) { auto r = __builtin_amdgcn_permlane16_swap(__float_as_uint(v), __float_as_uint(v), false, false); return __uint_as_float(r[0]) + __uint_as_float(r[1]); }
DEV int opq(int v) { asm volatile("" : "+v"(v)); return v; }
DEV v2f cmul(v2f a, v2f w, v2f wn) { return fma2(a.yx, wn, a * w.xx); }
DEV v2f cneg(v2f w) { v2f r = {-w.y, w.y}; return r; }
DEV v2f cexpx(float er, float ei, float x) {
  const float e = __builtin_amdgcn_exp2f(er * x), ph = __builtin_amdgcn_fractf(ei * x);
  v2f c = {__builtin_amdgcn_cosf(ph), __builtin_amdgcn_sinf(ph)};
  return c * bc(e);
}

#define BAR() asm volatile("s_waitcnt lgkmcnt(0)\n\ts_barrier" ::: "memory")
#define VMW(n) asm volatile("s_waitcnt vmcnt(" #n ")" ::: "memory")
#define LGK0() __builtin_amdgcn_s_waitcnt(0xC07F)
#define MEMB() asm volatile("" ::: "memory")
#define SB() __builtin_amdgcn_sched_barrier(0)
#define SPIN _Pragma("unroll 1") for (u32 sp = 0; sp < SPIN_CAP; ++sp)
#define LDS128(off) (*(LAS v4u*)(lds + (off)))
#define LDS128F(off) (*(LAS v4f*)(lds + (off)))
#define LDS64(off) (*(LAS v2u*)(lds + (off)))
#define LDS64F(off) (*(LAS v2f*)(lds + (off)))
#define LDS32(off) (*(LAS u32*)(lds + (off)))
#define LDS32F(off) (*(LAS float*)(lds + (off)))
#define GLD(off) __builtin_amdgcn_raw_buffer_load_b128(rs, (off), 0, 16)
#define GLDS(voff, soff) __builtin_amdgcn_raw_buffer_load_b128(rs, (voff), (soff), 16)
#define GST(v, off) __builtin_amdgcn_raw_buffer_store_b128((v), rs, (off), 0, 16)

DEV void dma16(u32 ldsaddr, const char* gbase, u32 voff) {
  asm volatile("s_mov_b32 m0, %0\n\ts_nop 0\n\tglobal_load_lds_dwordx4 %1, %2" :: "s"(ldsaddr), "v"(voff), "s"(gbase) : "memory");
}
DEV void dma16_dev(u32 ldsaddr, const char* gbase, u32 voff) {
  asm volatile("s_mov_b32 m0, %0\n\ts_nop 0\n\tglobal_load_lds_dwordx4 %1, %2 sc1" :: "s"(ldsaddr), "v"(voff), "s"(gbase) : "memory");
}
template <int OFF> DEV void st_tid(u32 v) { asm volatile("ds_write_addtid_b32 %0 offset:%1" :: "v"(v), "i"(OFF) : "memory"); }

#define TS(i) do { if (i == 0) pr0_ = __builtin_amdgcn_s_memrealtime(); if (i == PROBE) pr1_ = __builtin_amdgcn_s_memrealtime(); } while (0)
#define TSEND() do { if (b == PB && wave == 0) { if (PROBE == 99) { const u64 c = __builtin_amdgcn_s_memtime(); while (__builtin_amdgcn_s_memtime() - c < 200000) {} } \
  else { const u64 c = __builtin_amdgcn_s_memrealtime(), dl = 10 * (pr1_ - pr0_); while (__builtin_amdgcn_s_memrealtime() - c < dl) {} } } } while (0)

DEV bool tagok(v4u g, u32 tagL, u32 tagH) { return g.x == tagL && g.w == tagH; }

DEV void ln1_publish(LAS char* lds, __amdgpu_buffer_rsrc_t rs, v4u xr, u32 b, int tid, int lane, u32 wave, u32 tagL, u32 tagH) {
  if (lane < 56) {
    const int o = L_LNT + ((wave >> 1) * 224 + 4 * lane) * 8 + (wave & 1) * 4;
    LDS32(o) = xr.x; LDS32(o + 8) = xr.y; LDS32(o + 16) = xr.z; LDS32(o + 24) = xr.w;
  }
  float s = 0.f, q = 0.f;
  sumsq(xr, s, q);
  s = dppadd<0x111, 0xf>(s); q = dppadd<0x111, 0xf>(q); s = dppadd<0x112, 0xf>(s); q = dppadd<0x112, 0xf>(q);
  s = dppadd<0x114, 0xf>(s); q = dppadd<0x114, 0xf>(q); s = dppadd<0x118, 0xf>(s); q = dppadd<0x118, 0xf>(q);
  s = swap32_add(swap16_add(s)); q = swap32_add(swap16_add(q));
  const float mean = s * (1.f / D), var = rbf(fmaxf(q * (1.f / D) - mean * mean, 0.f));
  const float sd = __builtin_amdgcn_sqrtf(rbf(var + 1e-5f));
  if (lane == 15) LDS32(L_STX + wave * 4) = pk(mean, sd);
  BAR();
  const int lo = L_LNT + ((tid & 3) * 224 + (tid >> 2)) * 8, go = OFF_XT + (tid >> 2) * 16384 + (4 * b + (tid & 3)) * 16;
  {
    v2u g = LDS64(lo);
    v4u o = {tagL, g.x, g.y, tagH};
    GST(o, go);
  }
  if (wave < 6) {
    v2u g = LDS64(lo + 1024);
    v4u o = {tagL, g.x, g.y, tagH};
    GST(o, go + 128 * 16384);
  } else if (wave == 7 && lane < 4) {
    v2u g = LDS64(L_STX + lane * 8);
    v4u o = {tagL, g.x, g.y, tagH};
    GST(o, OFF_STAT + (4 * b + lane) * 16);
  }
}

DEV void conv_epi(LAS char* lds, __amdgpu_buffer_rsrc_t rs, int dump, int lz, int goff, float ga, float gd, float g2) {
  const v4f p0 = LDS128F(dump), p1 = LDS128F(dump + 4096);
  const v2u hh = LDS64(L_Z + 480 + lz), xx = LDS64(L_XT + lz);
  const v2f o0 = fma2(bc(ga), p0.xy + p1.xy, fma2(bc(gd), unpk(hh.x), unpk(xx.x)));
  const v2f o1 = fma2(bc(ga), p0.zw + p1.zw, fma2(bc(gd), unpk(hh.y), unpk(xx.y)));
  const u32 X0 = pk(o0), X1 = pk(o1);
  const u32 S0 = pk(unpk(X0) * bc(g2)), S1 = pk(unpk(X1) * bc(g2));
  auto r0 = __builtin_amdgcn_permlane16_swap(X0, S0, false, false);
  auto r1 = __builtin_amdgcn_permlane16_swap(X1, S1, false, false);
  v4u v = {r0[0], r1[0], r0[1], r1[1]};
  GST(v, goff);
}

__global__ void __launch_bounds__(512, 2) k_all(const u16* __restrict__ x, const u16* __restrict__ Are, const u16* __restrict__ Aim,
    const u16* __restrict__ Bre, const u16* __restrict__ Bim, const u16* __restrict__ Cre, const u16* __restrict__ Cim,
    const u16* __restrict__ logdt, const u16* __restrict__ Dskip, const u16* __restrict__ w1, const u16* __restrict__ w2,
    const u16* __restrict__ cvec, const u16* __restrict__ wmod, u16* __restrict__ out, char* ws) {
  __shared__ __attribute__((aligned(16))) char smem[LDS_BYTES];
  LAS char* lds = (LAS char*)smem;
  const u32 lds0 = (u32)(size_t)lds;
  u64 pc;
  u64 prs_;
  asm volatile("s_memrealtime %0" : "=s"(prs_));
  __builtin_amdgcn_sched_barrier(0);
  asm volatile("s_getpc_b64 %0" : "=s"(pc));
  asm volatile("" :: "s"(x), "s"(Are), "s"(Aim), "s"(Bre), "s"(Bim), "s"(Cre), "s"(Cim), "s"(logdt), "s"(Dskip), "s"(w1), "s"(w2), "s"(cvec), "s"(wmod), "s"(out), "s"(ws));
  const int tid = threadIdx.x, lane = tid & 63;
  const u32 wave = __builtin_amdgcn_readfirstlane(tid >> 6);
  const u32 b = blockIdx.x, xcd = b & 7, j = b >> 3;
  u64 pr0_ = 0, pr1_ = 0;
  asm volatile("s_waitcnt lgkmcnt(0)" : "+s"(prs_));
  TS(0);
  pr1_ = pr0_; pr0_ = prs_;
  const __amdgpu_buffer_rsrc_t rs = __builtin_amdgcn_make_buffer_rsrc(ws, 0, WS_BYTES, 0x00020000);
  const v4u xr = __builtin_amdgcn_raw_buffer_load_b128(__builtin_amdgcn_make_buffer_rsrc((void*)(x + (8 * b + wave) * D), 0, 2 * D, 0x00020000), lane * 16, 0, 0);
  const v2u e0 = __builtin_amdgcn_raw_buffer_load_b64(rs, 0, 0, 0);
  SB();
  const v4u SENT = {SENTW, SENTW, SENTW, SENTW};
  auto gettag = [&](u32& tl, u32& th) {
    const u64 T = (((u64)__builtin_amdgcn_readfirstlane(e0.y) << 32) | __builtin_amdgcn_readfirstlane(e0.x)) + 0x9E3779B97F4A7C15ull;
    tl = (u32)T; th = (u32)(T >> 32);
  };
  u32 tagL, tagH;

  if (j >= 28) {
    const u32 a = (j - 28) * 8 + xcd;
    const int h = lane >> 5, trq = (lane & 15) >> 2, trc = ((lane >> 4) & 1) * 32 + (lane & 3) * 8;
    const u32 wv = wave < 7 ? wave : 0;
    auto stage = [&](u32 ldst, const char* src, u32 rstride) {
      if (wave < 7) {
        const u32 vo = ((lane >> 3) + 64 * wave) * rstride + (lane & 7) * 16;
#pragma unroll
        for (int i = 0; i < 8; ++i) dma16(lds0 + ldst + wave * 8192 + i * 1024, src + i * 8 * rstride, vo);
      }
    };
    auto gemv = [&](u32 lsrc, int atab, float& r0, float& r1) {
      v16f c0 = {0, 0, 0, 0, 0, 0, 0, 0, 0, 0, 0, 0, 0, 0, 0, 0}, c1 = c0;
      LAS char* pb = lds + opq(lsrc + wv * 8192 + (8 * h + trq) * 128 + trc);
#pragma unroll
      for (int k = 0; k < 4; ++k) {
        const v4u af = LDS128(atab + wv * 128 + k * 32 + h * 16);
        const v2u b0 = trd(pb + k * 2048), b1 = trd(pb + k * 2048 + 512), b2 = trd(pb + k * 2048 + 64), b3 = trd(pb + k * 2048 + 576);
        c0 = mfma32(af, (v4u){b0.x, b0.y, b1.x, b1.y}, c0); c1 = mfma32(af, (v4u){b2.x, b2.y, b3.x, b3.y}, c1);
      }
      r0 = h ? c1[0] : c0[0]; r1 = h ? c1[1] : c0[1];
    };
    const u32 cv = cvec[64 * wv + lane];
    u32 lc = a < 21 ? a : (a < 28 ? a + 7 : a - 7);
    stage(L_AR0, (const char*)wmod + lc * 128, 6 * D * 2);
    SB();
    {
      const float cf_ = blo(cv);
      *(LAS u16*)(lds + L_ASC + (64 * wv + lane) * 2) = (u16)pk(cf_ * __builtin_amdgcn_rcpf(1.f + __expf(-cf_)), 0.f);
    }
    gettag(tagL, tagH);
    TS(1);
    ln1_publish(lds, rs, xr, b, tid, lane, wave, tagL, tagH);
    TS(2);
    VMW(1);
    if (a >= 4) stage(L_AR1, (const char*)w1 + (a - 4) * 128, F * 2);
    _Pragma("unroll 1") for (int round = 0;; ++round) {
      float r0, r1;
      gemv(L_AR0, L_ASC, r0, r1);
      LDS32F(L_ARED + wave * 256 + lane * 4) = r0;
      BAR();
      const bool more = round == 0 && a < 10;
      if (more) stage(L_AR0, (const char*)wmod + (a < 3 ? 25 + a : 32 + a) * 128, 6 * D * 2);
      if (tid < 16) {
        v4f s = LDS128F(L_ARED + tid * 16);
#pragma unroll
        for (int w = 1; w < 7; ++w) s += LDS128F(L_ARED + w * 256 + tid * 16);
        v4u o = {tagL, pk(s[0], s[1]), pk(s[2], s[3]), tagH};
        GST(o, OFF_MOD + (16 * lc + tid) * 16);
      }
      if (!more) break;
      lc = a < 3 ? 25 + a : 32 + a;
      VMW(0);
      BAR();
    }
    TS(3);
    const int hc = OFF_H + a * HTILE + tid * 16;
#pragma unroll
    for (int i = 0; i < 8; ++i) GST(SENT, hc + i * 8192);
    SB();
    TS(4);
    if (a >= 4) {
      if (wave < 4) {
        const int gi = 336 + (tid < 224 ? tid : 0);
        v4u g;
        SPIN {
          MEMB();
          g = GLD(OFF_MOD + gi * 16);
          if (__all(tagok(g, tagL, tagH))) break;
          __builtin_amdgcn_s_sleep(8);
        }
        v2u f = {g.y, g.z};
        if (tid >= 112) { f.x = pk(unpk(f.x) + bc(1.f)); f.y = pk(unpk(f.y) + bc(1.f)); }
        if (tid < 224) LDS64(L_AG + tid * 8) = f;
      }
      VMW(0);
      BAR();
      TS(5);
      float r0, r1;
      gemv(L_AR1, L_AG + ((lane & 1) ? 0 : 2 * D), r0, r1);
      { v2f o = {r0, r1}; LDS64F(L_ARED2 + wave * 512 + lane * 8) = o; }
      BAR();
      if (tid < 64) {
        v2f s = LDS64F(L_ARED2 + tid * 8);
#pragma unroll
        for (int w = 1; w < 7; ++w) s += LDS64F(L_ARED2 + w * 512 + tid * 8);
        v4u o = {tagL, __float_as_uint(s[0]), __float_as_uint(s[1]), tagH};
        GST(o, OFF_UV + (64 * (a - 4) + tid) * 16);
      }
    }
    SB();
    TS(6);
#pragma unroll
    for (int i = 8; i < 14; ++i) GST(SENT, hc + i * 8192);
#pragma unroll
    for (int i = 0; i < 14; ++i) GST(SENT, hc + 32 * HTILE + i * 8192);
    VMW(0);
    BAR();
    if (tid == 0) { v4u f = {tagL, 0, 0, tagH}; GST(f, OFF_CFL + a * 128); }
    TS(29); TSEND();
    return;
  }

  const u32 p = xcd * 28 + j;
  const u32 jq = (j * 37) >> 8;
  const u32 grp = xcd * 4 + jq, sl = j - 7 * jq;
  const u32 chw = wave >> 2;

  const u32 pi = ((2 * p + chw) * 256 + (tid & 255)) * 2;
  auto ldp = [&](const u16* q) { return (u32)*(const GAS u16*)((const GAS char*)q + pi); };
  const u32 pAr = ldp(Are), pAi = ldp(Aim), pBr = ldp(Bre), pBi = ldp(Bim), pCr = ldp(Cre), pCi = ldp(Cim);
  const u32 pLd2 = *(const GAS u32*)(logdt + 2 * p), dk2 = *(const GAS u32*)(Dskip + 2 * p);
  SB();
  const bool isw1 = xcd < 4;
  u32 wt_dst = 0;
  if (wave < 7) {
    const char* src; u32 rstride;
    if (isw1) {
      const u32 kap = p >> 2, fg = (p & 3) * 7 + wave;
      src = (const char*)w1 + kap * (16 * F * 2) + fg * 128; rstride = F * 2;
      wt_dst = OFF_W1T + (fg * 56 + kap) * 1024;
    } else {
      const u32 kk = p - 112;
      src = (const char*)w2 + kk * (16 * D * 2) + wave * 128; rstride = D * 2;
      wt_dst = OFF_W2T + (wave * 224 + 2 * kk) * 1024;
    }
    const u32 vo = (lane >> 3) * rstride + (lane & 7) * 16;
    dma16(lds0 + wave * KG_W, src, vo);
    dma16(lds0 + wave * KG_W + 1024, src + 8 * rstride, vo);
  } else if (CODE_PF) {
    u32 vo = (j & 3) * 8192 + lane * 128; vo = vo < CODE_PF ? vo : 0;
    asm volatile("s_mov_b32 m0, %0\n\ts_nop 0\n\tglobal_load_lds_dword %1, %2" :: "s"(lds0 + L_PF), "v"(vo), "s"(pc & ~255ull) : "memory");
  }
  SB();
  {
    const int o = OFF_X1 + (tid >> 3) * XHALF + p * 128 + (tid & 7) * 16;
    GST(SENT, o); GST(SENT, o + 64 * XHALF);
  }
  SB();
  int kap[7];
#pragma unroll
  for (int e = 0; e < 7; ++e) {
    const int l = 4 * tid + e - 3;
    const int kb = (int)(pk((float)l, (float)l) & 0xffffu) - 0x4280;
    kap[e] = opq(L_KLIN + 4 * (l < 128 ? (l < 0 ? 643 : l) : kb));
  }
  int rd_src, rd_dst, rd_dk;
  {
    const int ch = tid >= 200, rem = tid - 200 * ch, jj = rem >> 3, iq = rem & 7;
    int cj, sh, base;
    if (jj < 16) { cj = jj; sh = iq >= 4; base = sh * 128; }
    else if (jj < 20) { cj = 512 + 4 * (jj - 16); sh = 2; base = 256; }
    else { cj = 1024 + 512 * ((jj - 20) >> 1) + 8 * (jj & 1); sh = 3; base = 384; }
    const bool okj = jj < 16 ? (iq < 4 || !(jj & 1)) : jj < 24;
    rd_src = opq(ch * 4 * KG_W + 144 * jj + 16 * iq);
    rd_dst = opq(L_KLIN + ch * KLIN_CH + 4 * ((okj || iq == 0) ? base + ((64 * iq + cj) >> sh) : 641));
    rd_dk = opq(okj ? 64 >> sh : 0);
    if (jj == 24 && iq == 0) rd_dk = -1;
  }
  SB();
  gettag(tagL, tagH);
  ln1_publish(lds, rs, xr, b, tid, lane, wave, tagL, tagH);
  TS(1);

  const int h = lane >> 5, trq = (lane & 15) >> 2, trc = ((lane >> 4) & 1) * 32 + (lane & 3) * 8;
  v4u wt0, wt1;
  if (wave < 7) {
    VMW(3);
    const int r0 = isw1 ? 8 * h + trq : 4 * h + trq, r1 = isw1 ? 512 : 1024;
    LAS char* pb = lds + wave * KG_W + r0 * 128 + trc;
    v2u a0 = trd(pb), a1 = trd(pb + r1), b0 = trd(pb + 64), b1 = trd(pb + 64 + r1);
    wt0 = (v4u){a0.x, a0.y, a1.x, a1.y}; wt1 = (v4u){b0.x, b0.y, b1.x, b1.y};
  }
  MEMB();

  {
    const float dt = rbf(expf(chw ? bhi(pLd2) : blo(pLd2)));
    const float er = (dt * blo(pAr)) * 1.4426950408889634f, ei = (dt * blo(pAi)) * 0.15915494309189535f;
    const v2f dB = {dt * blo(pBr), dt * blo(pBi)}, Cc = {blo(pCr), blo(pCi)};
    v2f X = cmul(Cc, dB, cneg(dB));
    X.y = -X.y;
    const v2f e1 = cexpx(er, ei, 1.f), e16 = cexpx(er, -ei, 16.f), e512 = cexpx(er, ei, 512.f);
    const v2f e1n = cneg(e1), e16n = cneg(e16), e512n = cneg(e512);
    asm volatile("s_mov_b32 m0, %0\n\ts_nop 0" :: "s"(lds0 + wave * KG_W) : "memory");
    st_tid<0>(0x00003f80u);
    v2f Y = e1, Y4, Y8, Y12;
#define YROW(jj) st_tid<jj * 272>(pk(Y)); if (jj == 4) Y4 = Y; if (jj == 8) Y8 = Y; if (jj == 12) Y12 = Y; if (jj < 15) Y = cmul(Y, e1, e1n);
    YROW(1) YROW(2) YROW(3) YROW(4) YROW(5) YROW(6) YROW(7) YROW(8) YROW(9) YROW(10) YROW(11) YROW(12) YROW(13) YROW(14) YROW(15)
    st_tid<16 * 272>(pk(e512));
    st_tid<17 * 272>(pk(cmul(Y4, e512, e512n)));
    Y = cmul(Y8, e512, e512n);  st_tid<18 * 272>(pk(Y));
    st_tid<19 * 272>(pk(cmul(Y12, e512, e512n)));
    v2f E = cmul(e512, e512, e512n); st_tid<20 * 272>(pk(E));
    Y = cmul(Y, e512, e512n);   st_tid<21 * 272>(pk(Y));
    E = cmul(E, e512, e512n);   st_tid<22 * 272>(pk(E));
    Y = cmul(Y, e512, e512n);   st_tid<23 * 272>(pk(Y));
    E = cmul(E, e512, e512n);   st_tid<24 * 272>(pk(E));
    if (wave < 7) {
      GST(wt0, lane * 16 + wt_dst);
      GST(wt1, lane * 16 + wt_dst + (isw1 ? 28 * 1024 : 1024));
    }
    const int r = lane & 31;
    v4u yf[8];
    LAS char* py = lds + wave * KG_W + (r < 24 ? r : 24) * 272 + 16 * h;
    LAS char* px = lds + wave * KG_W + r * 272 + 16 * h;
#pragma unroll
    for (int m = 0; m < 8; ++m) yf[m] = *(LAS v4u*)(py + 32 * m);
#define XROW(i) st_tid<(i) * 272>(pk(X)); if ((i) < 31) X = cmul(X, e16, e16n);
#define XROW4(i) XROW(i) XROW(i + 1) XROW(i + 2) XROW(i + 3)
    XROW4(0) XROW4(4) XROW4(8) XROW4(12) XROW4(16) XROW4(20) XROW4(24) XROW4(28)
    v16f acc = {0, 0, 0, 0, 0, 0, 0, 0, 0, 0, 0, 0, 0, 0, 0, 0};
    v4u xf[8];
#pragma unroll
    for (int m = 0; m < 8; ++m) xf[m] = *(LAS v4u*)(px + 32 * m);
#pragma unroll
    for (int m = 0; m < 8; ++m) acc = mfma32(xf[m], yf[m], acc);
#pragma unroll
    for (int q = 0; q < 4; ++q) {
      v4f o = {acc[4 * q], acc[4 * q + 1], acc[4 * q + 2], acc[4 * q + 3]};
      LDS128F(wave * KG_W + 144 * r + 32 * q + 16 * h) = o;
    }
  }
  TS(2);
  BAR();
  TS(3);
  if (wave == 7) {
    const v4u z0 = {0, 0, 0, 0};
    if (lane < 60) LDS128(L_Z + (lane >= 30) * (Z_CH - 480) + lane * 16) = z0;
    if (lane < 2) LDS32(L_KLIN + lane * KLIN_CH + 4 * 643) = 0;
  } else if (tid < 400) {
    const v4f s = (LDS128F(rd_src) + LDS128F(rd_src + KG_W)) + (LDS128F(rd_src + 2 * KG_W) + LDS128F(rd_src + 3 * KG_W));
    LDS32F(rd_dst) = s[0];
    if (rd_dk >= 0) { LDS32F(rd_dst + rd_dk) = s[1]; LDS32F(rd_dst + 2 * rd_dk) = s[2]; LDS32F(rd_dst + 3 * rd_dk) = s[3]; }
  }
  VMW(0);
  BAR();
  TS(19);
  if (tid == 448) { v4u f = {tagL, 0, 0, tagH};
    f.y = (u32)prs_;
    GST(f, OFF_WFL + p * 128); }
#pragma unroll
  for (int ch = 0; ch < 2; ++ch) {
    float k[7];
#pragma unroll
    for (int e = 0; e < 7; ++e) k[e] = LDS32F(kap[e] + ch * KLIN_CH);
    const u32 d10 = pk(k[1], k[0]), d21 = pk(k[2], k[1]), d32 = pk(k[3], k[2]), d43 = pk(k[4], k[3]), d54 = pk(k[5], k[4]), d65 = pk(k[6], k[5]);
    const int o = L_KC + ch * KC_CH + (511 - tid) * 8;
    v2u c0 = {d32, d10}, c1 = {d43, d21}, c2 = {d54, d32}, c3 = {d65, d43};
    LDS64(o) = c0; LDS64(o + KC_S) = c1; LDS64(o + 2 * KC_S) = c2; LDS64(o + 3 * KC_S) = c3;
  }
  if (tid < 32) { const v2u z0 = {0, 0}; LDS64(L_KC + (tid >> 2) * KC_S + 4096 + (tid & 3) * 8) = z0; }
  TS(4);

  float ga_[2], gd_[2], g2_[2];
  {
    v4u gx0, gx1, gs0, gs1, gm;
    const int l5 = lane & 31;
    const int mo = l5 < 4 ? OFF_MOD + ((l5 == 3 ? 4 : l5) * 112 + (p >> 1)) * 16 : OFF_WFL + (wave * 28 + l5 - 4) * 128;
    SPIN {
      MEMB();
      gx0 = GLDS(tid * 16, OFF_XT + p * 16384); gx1 = GLDS(tid * 16, OFF_XT + p * 16384 + 8192);
      gs0 = GLDS(tid * 16, OFF_STAT); gs1 = GLDS(tid * 16, OFF_STAT + 8192);
      gm = GLD(mo);
      const u32 bad = ((gx0.x ^ tagL) | (gx1.x ^ tagL) | (gs0.x ^ tagL)) | ((gs1.x ^ tagL) | (gm.x ^ tagL) | (gx0.w ^ tagH)) |
                      ((gx1.w ^ tagH) | (gs0.w ^ tagH) | (gs1.w ^ tagH)) | (gm.w ^ tagH);
      if (!__any(bad != 0)) break;
      __builtin_amdgcn_s_sleep(4);
    }
    const int mv = (p & 1) ? gm.z : gm.y;
    const u32 sa2 = __builtin_amdgcn_readlane(mv, 0), ca2 = __builtin_amdgcn_readlane(mv, 1), ga2 = __builtin_amdgcn_readlane(mv, 2), cb2 = __builtin_amdgcn_readlane(mv, 3);
    const v2f sa = unpk(sa2), g1 = unpk(pk(unpk(ca2) + bc(1.f))), g2 = unpk(pk(unpk(cb2) + bc(1.f))), ga = unpk(ga2), gd = ga * unpk(dk2);
    ga_[0] = ga.x; ga_[1] = ga.y; gd_[0] = gd.x; gd_[1] = gd.y; g2_[0] = g2.x; g2_[1] = g2.y;
#pragma unroll
    for (int q = 0; q < 2; ++q) {
      const v4u gx = q ? gx1 : gx0, gs = q ? gs1 : gs0;
      const v2f h0 = fma2((unpk(gx.y) - bc(blo(gs.y))) * bc(__builtin_amdgcn_rcpf(bhi(gs.y))), g1, sa);
      const v2f h1 = fma2((unpk(gx.z) - bc(blo(gs.z))) * bc(__builtin_amdgcn_rcpf(bhi(gs.z))), g1, sa);
      const int lo = (q * 512 + tid) * 4;
      LDS32(L_Z + 480 + lo) = pk(h0.x, h1.x);
      LDS32(L_Z + Z_CH + 480 + lo) = pk(h0.y, h1.y);
      LDS32(L_XT + lo) = __builtin_amdgcn_perm(gx.z, gx.y, 0x05040100u);
      LDS32(L_XT + Z_CH + lo) = __builtin_amdgcn_perm(gx.z, gx.y, 0x07060302u);
    }
  }
  BAR();
  TS(5);

  v4u w1f[28];
  const int w1o = OFF_W1T + (sl * 8 + wave) * 28 * 1024;
  const int c16 = lane & 15, g4 = lane >> 4;
  const int epi_l = 32 * c16 + 8 * g4;
  const int epi_g = OFF_X1 + (c16 >> 1) * XTILE + ((g4 & 1) ? 0 : XHALF) + p * 128 + (c16 & 1) * 32 + (g4 >> 1) * 16;
  auto ldw1 = [&](int i0, int i1) {
    const int l16 = opq(lane * 16);
#pragma unroll
    for (int i = i0; i < i1; ++i) { w1f[i] = GLDS(l16 + (i & 3) * 1024, w1o + (i >> 2) * 4096); SB(); }
  };
  if (wave >= 4) {
    const int ch = (wave >> 1) & 1, kh = wave & 1;
    LAS char* pa = lds + opq(L_KC + ch * KC_CH + (c16 & 3) * KC_S + 4104 - 8 * (c16 >> 2) - 16 * g4 - 256 * kh - 64 * 59);
    LAS char* pbz = lds + opq(L_Z + ch * Z_CH + 496 + 32 * c16 - 16 * g4 - 256 * kh - 192);
    v4u A[2][8], B[2][8];
    auto rd = [&](int s, int rho, int na, int n) {
      if (n >= 3 * na) return;
      const int i = n / 3, m = n % 3;
      if (m == 0) B[s][na - 1 - i] = *(LAS v4u*)(pbz + 64 * (8 * (na - 1 - i) + 3 - rho));
      else { const v2u u = *(LAS v2u*)(pa + 64 * (59 - 8 * i - rho) + 8 * (m - 1)); if (m == 1) { A[s][i].x = u.x; A[s][i].y = u.y; } else { A[s][i].z = u.x; A[s][i].w = u.y; } }
      SB();
    };
    v4f acc[4];
    __builtin_amdgcn_s_setprio(1);
#pragma unroll
    for (int n = 0; n < 12; ++n) rd(0, 0, 4, n);
#pragma unroll
    for (int t = 0; t < 4; ++t) acc[t] = (v4f){0, 0, 0, 0};
#pragma unroll
    for (int rho = 0; rho < 4; ++rho) {
      LGK0(); SB();
      int n = 0;
#pragma unroll
      for (int a = 0; a < 4; ++a) {
#pragma unroll
        for (int dl = 3 - a; dl >= 0; --dl) {
          acc[a + dl] = mfma16(A[rho & 1][a], B[rho & 1][dl], acc[a + dl]);
          if (rho < 3) { rd((rho + 1) & 1, rho + 1, 4, n); rd((rho + 1) & 1, rho + 1, 4, n + 1); }
          else { rd(0, 0, 8, n); rd(0, 0, 8, n + 1); }
          n += 2; SB();
        }
      }
    }
#pragma unroll
    for (int n = 20; n < 24; ++n) rd(0, 0, 8, n);
#pragma unroll
    for (int t = 0; t < 4; ++t) LDS128F(L_DUMP0 + ((wave - 4) * 4 + t) * 1024 + lane * 16) = acc[t];
    TS(20);
    BAR();
    TS(6);
#pragma unroll
    for (int t = 0; t < 4; ++t) acc[t] = (v4f){0, 0, 0, 0};
#pragma unroll
    for (int rho = 0; rho < 4; ++rho) {
      LGK0(); SB();
      int n = 0, cnt = 0;
#pragma unroll
      for (int a = 0; a < 8; ++a) {
#pragma unroll
        for (int dl = 7 - a; dl >= 0 && dl >= 4 - a; --dl) {
          acc[a + dl - 4] = mfma16(A[rho & 1][a], B[rho & 1][dl], acc[a + dl - 4]);
          if (rho < 3) { rd((rho + 1) & 1, rho + 1, 8, n++); if (cnt < 6) rd((rho + 1) & 1, rho + 1, 8, n++); }
          ++cnt; SB();
        }
      }
    }
#pragma unroll
    for (int t = 0; t < 4; ++t) LDS128F(L_DUMP1 + ((wave - 4) * 4 + t) * 1024 + lane * 16) = acc[t];
    __builtin_amdgcn_s_setprio(0);
    TS(21);
    BAR();
  } else {
    if (b == 0 && tid == 0) { v2u e = {tagL, tagH}; __builtin_amdgcn_raw_buffer_store_b64(e, rs, 0, 0, 16); }
    ldw1(0, 8);
    TS(20);
    BAR();
    TS(6);
    const int ch = wave >> 1, t0 = (wave & 1) * 2;
    const float ga = ch ? ga_[1] : ga_[0], gd = ch ? gd_[1] : gd_[0], g2 = ch ? g2_[1] : g2_[0];
    const int dm = L_DUMP0 + (ch * 8 + t0) * 1024 + lane * 16, lz = ch * Z_CH + t0 * 512 + epi_l, go = epi_g + ch * 64 + t0 * 8 * XTILE;
    conv_epi(lds, rs, dm, lz, go, ga, gd, g2);
    conv_epi(lds, rs, dm + 1024, lz + 512, go + 8 * XTILE, ga, gd, g2);
    SB();
    TS(7);
    ldw1(8, 28);
    TS(21);
    BAR();
  }
  TS(8);
  {
    const int t = wave & 3;
    conv_epi(lds, rs, L_DUMP1 + (chw * 8 + t) * 1024 + lane * 16, chw * Z_CH + (4 + t) * 512 + epi_l, epi_g + chw * 64 + (4 + t) * 8 * XTILE,
             chw ? ga_[1] : ga_[0], chw ? gd_[1] : gd_[0], chw ? g2_[1] : g2_[0]);
  }
  SB();
  TS(9);

  const int uvo = h ? OFF_CFL + grp * 128 : OFF_UV + (256 * sl + 32 * wave + lane) * 16;
  const int gbo = OFF_MOD + (560 + 16 * sl + (tid & 15)) * 16;
  v4u uv = GLD(uvo), gbv = GLD(gbo);
  SB();
  const int trb = (8 * h + trq) * 64 + trc;
  const char* xg = ws + OFF_X1 + grp * XTILE + wave * 7168;
  const u32 xl = lane * 16;
  auto dma_x = [&](int pass, int i) {
    const int half = i >= 7, q = i - 7 * half, o = half * XHALF + (q >> 2) * 4096;
    const char* src = xg + pass * 32 * XTILE + o;
    if ((q & 3) == 0) asm volatile("s_mov_b32 m0, %0\n\ts_nop 0" :: "s"(lds0 + pass * XTILE + wave * 7168 + o) : "memory");
    asm volatile("global_load_lds_dwordx4 %0, %1 offset:%2 sc1" :: "v"(xl), "s"(src), "i"((q & 3) * 1024) : "memory");
  };
  auto accept_x = [&](int pass) {
    const int lx = pass * XTILE + wave * 7168;
    float s, q;
    SPIN {
      u32 m = 0;
      v4u t[7], f[7];
#pragma unroll
      for (int i = 0; i < 7; ++i) t[i] = LDS128(lx + i * 1024 + lane * 16);
#pragma unroll
      for (int i = 0; i < 7; ++i) f[i] = trfrag(lds + lx + XHALF + trb + i * 1024);
#pragma unroll
      for (int i = 0; i < 7; ++i) m = max4(m, t[i]);
      s = 0.f; q = 0.f;
#pragma unroll
      for (int i = 0; i < 7; ++i) sumsq(f[i], s, q);
      if (!__any(m == SENTW || q != q)) break;
      MEMB();
#pragma unroll
      for (int i = 0; i < 7; ++i) m = max4(m, LDS128(lx + XHALF + i * 1024 + lane * 16));
      if (!__any(m == SENTW)) break;
      __builtin_amdgcn_s_sleep(4);
#pragma unroll
      for (int i = 0; i < 14; ++i) dma_x(pass, i);
      VMW(0);
    }
    s = swap32_add(s); q = swap32_add(q);
    if (!h) { v2f o = {s, q}; LDS64F(L_ST + pass * ST_P + lane * 48 + wave * 8) = o; }
  };
  if (wave < 4) {
#pragma unroll
    for (int i = 0; i < 14; ++i) dma_x(0, i);
    VMW(0);
    asm volatile("" : "+v"(gbv));
    accept_x(0);
  } else {
    ldw1(0, 14);
  }
  TS(10);
  BAR();
  TS(11);
  if (wave >= 4) ldw1(14, 28);
  SPIN {
    if (__all(tagok(uv, tagL, tagH) && tagok(gbv, tagL, tagH))) break;
    __builtin_amdgcn_s_sleep(4); MEMB();
    uv = GLD(uvo); gbv = GLD(gbo);
  }
  v4u uvf;
  {
    const float u = __uint_as_float(uv.y), v = __uint_as_float(uv.z), uh = rbf(u), vh = rbf(v);
    uvf = (v4u){h ? 0u : pk(uh, u - uh), h ? 0u : pk(vh, v - vh), 0u, 0u};
  }
  const u32 gb0 = gbv.y, gb1 = gbv.z;

  v4u w2f[28];
  const int w2o = OFF_W2T + (sl * 8 + wave) * 28 * 1024;
  const v16f zero16 = {0, 0, 0, 0, 0, 0, 0, 0, 0, 0, 0, 0, 0, 0, 0, 0};
  auto gemm1 = [&](int pass, auto extra) {
    const int lx = pass * XTILE;
    LAS char* pb = lds + lx + trb;
    const v2u res = trd(lds + lx + XHALF + (64 * sl + 8 * wave + 4 * h + trq) * 64 + trc);
    v4u bf[4];
#pragma unroll
    for (int k = 0; k < 3; ++k) bf[k] = trfrag(pb + k * 1024);
    LDS64(L_RES + pass * RES_P + (2 * wave + h) * 272 + (lane & 31) * 8) = res;
    SB();
    v16f acc = zero16;
#pragma unroll
    for (int k = 0; k < 28; ++k) {
      if (k + 3 < 28) bf[(k + 3) & 3] = trfrag(pb + (k + 3) * 1024);
      acc = mfma32(w1f[k], bf[k & 3], acc);
      extra(k);
      SB();
    }
    return acc;
  };
  auto epi1 = [&](int pass, v16f acc) {
    const v4f a0 = LDS128F(L_ST + pass * ST_P + (lane & 31) * 48 + h * 16);
    const float mean = swap32_add(a0[0] + a0[2]) * (1.f / D);
    const float ex2 = swap32_add(a0[1] + a0[3]) * (1.f / D);
    const float var = rbf(fmaxf(ex2 - mean * mean, 0.f));
    const float sd = rbf(__builtin_amdgcn_sqrtf(rbf(var + 1e-5f)));
    const v4u bs = {h ? 0u : pk(-mean, -mean), h ? 0u : pk(sd, sd), 0u, 0u};
    acc = mfma32(uvf, bs, acc);
    const v2f rl = bc(__builtin_amdgcn_rcpf(sd) * -1.4426950408889634f), sd2 = bc(sd);
    u32 hp[8];
#pragma unroll
    for (int e = 0; e < 8; ++e) {
      const v2f a = {acc[2 * e], acc[2 * e + 1]}, t = a * rl;
      const v2f ex = {__builtin_amdgcn_exp2f(t.x), __builtin_amdgcn_exp2f(t.y)}, dn = fma2(ex, sd2, sd2);
      const v2f rc = {__builtin_amdgcn_rcpf(dn.x), __builtin_amdgcn_rcpf(dn.y)};
      hp[e] = pk(a * rc);
    }
    const int ho = opq(OFF_H + (grp + 32 * pass) * HTILE + (16 * sl + 2 * wave) * 1024 + lane * 16);
    v4u h0 = {hp[0], hp[1], hp[2], hp[3]}, h1 = {hp[4], hp[5], hp[6], hp[7]};
    GST(h0, ho); GST(h1, ho + 1024);
    SB();
  };
  const int l16w = opq(lane * 16);
  auto ldw2 = [&](int i) { w2f[i] = GLDS(l16w + (i & 3) * 1024, w2o + (i >> 2) * 4096); };
  v16f acc;
  if (wave < 4) {
    acc = gemm1(0, [&](int k) { if (!(k & 1)) dma_x(1, k >> 1); });
    TS(12);
    epi1(0, acc);
    VMW(2);
    accept_x(1);
    TS(13);
    BAR();
    acc = gemm1(1, [&](int k) { if (!(k & 1)) ldw2(k >> 1); });
  } else {
    acc = gemm1(0, [&](int k) { if (k >= 14 && !(k & 1)) ldw2((k - 14) >> 1); });
    TS(12);
    BAR();
    epi1(0, acc);
    TS(13);
    acc = gemm1(1, [&](int k) { if (!(k & 3)) ldw2(7 + (k >> 2)); });
  }
  TS(14);
  epi1(1, acc);
#pragma unroll
  for (int i = 14; i < 28; ++i) { ldw2(i); SB(); }
  BAR();
  TS(15);

  const int hoA = OFF_H + grp * HTILE + wave * 14 * 1024;
  auto gemm2 = [&](int ho, int lr) {
    v4u hf[14];
    const int l16 = opq(lane * 16);
#pragma unroll
    for (int i = 0; i < 14; ++i) { hf[i] = GLDS(l16 + (i & 3) * 1024, ho + (i >> 2) * 4096); SB(); }
    __builtin_amdgcn_s_setprio(0);
    v16f c0 = zero16, c1 = zero16;
#pragma unroll
    for (int i = 0; i < 14; ++i) { c0 = mfma32(w2f[2 * i], hf[i], c0); c1 = mfma32(w2f[2 * i + 1], hf[i], c1); SB(); }
    if (__any(c0[0] != c0[0])) {
      _Pragma("unroll 1") for (int i = 0; i < 14; ++i) {
        SPIN {
          MEMB();
          const v4u t = GLDS(lane * 16, ho + i * 1024);
          if (!__any(t.x == SENTW || t.y == SENTW || t.z == SENTW || t.w == SENTW)) break;
          __builtin_amdgcn_s_sleep(4);
        }
      }
      MEMB();
      c0 = zero16; c1 = zero16;
#pragma unroll
      for (int i = 0; i < 14; ++i) {
        const v4u t = GLDS(lane * 16, ho + i * 1024);
        c0 = mfma32(w2f[2 * i], t, c0); c1 = mfma32(w2f[2 * i + 1], t, c1);
        SB();
      }
    }
    const int o = lr + wave * RED_W + h * 528 + (lane & 31) * 16;
#pragma unroll
    for (int q = 0; q < 4; ++q) {
      v4f o0 = {c0[4 * q], c0[4 * q + 1], c0[4 * q + 2], c0[4 * q + 3]}, o1 = {c1[4 * q], c1[4 * q + 1], c1[4 * q + 2], c1[4 * q + 3]};
      LDS128F(o + 2 * q * 528) = o0;
      LDS128F(o + (8 + 2 * q) * 528) = o1;
    }
    SB();
  };
  __builtin_amdgcn_s_setprio(1);
  gemm2(hoA, L_REDA);
  TS(16);
  gemm2(hoA + 32 * HTILE, L_REDB);
  TS(17);
  BAR();
  {
    const int dq = tid & 15, l = tid >> 4;
#pragma unroll
    for (int pass = 0; pass < 2; ++pass) {
      const int lr = pass * L_REDB + dq * 528 + l * 16;
      v4f s = LDS128F(lr);
#pragma unroll
      for (int w = 1; w < 8; ++w) s += LDS128F(lr + w * RED_W);
      const v2u rv = LDS64(L_RES + pass * RES_P + dq * 272 + l * 8);
      v2u o = {pk(fma2(unpk(gb0), s.xy, unpk(rv.x))), pk(fma2(unpk(gb1), s.zw, unpk(rv.y)))};
      *(GAS v2u*)(out + (pass * 1024 + grp * 32 + l) * D + 64 * sl + 4 * dq) = o;
    }
  }
  TS(29); TSEND();
}

extern "C" void kernel_launch(void* const* d_in, const int* in_sizes, int n_in, void* d_out, int out_size, void* d_ws, size_t ws_size,
                              hipStream_t stream) {
  k_all<<<256, 512, 0, stream>>>((const u16*)d_in[0], (const u16*)d_in[1], (const u16*)d_in[2], (const u16*)d_in[3], (const u16*)d_in[4],
                                 (const u16*)d_in[5], (const u16*)d_in[6], (const u16*)d_in[7], (const u16*)d_in[8], (const u16*)d_in[10],
                                 (const u16*)d_in[11], (const u16*)d_in[12], (const u16*)d_in[13], (u16*)d_out, (char*)d_ws);
}
```

```cpp
#include <hip/hip_runtime.h>
#include <stdint.h>

typedef unsigned short u16;
typedef unsigned u32;
typedef unsigned long long u64;
typedef u32 v4u __attribute__((ext_vector_type(4)));
typedef u32 v2u __attribute__((ext_vector_type(2)));
typedef float v4f __attribute__((ext_vector_type(4)));
typedef float v2f __attribute__((ext_vector_type(2)));
typedef float v16f __attribute__((ext_vector_type(16)));
typedef __bf16 bf8 __attribute__((ext_vector_type(8)));
typedef __bf16 bf2 __attribute__((ext_vector_type(2)));
typedef short s4 __attribute__((ext_vector_type(4)));
#define GAS __attribute__((address_space(1)))
#define LAS __attribute__((address_space(3)))
#define DEV __device__ __forceinline__

constexpr int D = 448, F = 1792;

constexpr int OFF_STAT = 4096;
constexpr int OFF_MOD = 20480;
constexpr int OFF_UV = 32768;
constexpr int OFF_CFL = 61440;
constexpr int OFF_WFL = 65536;
constexpr int OFF_XT = 131072;
constexpr int OFF_W1T = 4 << 20;
constexpr int OFF_W2T = 6 << 20;
constexpr int OFF_X1 = 8 << 20;
constexpr int OFF_H = 12 << 20;
constexpr int OFF_DBG = 24 << 20;
constexpr int WS_BYTES = 32 << 20;
constexpr int XTILE = 57344, XHALF = 28672, HTILE = 114688;

constexpr int KG_W = 8704;
constexpr int L_KC = 0, KC_S = 4160, KC_CH = 4 * KC_S;
constexpr int L_DUMP0 = 33280;
constexpr int L_LNT = 69632;
constexpr int L_STX = 76800;
constexpr int L_KLIN = 76864, KLIN_CH = 2576;
constexpr int L_Z = 82048, Z_CH = 4608;
constexpr int L_XT = 91264;
constexpr int L_DUMP1 = 100480;
constexpr int L_XA = 0, L_XB = XTILE;
constexpr int RED_W = 8448, L_REDA = 0, L_REDB = 8 * RED_W;
constexpr int L_RES = 135168, RES_P = 4352;
constexpr int L_ST = 143872, ST_P = 1536;
constexpr int L_PF = 146944;
constexpr int LDS_BYTES = 147200;
constexpr int L_AR0 = 0, L_AR1 = 81920;
constexpr int L_ASC = 57344;
constexpr int L_ARED = 58368;
constexpr int L_AG = 60416;
constexpr int L_ARED2 = 62464;

constexpr u32 SPIN_CAP = 1u << 14;
constexpr u32 SENTW = 0xffbfffbfu;
#define CODE_PF 20480

DEV float blo(u32 v) { return __uint_as_float(v << 16); }
DEV float bhi(u32 v) { return __uint_as_float(v & 0xffff0000u); }
DEV v2f unpk(u32 v) { v2f r = {blo(v), bhi(v)}; return r; }
DEV u32 pk(float a, float b) { bf2 r = {(__bf16)a, (__bf16)b}; return __builtin_bit_cast(u32, r); }
DEV u32 pk(v2f a) { return pk(a.x, a.y); }
DEV float rbf(float a) { return blo(pk(a, a)); }
DEV u32 umax(u32 a, u32 b) { return a > b ? a : b; }
DEV u32 max4(u32 m, v4u v) { return umax(umax(umax(umax(m, v.x), v.y), v.z), v.w); }
DEV v2f fma2(v2f a, v2f b, v2f c) { return __builtin_elementwise_fma(a, b, c); }
DEV v2f bc(float a) { v2f r = {a, a}; return r; }
DEV v4f mfma16(v4u a, v4u b, v4f c) { return __builtin_amdgcn_mfma_f32_16x16x32_bf16(__builtin_bit_cast(bf8, a), __builtin_bit_cast(bf8, b), c, 0, 0, 0); }
DEV v16f mfma32(v4u a, v4u b, v16f c) { return __builtin_amdgcn_mfma_f32_32x32x16_bf16(__builtin_bit_cast(bf8, a), __builtin_bit_cast(bf8, b), c, 0, 0, 0); }
DEV float dot2(u32 a, u32 b, float c) { return __builtin_amdgcn_fdot2_f32_bf16(__builtin_bit_cast(bf2, a), __builtin_bit_cast(bf2, b), c, false); }
DEV void sumsq(v4u f, float& s, float& q) {
  s = dot2(f.x, 0x3f803f80u, s); q = dot2(f.x, f.x, q); s = dot2(f.y, 0x3f803f80u, s); q = dot2(f.y, f.y, q);
  s = dot2(f.z, 0x3f803f80u, s); q = dot2(f.z, f.z, q); s = dot2(f.w, 0x3f803f80u, s); q = dot2(f.w, f.w, q);
}
DEV v2u trd(LAS char* p) { return __builtin_bit_cast(v2u, __builtin_amdgcn_ds_read_tr16_b64_v4i16((LAS s4*)p)); }
DEV v4u trfrag(LAS char* p) { v2u a = trd(p), b = trd(p + 256); v4u r = {a.x, a.y, b.x, b.y}; return r; }
template <int CTRL, int RM> DEV float dppadd(float v) {
  return v + __builtin_bit_cast(float, __builtin_amdgcn_update_dpp(0, __builtin_bit_cast(int, v), CTRL, RM, 0xf, RM == 0xf));
}
DEV float rdl(float v, int l) { return __builtin_bit_cast(float, __builtin_amdgcn_readlane(__builtin_bit_cast(int, v), l)); }
DEV float swap32_add(float v) { auto r = __builtin_amdgcn_permlane32_swap(__float_as_uint(v), __float_as_uint(v), false, false); return __uint_as_float(r[0]) + __uint_as_float(r[1]); }
DEV float swap16_add(float v) { auto r = __builtin_amdgcn_permlane16_swap(__float_as_uint(v), __float_as_uint(v), false, false); return __uint_as_float(r[0]) + __uint_as_float(r[1]); }
DEV int opq(int v) { asm volatile("" : "+v"(v)); return v; }
DEV v2f cmul(v2f a, v2f w, v2f wn) { return fma2(a.yx, wn, a * w.xx); }
DEV v2f cneg(v2f w) { v2f r = {-w.y, w.y}; return r; }
DEV v2f cexpx(float er, float ei, float x) {
  const float e = __builtin_amdgcn_exp2f(er * x), ph = __builtin_amdgcn_fractf(ei * x);
  v2f c = {__builtin_amdgcn_cosf(ph), __builtin_amdgcn_sinf(ph)};
  return c * bc(e);
}

#define BAR() asm volatile("s_waitcnt lgkmcnt(0)\n\ts_barrier" ::: "memory")
#define VMW(n) asm volatile("s_waitcnt vmcnt(" #n ")" ::: "memory")
#define LGK0() __builtin_amdgcn_s_waitcnt(0xC07F)
#define MEMB() asm volatile("" ::: "memory")
#define SB() __builtin_amdgcn_sched_barrier(0)
#define SPIN _Pragma("unroll 1") for (u32 sp = 0; sp < SPIN_CAP; ++sp)
#define LDS128(off) (*(LAS v4u*)(lds + (off)))
#define LDS128F(off) (*(LAS v4f*)(lds + (off)))
#define LDS64(off) (*(LAS v2u*)(lds + (off)))
#define LDS64F(off) (*(LAS v2f*)(lds + (off)))
#define LDS32(off) (*(LAS u32*)(lds + (off)))
#define LDS32F(off) (*(LAS float*)(lds + (off)))
#define GLD(off) __builtin_amdgcn_raw_buffer_load_b128(rs, (off), 0, 16)
#define GLDS(voff, soff) __builtin_amdgcn_raw_buffer_load_b128(rs, (voff), (soff), 16)
#define GST(v, off) __builtin_amdgcn_raw_buffer_store_b128((v), rs, (off), 0, 16)

DEV void dma16(u32 ldsaddr, const char* gbase, u32 voff) {
  asm volatile("s_mov_b32 m0, %0\n\ts_nop 0\n\tglobal_load_lds_dwordx4 %1, %2" :: "s"(ldsaddr), "v"(voff), "s"(gbase) : "memory");
}
DEV void dma16_dev(u32 ldsaddr, const char* gbase, u32 voff) {
  asm volatile("s_mov_b32 m0, %0\n\ts_nop 0\n\tglobal_load_lds_dwordx4 %1, %2 sc1" :: "s"(ldsaddr), "v"(voff), "s"(gbase) : "memory");
}
template <int OFF> DEV void st_tid(u32 v) { asm volatile("ds_write_addtid_b32 %0 offset:%1" :: "v"(v), "i"(OFF) : "memory"); }

#define TS(i)
#define TSEND()

DEV bool tagok(v4u g, u32 tagL, u32 tagH) { return g.x == tagL && g.w == tagH; }

DEV void ln1_publish(LAS char* lds, __amdgpu_buffer_rsrc_t rs, v4u xr, u32 b, int tid, int lane, u32 wave, u32 tagL, u32 tagH) {
  if (lane < 56) {
    const int o = L_LNT + ((wave >> 1) * 224 + 4 * lane) * 8 + (wave & 1) * 4;
    LDS32(o) = xr.x; LDS32(o + 8) = xr.y; LDS32(o + 16) = xr.z; LDS32(o + 24) = xr.w;
  }
  float s = 0.f, q = 0.f;
  sumsq(xr, s, q);
  s = dppadd<0x111, 0xf>(s); q = dppadd<0x111, 0xf>(q); s = dppadd<0x112, 0xf>(s); q = dppadd<0x112, 0xf>(q);
  s = dppadd<0x114, 0xf>(s); q = dppadd<0x114, 0xf>(q); s = dppadd<0x118, 0xf>(s); q = dppadd<0x118, 0xf>(q);
  s = swap32_add(swap16_add(s)); q = swap32_add(swap16_add(q));
  const float mean = s * (1.f / D), var = rbf(fmaxf(q * (1.f / D) - mean * mean, 0.f));
  const float sd = __builtin_amdgcn_sqrtf(rbf(var + 1e-5f));
  if (lane == 15) LDS32(L_STX + wave * 4) = pk(mean, sd);
  BAR();
  const int lo = L_LNT + ((tid & 3) * 224 + (tid >> 2)) * 8, go = OFF_XT + (tid >> 2) * 16384 + (4 * b + (tid & 3)) * 16;
  {
    v2u g = LDS64(lo);
    v4u o = {tagL, g.x, g.y, tagH};
    GST(o, go);
  }
  if (wave < 6) {
    v2u g = LDS64(lo + 1024);
    v4u o = {tagL, g.x, g.y, tagH};
    GST(o, go + 128 * 16384);
  } else if (wave == 7 && lane < 4) {
    v2u g = LDS64(L_STX + lane * 8);
    v4u o = {tagL, g.x, g.y, tagH};
    GST(o, OFF_STAT + (4 * b + lane) * 16);
  }
}

DEV void conv_epi(LAS char* lds, __amdgpu_buffer_rsrc_t rs, int dump, int lz, int goff, float ga, float gd, float g2) {
  const v4f p0 = LDS128F(dump), p1 = LDS128F(dump + 4096);
  const v2u hh = LDS64(L_Z + 480 + lz), xx = LDS64(L_XT + lz);
  const v2f o0 = fma2(bc(ga), p0.xy + p1.xy, fma2(bc(gd), unpk(hh.x), unpk(xx.x)));
  const v2f o1 = fma2(bc(ga), p0.zw + p1.zw, fma2(bc(gd), unpk(hh.y), unpk(xx.y)));
  const u32 X0 = pk(o0), X1 = pk(o1);
  const u32 S0 = pk(unpk(X0) * bc(g2)), S1 = pk(unpk(X1) * bc(g2));
  auto r0 = __builtin_amdgcn_permlane16_swap(X0, S0, false, false);
  auto r1 = __builtin_amdgcn_permlane16_swap(X1, S1, false, false);
  v4u v = {r0[0], r1[0], r0[1], r1[1]};
  GST(v, goff);
}

__global__ void __launch_bounds__(512, 2) k_all(const u16* __restrict__ x, const u16* __restrict__ Are, const u16* __restrict__ Aim,
    const u16* __restrict__ Bre, const u16* __restrict__ Bim, const u16* __restrict__ Cre, const u16* __restrict__ Cim,
    const u16* __restrict__ logdt, const u16* __restrict__ Dskip, const u16* __restrict__ w1, const u16* __restrict__ w2,
    const u16* __restrict__ cvec, const u16* __restrict__ wmod, u16* __restrict__ out, char* ws) {
  __shared__ __attribute__((aligned(16))) char smem[LDS_BYTES];
  LAS char* lds = (LAS char*)smem;
  const u32 lds0 = (u32)(size_t)lds;
  u64 pc;
  asm volatile("s_getpc_b64 %0" : "=s"(pc));
  asm volatile("" :: "s"(x), "s"(Are), "s"(Aim), "s"(Bre), "s"(Bim), "s"(Cre), "s"(Cim), "s"(logdt), "s"(Dskip), "s"(w1), "s"(w2), "s"(cvec), "s"(wmod), "s"(out), "s"(ws));
  const int tid = threadIdx.x, lane = tid & 63;
  const u32 wave = __builtin_amdgcn_readfirstlane(tid >> 6);
  const u32 b = blockIdx.x, xcd = b & 7, j = b >> 3;
  TS(0);
  const __amdgpu_buffer_rsrc_t rs = __builtin_amdgcn_make_buffer_rsrc(ws, 0, WS_BYTES, 0x00020000);
  const v4u xr = __builtin_amdgcn_raw_buffer_load_b128(__builtin_amdgcn_make_buffer_rsrc((void*)(x + (8 * b + wave) * D), 0, 2 * D, 0x00020000), lane * 16, 0, 0);
  const v2u e0 = __builtin_amdgcn_raw_buffer_load_b64(rs, 0, 0, 0);
  SB();
  const v4u SENT = {SENTW, SENTW, SENTW, SENTW};
  auto gettag = [&](u32& tl, u32& th) {
    const u64 T = (((u64)__builtin_amdgcn_readfirstlane(e0.y) << 32) | __builtin_amdgcn_readfirstlane(e0.x)) + 0x9E3779B97F4A7C15ull;
    tl = (u32)T; th = (u32)(T >> 32);
  };
  u32 tagL, tagH;

  if (j >= 28) {
    const u32 a = (j - 28) * 8 + xcd;
    const int h = lane >> 5, trq = (lane & 15) >> 2, trc = ((lane >> 4) & 1) * 32 + (lane & 3) * 8;
    const u32 wv = wave < 7 ? wave : 0;
    auto stage = [&](u32 ldst, const char* src, u32 rstride) {
      if (wave < 7) {
        const u32 vo = ((lane >> 3) + 64 * wave) * rstride + (lane & 7) * 16;
#pragma unroll
        for (int i = 0; i < 8; ++i) dma16(lds0 + ldst + wave * 8192 + i * 1024, src + i * 8 * rstride, vo);
      }
    };
    auto gemv = [&](u32 lsrc, int atab, float& r0, float& r1) {
      v16f c0 = {0, 0, 0, 0, 0, 0, 0, 0, 0, 0, 0, 0, 0, 0, 0, 0}, c1 = c0;
      LAS char* pb = lds + opq(lsrc + wv * 8192 + (8 * h + trq) * 128 + trc);
#pragma unroll
      for (int k = 0; k < 4; ++k) {
        const v4u af = LDS128(atab + wv * 128 + k * 32 + h * 16);
        const v2u b0 = trd(pb + k * 2048), b1 = trd(pb + k * 2048 + 512), b2 = trd(pb + k * 2048 + 64), b3 = trd(pb + k * 2048 + 576);
        c0 = mfma32(af, (v4u){b0.x, b0.y, b1.x, b1.y}, c0); c1 = mfma32(af, (v4u){b2.x, b2.y, b3.x, b3.y}, c1);
      }
      r0 = h ? c1[0] : c0[0]; r1 = h ? c1[1] : c0[1];
    };
    const u32 cv = cvec[64 * wv + lane];
    u32 lc = a < 21 ? a : (a < 28 ? a + 7 : a - 7);
    stage(L_AR0, (const char*)wmod + lc * 128, 6 * D * 2);
    SB();
    {
      const float cf_ = blo(cv);
      *(LAS u16*)(lds + L_ASC + (64 * wv + lane) * 2) = (u16)pk(cf_ * __builtin_amdgcn_rcpf(1.f + __expf(-cf_)), 0.f);
    }
    gettag(tagL, tagH);
    TS(1);
    ln1_publish(lds, rs, xr, b, tid, lane, wave, tagL, tagH);
    TS(2);
    VMW(1);
    if (a >= 4) stage(L_AR1, (const char*)w1 + (a - 4) * 128, F * 2);
    _Pragma("unroll 1") for (int round = 0;; ++round) {
      float r0, r1;
      gemv(L_AR0, L_ASC, r0, r1);
      LDS32F(L_ARED + wave * 256 + lane * 4) = r0;
      BAR();
      const bool more = round == 0 && a < 10;
      if (more) stage(L_AR0, (const char*)wmod + (a < 3 ? 25 + a : 32 + a) * 128, 6 * D * 2);
      if (tid < 16) {
        v4f s = LDS128F(L_ARED + tid * 16);
#pragma unroll
        for (int w = 1; w < 7; ++w) s += LDS128F(L_ARED + w * 256 + tid * 16);
        v4u o = {tagL, pk(s[0], s[1]), pk(s[2], s[3]), tagH};
        GST(o, OFF_MOD + (16 * lc + tid) * 16);
      }
      if (!more) break;
      lc = a < 3 ? 25 + a : 32 + a;
      VMW(0);
      BAR();
    }
    TS(3);
    const int hc = OFF_H + a * HTILE + tid * 16;
#pragma unroll
    for (int i = 0; i < 8; ++i) GST(SENT, hc + i * 8192);
    SB();
    TS(4);
    if (a >= 4) {
      if (wave < 4) {
        const int gi = 336 + (tid < 224 ? tid : 0);
        v4u g;
        SPIN {
          MEMB();
          g = GLD(OFF_MOD + gi * 16);
          if (__all(tagok(g, tagL, tagH))) break;
          __builtin_amdgcn_s_sleep(8);
        }
        v2u f = {g.y, g.z};
        if (tid >= 112) { f.x = pk(unpk(f.x) + bc(1.f)); f.y = pk(unpk(f.y) + bc(1.f)); }
        if (tid < 224) LDS64(L_AG + tid * 8) = f;
      }
      VMW(0);
      BAR();
      TS(5);
      float r0, r1;
      gemv(L_AR1, L_AG + ((lane & 1) ? 0 : 2 * D), r0, r1);
      { v2f o = {r0, r1}; LDS64F(L_ARED2 + wave * 512 + lane * 8) = o; }
      BAR();
      if (tid < 64) {
        v2f s = LDS64F(L_ARED2 + tid * 8);
#pragma unroll
        for (int w = 1; w < 7; ++w) s += LDS64F(L_ARED2 + w * 512 + tid * 8);
        v4u o = {tagL, __float_as_uint(s[0]), __float_as_uint(s[1]), tagH};
        GST(o, OFF_UV + (64 * (a - 4) + tid) * 16);
      }
    }
    SB();
    TS(6);
#pragma unroll
    for (int i = 8; i < 14; ++i) GST(SENT, hc + i * 8192);
#pragma unroll
    for (int i = 0; i < 14; ++i) GST(SENT, hc + 32 * HTILE + i * 8192);
    VMW(0);
    BAR();
    if (tid == 0) { v4u f = {tagL, 0, 0, tagH}; GST(f, OFF_CFL + a * 128); }
    TS(29); TSEND();
    return;
  }

  const u32 p = xcd * 28 + j;
  const u32 jq = (j * 37) >> 8;
  const u32 grp = xcd * 4 + jq, sl = j - 7 * jq;
  const u32 chw = wave >> 2;

  const u32 pi = ((2 * p + chw) * 256 + (tid & 255)) * 2;
  auto ldp = [&](const u16* q) { return (u32)*(const GAS u16*)((const GAS char*)q + pi); };
  const u32 pAr = ldp(Are), pAi = ldp(Aim), pBr = ldp(Bre), pBi = ldp(Bim), pCr = ldp(Cre), pCi = ldp(Cim);
  const u32 pLd2 = *(const GAS u32*)(logdt + 2 * p), dk2 = *(const GAS u32*)(Dskip + 2 * p);
  SB();
  const bool isw1 = xcd < 4;
  u32 wt_dst = 0;
  if (wave < 7) {
    const char* src; u32 rstride;
    if (isw1) {
      const u32 kap = p >> 2, fg = (p & 3) * 7 + wave;
      src = (const char*)w1 + kap * (16 * F * 2) + fg * 128; rstride = F * 2;
      wt_dst = OFF_W1T + (fg * 56 + kap) * 1024;
    } else {
      const u32 kk = p - 112;
      src = (const char*)w2 + kk * (16 * D * 2) + wave * 128; rstride = D * 2;
      wt_dst = OFF_W2T + (wave * 224 + 2 * kk) * 1024;
    }
    const u32 vo = (lane >> 3) * rstride + (lane & 7) * 16;
    dma16(lds0 + wave * KG_W, src, vo);
    dma16(lds0 + wave * KG_W + 1024, src + 8 * rstride, vo);
  } else if (CODE_PF) {
    u32 vo = (j & 3) * 8192 + lane * 128; vo = vo < CODE_PF ? vo : 0;
    asm volatile("s_mov_b32 m0, %0\n\ts_nop 0\n\tglobal_load_lds_dword %1, %2" :: "s"(lds0 + L_PF), "v"(vo), "s"(pc & ~255ull) : "memory");
  }
  SB();
  {
    const int o = OFF_X1 + (tid >> 3) * XHALF + p * 128 + (tid & 7) * 16;
    GST(SENT, o); GST(SENT, o + 64 * XHALF);
  }
  SB();
  int kap[7];
#pragma unroll
  for (int e = 0; e < 7; ++e) {
    const int l = 4 * tid + e - 3;
    const int kb = (int)(pk((float)l, (float)l) & 0xffffu) - 0x4280;
    kap[e] = opq(L_KLIN + 4 * (l < 128 ? (l < 0 ? 643 : l) : kb));
  }
  int rd_src, rd_dst, rd_dk;
  {
    const int ch = tid >= 200, rem = tid - 200 * ch, jj = rem >> 3, iq = rem & 7;
    int cj, sh, base;
    if (jj < 16) { cj = jj; sh = iq >= 4; base = sh * 128; }
    else if (jj < 20) { cj = 512 + 4 * (jj - 16); sh = 2; base = 256; }
    else { cj = 1024 + 512 * ((jj - 20) >> 1) + 8 * (jj & 1); sh = 3; base = 384; }
    const bool okj = jj < 16 ? (iq < 4 || !(jj & 1)) : jj < 24;
    rd_src = opq(ch * 4 * KG_W + 144 * jj + 16 * iq);
    rd_dst = opq(L_KLIN + ch * KLIN_CH + 4 * ((okj || iq == 0) ? base + ((64 * iq + cj) >> sh) : 641));
    rd_dk = opq(okj ? 64 >> sh : 0);
    if (jj == 24 && iq == 0) rd_dk = -1;
  }
  SB();
  gettag(tagL, tagH);
  ln1_publish(lds, rs, xr, b, tid, lane, wave, tagL, tagH);
  TS(1);

  const int h = lane >> 5, trq = (lane & 15) >> 2, trc = ((lane >> 4) & 1) * 32 + (lane & 3) * 8;
  v4u wt0, wt1;
  if (wave < 7) {
    VMW(3);
    const int r0 = isw1 ? 8 * h + trq : 4 * h + trq, r1 = isw1 ? 512 : 1024;
    LAS char* pb = lds + wave * KG_W + r0 * 128 + trc;
    v2u a0 = trd(pb), a1 = trd(pb + r1), b0 = trd(pb + 64), b1 = trd(pb + 64 + r1);
    wt0 = (v4u){a0.x, a0.y, a1.x, a1.y}; wt1 = (v4u){b0.x, b0.y, b1.x, b1.y};
  }
  MEMB();

  {
    const float dt = rbf(expf(chw ? bhi(pLd2) : blo(pLd2)));
    const float er = (dt * blo(pAr)) * 1.4426950408889634f, ei = (dt * blo(pAi)) * 0.15915494309189535f;
    const v2f dB = {dt * blo(pBr), dt * blo(pBi)}, Cc = {blo(pCr), blo(pCi)};
    v2f X = cmul(Cc, dB, cneg(dB));
    X.y = -X.y;
    const v2f e1 = cexpx(er, ei, 1.f), e16 = cexpx(er, -ei, 16.f), e512 = cexpx(er, ei, 512.f);
    const v2f e1n = cneg(e1), e16n = cneg(e16), e512n = cneg(e512);
    asm volatile("s_mov_b32 m0, %0\n\ts_nop 0" :: "s"(lds0 + wave * KG_W) : "memory");
    st_tid<0>(0x00003f80u);
    v2f Y = e1, Y4, Y8, Y12;
#define YROW(jj) st_tid<jj * 272>(pk(Y)); if (jj == 4) Y4 = Y; if (jj == 8) Y8 = Y; if (jj == 12) Y12 = Y; if (jj < 15) Y = cmul(Y, e1, e1n);
    YROW(1) YROW(2) YROW(3) YROW(4) YROW(5) YROW(6) YROW(7) YROW(8) YROW(9) YROW(10) YROW(11) YROW(12) YROW(13) YROW(14) YROW(15)
    st_tid<16 * 272>(pk(e512));
    st_tid<17 * 272>(pk(cmul(Y4, e512, e512n)));
    Y = cmul(Y8, e512, e512n);  st_tid<18 * 272>(pk(Y));
    st_tid<19 * 272>(pk(cmul(Y12, e512, e512n)));
    v2f E = cmul(e512, e512, e512n); st_tid<20 * 272>(pk(E));
    Y = cmul(Y, e512, e512n);   st_tid<21 * 272>(pk(Y));
    E = cmul(E, e512, e512n);   st_tid<22 * 272>(pk(E));
    Y = cmul(Y, e512, e512n);   st_tid<23 * 272>(pk(Y));
    E = cmul(E, e512, e512n);   st_tid<24 * 272>(pk(E));
    if (wave < 7) {
      GST(wt0, lane * 16 + wt_dst);
      GST(wt1, lane * 16 + wt_dst + (isw1 ? 28 * 1024 : 1024));
    }
    const int r = lane & 31;
    v4u yf[8];
    LAS char* py = lds + wave * KG_W + (r < 24 ? r : 24) * 272 + 16 * h;
    LAS char* px = lds + wave * KG_W + r * 272 + 16 * h;
#pragma unroll
    for (int m = 0; m < 8; ++m) yf[m] = *(LAS v4u*)(py + 32 * m);
#define XROW(i) st_tid<(i) * 272>(pk(X)); if ((i) < 31) X = cmul(X, e16, e16n);
#define XROW4(i) XROW(i) XROW(i + 1) XROW(i + 2) XROW(i + 3)
    XROW4(0) XROW4(4) XROW4(8) XROW4(12) XROW4(16) XROW4(20) XROW4(24) XROW4(28)
    v16f acc = {0, 0, 0, 0, 0, 0, 0, 0, 0, 0, 0, 0, 0, 0, 0, 0};
    v4u xf[8];
#pragma unroll
    for (int m = 0; m < 8; ++m) xf[m] = *(LAS v4u*)(px + 32 * m);
#pragma unroll
    for (int m = 0; m < 8; ++m) acc = mfma32(xf[m], yf[m], acc);
#pragma unroll
    for (int q = 0; q < 4; ++q) {
      v4f o = {acc[4 * q], acc[4 * q + 1], acc[4 * q + 2], acc[4 * q + 3]};
      LDS128F(wave * KG_W + 144 * r + 32 * q + 16 * h) = o;
    }
  }
  TS(2);
  BAR();
  TS(3);
  if (wave == 7) {
    const v4u z0 = {0, 0, 0, 0};
    if (lane < 60) LDS128(L_Z + (lane >= 30) * (Z_CH - 480) + lane * 16) = z0;
    if (lane < 2) LDS32(L_KLIN + lane * KLIN_CH + 4 * 643) = 0;
  } else if (tid < 400) {
    const v4f s = (LDS128F(rd_src) + LDS128F(rd_src + KG_W)) + (LDS128F(rd_src + 2 * KG_W) + LDS128F(rd_src + 3 * KG_W));
    LDS32F(rd_dst) = s[0];
    if (rd_dk >= 0) { LDS32F(rd_dst + rd_dk) = s[1]; LDS32F(rd_dst + 2 * rd_dk) = s[2]; LDS32F(rd_dst + 3 * rd_dk) = s[3]; }
  }
  VMW(0);
  BAR();
  TS(19);
  if (tid == 448) { v4u f = {tagL, 0, 0, tagH}; GST(f, OFF_WFL + p * 128); }
#pragma unroll
  for (int ch = 0; ch < 2; ++ch) {
    float k[7];
#pragma unroll
    for (int e = 0; e < 7; ++e) k[e] = LDS32F(kap[e] + ch * KLIN_CH);
    const u32 d10 = pk(k[1], k[0]), d21 = pk(k[2], k[1]), d32 = pk(k[3], k[2]), d43 = pk(k[4], k[3]), d54 = pk(k[5], k[4]), d65 = pk(k[6], k[5]);
    const int o = L_KC + ch * KC_CH + (511 - tid) * 8;
    v2u c0 = {d32, d10}, c1 = {d43, d21}, c2 = {d54, d32}, c3 = {d65, d43};
    LDS64(o) = c0; LDS64(o + KC_S) = c1; LDS64(o + 2 * KC_S) = c2; LDS64(o + 3 * KC_S) = c3;
  }
  if (tid < 32) { const v2u z0 = {0, 0}; LDS64(L_KC + (tid >> 2) * KC_S + 4096 + (tid & 3) * 8) = z0; }
  TS(4);

  float ga_[2], gd_[2], g2_[2];
  {
    v4u gx0, gx1, gs0, gs1, gm;
    const int l5 = lane & 31;
    const int mo = l5 < 4 ? OFF_MOD + ((l5 == 3 ? 4 : l5) * 112 + (p >> 1)) * 16 : OFF_WFL + (wave * 28 + l5 - 4) * 128;
    SPIN {
      MEMB();
      gx0 = GLDS(tid * 16, OFF_XT + p * 16384); gx1 = GLDS(tid * 16, OFF_XT + p * 16384 + 8192);
      gs0 = GLDS(tid * 16, OFF_STAT); gs1 = GLDS(tid * 16, OFF_STAT + 8192);
      gm = GLD(mo);
      const u32 bad = ((gx0.x ^ tagL) | (gx1.x ^ tagL) | (gs0.x ^ tagL)) | ((gs1.x ^ tagL) | (gm.x ^ tagL) | (gx0.w ^ tagH)) |
                      ((gx1.w ^ tagH) | (gs0.w ^ tagH) | (gs1.w ^ tagH)) | (gm.w ^ tagH);
      if (!__any(bad != 0)) break;
      __builtin_amdgcn_s_sleep(4);
    }
    const int mv = (p & 1) ? gm.z : gm.y;
    const u32 sa2 = __builtin_amdgcn_readlane(mv, 0), ca2 = __builtin_amdgcn_readlane(mv, 1), ga2 = __builtin_amdgcn_readlane(mv, 2), cb2 = __builtin_amdgcn_readlane(mv, 3);
    const v2f sa = unpk(sa2), g1 = unpk(pk(unpk(ca2) + bc(1.f))), g2 = unpk(pk(unpk(cb2) + bc(1.f))), ga = unpk(ga2), gd = ga * unpk(dk2);
    ga_[0] = ga.x; ga_[1] = ga.y; gd_[0] = gd.x; gd_[1] = gd.y; g2_[0] = g2.x; g2_[1] = g2.y;
#pragma unroll
    for (int q = 0; q < 2; ++q) {
      const v4u gx = q ? gx1 : gx0, gs = q ? gs1 : gs0;
      const v2f h0 = fma2((unpk(gx.y) - bc(blo(gs.y))) * bc(__builtin_amdgcn_rcpf(bhi(gs.y))), g1, sa);
      const v2f h1 = fma2((unpk(gx.z) - bc(blo(gs.z))) * bc(__builtin_amdgcn_rcpf(bhi(gs.z))), g1, sa);
      const int lo = (q * 512 + tid) * 4;
      LDS32(L_Z + 480 + lo) = pk(h0.x, h1.x);
      LDS32(L_Z + Z_CH + 480 + lo) = pk(h0.y, h1.y);
      LDS32(L_XT + lo) = __builtin_amdgcn_perm(gx.z, gx.y, 0x05040100u);
      LDS32(L_XT + Z_CH + lo) = __builtin_amdgcn_perm(gx.z, gx.y, 0x07060302u);
    }
  }
  BAR();
  TS(5);

  v4u w1f[28];
  const int w1o = OFF_W1T + (sl * 8 + wave) * 28 * 1024;
  const int c16 = lane & 15, g4 = lane >> 4;
  const int epi_l = 32 * c16 + 8 * g4;
  const int epi_g = OFF_X1 + (c16 >> 1) * XTILE + ((g4 & 1) ? 0 : XHALF) + p * 128 + (c16 & 1) * 32 + (g4 >> 1) * 16;
  auto ldw1 = [&](int i0, int i1) {
    const int l16 = opq(lane * 16);
#pragma unroll
    for (int i = i0; i < i1; ++i) { w1f[i] = GLDS(l16 + (i & 3) * 1024, w1o + (i >> 2) * 4096); SB(); }
  };
  if (wave >= 4) {
    const int ch = (wave >> 1) & 1, kh = wave & 1;
    LAS char* pa = lds + opq(L_KC + ch * KC_CH + (c16 & 3) * KC_S + 4104 - 8 * (c16 >> 2) - 16 * g4 - 256 * kh - 64 * 59);
    LAS char* pbz = lds + opq(L_Z + ch * Z_CH + 496 + 32 * c16 - 16 * g4 - 256 * kh - 192);
    v4u A[2][8], B[2][8];
    auto rd = [&](int s, int rho, int na, int n) {
      if (n >= 3 * na) return;
      const int i = n / 3, m = n % 3;
      if (m == 0) B[s][na - 1 - i] = *(LAS v4u*)(pbz + 64 * (8 * (na - 1 - i) + 3 - rho));
      else { const v2u u = *(LAS v2u*)(pa + 64 * (59 - 8 * i - rho) + 8 * (m - 1)); if (m == 1) { A[s][i].x = u.x; A[s][i].y = u.y; } else { A[s][i].z = u.x; A[s][i].w = u.y; } }
      SB();
    };
    v4f acc[4];
    __builtin_amdgcn_s_setprio(1);
#pragma unroll
    for (int n = 0; n < 12; ++n) rd(0, 0, 4, n);
#pragma unroll
    for (int t = 0; t < 4; ++t) acc[t] = (v4f){0, 0, 0, 0};
#pragma unroll
    for (int rho = 0; rho < 4; ++rho) {
      LGK0(); SB();
      int n = 0;
#pragma unroll
      for (int a = 0; a < 4; ++a) {
#pragma unroll
        for (int dl = 3 - a; dl >= 0; --dl) {
          acc[a + dl] = mfma16(A[rho & 1][a], B[rho & 1][dl], acc[a + dl]);
          if (rho < 3) { rd((rho + 1) & 1, rho + 1, 4, n); rd((rho + 1) & 1, rho + 1, 4, n + 1); }
          else { rd(0, 0, 8, n); rd(0, 0, 8, n + 1); }
          n += 2; SB();
        }
      }
    }
#pragma unroll
    for (int n = 20; n < 24; ++n) rd(0, 0, 8, n);
#pragma unroll
    for (int t = 0; t < 4; ++t) LDS128F(L_DUMP0 + ((wave - 4) * 4 + t) * 1024 + lane * 16) = acc[t];
    TS(20);
    BAR();
    TS(6);
#pragma unroll
    for (int t = 0; t < 4; ++t) acc[t] = (v4f){0, 0, 0, 0};
#pragma unroll
    for (int rho = 0; rho < 4; ++rho) {
      LGK0(); SB();
      int n = 0, cnt = 0;
#pragma unroll
      for (int a = 0; a < 8; ++a) {
#pragma unroll
        for (int dl = 7 - a; dl >= 0 && dl >= 4 - a; --dl) {
          acc[a + dl - 4] = mfma16(A[rho & 1][a], B[rho & 1][dl], acc[a + dl - 4]);
          if (rho < 3) { rd((rho + 1) & 1, rho + 1, 8, n++); if (cnt < 6) rd((rho + 1) & 1, rho + 1, 8, n++); }
          ++cnt; SB();
        }
      }
    }
#pragma unroll
    for (int t = 0; t < 4; ++t) LDS128F(L_DUMP1 + ((wave - 4) * 4 + t) * 1024 + lane * 16) = acc[t];
    __builtin_amdgcn_s_setprio(0);
    TS(21);
    BAR();
  } else {
    if (b == 0 && tid == 0) { v2u e = {tagL, tagH}; __builtin_amdgcn_raw_buffer_store_b64(e, rs, 0, 0, 16); }
    ldw1(0, 8);
    TS(20);
    BAR();
    TS(6);
    const int ch = wave >> 1, t0 = (wave & 1) * 2;
    const float ga = ch ? ga_[1] : ga_[0], gd = ch ? gd_[1] : gd_[0], g2 = ch ? g2_[1] : g2_[0];
    const int dm = L_DUMP0 + (ch * 8 + t0) * 1024 + lane * 16, lz = ch * Z_CH + t0 * 512 + epi_l, go = epi_g + ch * 64 + t0 * 8 * XTILE;
    conv_epi(lds, rs, dm, lz, go, ga, gd, g2);
    conv_epi(lds, rs, dm + 1024, lz + 512, go + 8 * XTILE, ga, gd, g2);
    SB();
    TS(7);
    ldw1(8, 28);
    TS(21);
    BAR();
  }
  TS(8);
  {
    const int t = wave & 3;
    conv_epi(lds, rs, L_DUMP1 + (chw * 8 + t) * 1024 + lane * 16, chw * Z_CH + (4 + t) * 512 + epi_l, epi_g + chw * 64 + (4 + t) * 8 * XTILE,
             chw ? ga_[1] : ga_[0], chw ? gd_[1] : gd_[0], chw ? g2_[1] : g2_[0]);
  }
  SB();
  TS(9);

  const int uvo = h ? OFF_CFL + grp * 128 : OFF_UV + (256 * sl + 32 * wave + lane) * 16;
  const int gbo = OFF_MOD + (560 + 16 * sl + (tid & 15)) * 16;
  v4u uv = GLD(uvo), gbv = GLD(gbo);
  SB();
  const int trb = (8 * h + trq) * 64 + trc;
  const char* xg = ws + OFF_X1 + grp * XTILE + wave * 7168;
  const u32 xl = lane * 16;
  auto dma_x = [&](int pass, int i) {
    const int half = i >= 7, q = i - 7 * half, o = half * XHALF + (q >> 2) * 4096;
    const char* src = xg + pass * 32 * XTILE + o;
    if ((q & 3) == 0) asm volatile("s_mov_b32 m0, %0\n\ts_nop 0" :: "s"(lds0 + pass * XTILE + wave * 7168 + o) : "memory");
    asm volatile("global_load_lds_dwordx4 %0, %1 offset:%2 sc1" :: "v"(xl), "s"(src), "i"((q & 3) * 1024) : "memory");
  };
  auto accept_x = [&](int pass) {
    const int lx = pass * XTILE + wave * 7168;
    float s, q;
    SPIN {
      u32 m = 0;
      v4u t[7], f[7];
#pragma unroll
      for (int i = 0; i < 7; ++i) t[i] = LDS128(lx + i * 1024 + lane * 16);
#pragma unroll
      for (int i = 0; i < 7; ++i) f[i] = trfrag(lds + lx + XHALF + trb + i * 1024);
#pragma unroll
      for (int i = 0; i < 7; ++i) m = max4(m, t[i]);
      s = 0.f; q = 0.f;
#pragma unroll
      for (int i = 0; i < 7; ++i) sumsq(f[i], s, q);
      if (!__any(m == SENTW || q != q)) break;
      MEMB();
#pragma unroll
      for (int i = 0; i < 7; ++i) m = max4(m, LDS128(lx + XHALF + i * 1024 + lane * 16));
      if (!__any(m == SENTW)) break;
      __builtin_amdgcn_s_sleep(4);
#pragma unroll
      for (int i = 0; i < 14; ++i) dma_x(pass, i);
      VMW(0);
    }
    s = swap32_add(s); q = swap32_add(q);
    if (!h) { v2f o = {s, q}; LDS64F(L_ST + pass * ST_P + lane * 48 + wave * 8) = o; }
  };
  if (wave < 4) {
#pragma unroll
    for (int i = 0; i < 14; ++i) dma_x(0, i);
    VMW(0);
    asm volatile("" : "+v"(gbv));
    accept_x(0);
  } else {
    ldw1(0, 14);
  }
  TS(10);
  BAR();
  TS(11);
  if (wave >= 4) ldw1(14, 28);
  SPIN {
    if (__all(tagok(uv, tagL, tagH) && tagok(gbv, tagL, tagH))) break;
    __builtin_amdgcn_s_sleep(4); MEMB();
    uv = GLD(uvo); gbv = GLD(gbo);
  }
  v4u uvf;
  {
    const float u = __uint_as_float(uv.y), v = __uint_as_float(uv.z), uh = rbf(u), vh = rbf(v);
    uvf = (v4u){h ? 0u : pk(uh, u - uh), h ? 0u : pk(vh, v - vh), 0u, 0u};
  }
  const u32 gb0 = gbv.y, gb1 = gbv.z;

  v4u w2f[28];
  const int w2o = OFF_W2T + (sl * 8 + wave) * 28 * 1024;
  const v16f zero16 = {0, 0, 0, 0, 0, 0, 0, 0, 0, 0, 0, 0, 0, 0, 0, 0};
  auto gemm1 = [&](int pass, auto extra) {
    const int lx = pass * XTILE;
    LAS char* pb = lds + lx + trb;
    const v2u res = trd(lds + lx + XHALF + (64 * sl + 8 * wave + 4 * h + trq) * 64 + trc);
    v4u bf[4];
#pragma unroll
    for (int k = 0; k < 3; ++k) bf[k] = trfrag(pb + k * 1024);
    LDS64(L_RES + pass * RES_P + (2 * wave + h) * 272 + (lane & 31) * 8) = res;
    SB();
    v16f acc = zero16;
#pragma unroll
    for (int k = 0; k < 28; ++k) {
      if (k + 3 < 28) bf[(k + 3) & 3] = trfrag(pb + (k + 3) * 1024);
      acc = mfma32(w1f[k], bf[k & 3], acc);
      extra(k);
      SB();
    }
    return acc;
  };
  auto epi1 = [&](int pass, v16f acc) {
    const v4f a0 = LDS128F(L_ST + pass * ST_P + (lane & 31) * 48 + h * 16);
    const float mean = swap32_add(a0[0] + a0[2]) * (1.f / D);
    const float ex2 = swap32_add(a0[1] + a0[3]) * (1.f / D);
    const float var = rbf(fmaxf(ex2 - mean * mean, 0.f));
    const float sd = rbf(__builtin_amdgcn_sqrtf(rbf(var + 1e-5f)));
    const v4u bs = {h ? 0u : pk(-mean, -mean), h ? 0u : pk(sd, sd), 0u, 0u};
    acc = mfma32(uvf, bs, acc);
    const v2f rl = bc(__builtin_amdgcn_rcpf(sd) * -1.4426950408889634f), sd2 = bc(sd);
    u32 hp[8];
#pragma unroll
    for (int e = 0; e < 8; ++e) {
      const v2f a = {acc[2 * e], acc[2 * e + 1]}, t = a * rl;
      const v2f ex = {__builtin_amdgcn_exp2f(t.x), __builtin_amdgcn_exp2f(t.y)}, dn = fma2(ex, sd2, sd2);
      const v2f rc = {__builtin_amdgcn_rcpf(dn.x), __builtin_amdgcn_rcpf(dn.y)};
      hp[e] = pk(a * rc);
    }
    const int ho = opq(OFF_H + (grp + 32 * pass) * HTILE + (16 * sl + 2 * wave) * 1024 + lane * 16);
    v4u h0 = {hp[0], hp[1], hp[2], hp[3]}, h1 = {hp[4], hp[5], hp[6], hp[7]};
    GST(h0, ho); GST(h1, ho + 1024);
    SB();
  };
  const int l16w = opq(lane * 16);
  auto ldw2 = [&](int i) { w2f[i] = GLDS(l16w + (i & 3) * 1024, w2o + (i >> 2) * 4096); };
  v16f acc;
  if (wave < 4) {
    acc = gemm1(0, [&](int k) { if (!(k & 1)) dma_x(1, k >> 1); });
    TS(12);
    epi1(0, acc);
    VMW(2);
    accept_x(1);
    TS(13);
    BAR();
    acc = gemm1(1, [&](int k) { if (!(k & 1)) ldw2(k >> 1); });
  } else {
    acc = gemm1(0, [&](int k) { if (k >= 14 && !(k & 1)) ldw2((k - 14) >> 1); });
    TS(12);
    BAR();
    epi1(0, acc);
    TS(13);
    acc = gemm1(1, [&](int k) { if (!(k & 3)) ldw2(7 + (k >> 2)); });
  }
  TS(14);
  epi1(1, acc);
#pragma unroll
  for (int i = 14; i < 28; ++i) { ldw2(i); SB(); }
  BAR();
  TS(15);

  const int hoA = OFF_H + grp * HTILE + wave * 14 * 1024;
  auto gemm2 = [&](int ho, int lr) {
    v4u hf[14];
    const int l16 = opq(lane * 16);
#pragma unroll
    for (int i = 0; i < 14; ++i) { hf[i] = GLDS(l16 + (i & 3) * 1024, ho + (i >> 2) * 4096); SB(); }
    __builtin_amdgcn_s_setprio(0);
    v16f c0 = zero16, c1 = zero16;
#pragma unroll
    for (int i = 0; i < 14; ++i) { c0 = mfma32(w2f[2 * i], hf[i], c0); c1 = mfma32(w2f[2 * i + 1], hf[i], c1); SB(); }
    if (__any(c0[0] != c0[0])) {
      _Pragma("unroll 1") for (int i = 0; i < 14; ++i) {
        SPIN {
          MEMB();
          const v4u t = GLDS(lane * 16, ho + i * 1024);
          if (!__any(t.x == SENTW || t.y == SENTW || t.z == SENTW || t.w == SENTW)) break;
          __builtin_amdgcn_s_sleep(4);
        }
      }
      MEMB();
      c0 = zero16; c1 = zero16;
#pragma unroll
      for (int i = 0; i < 14; ++i) {
        const v4u t = GLDS(lane * 16, ho + i * 1024);
        c0 = mfma32(w2f[2 * i], t, c0); c1 = mfma32(w2f[2 * i + 1], t, c1);
        SB();
      }
    }
    const int o = lr + wave * RED_W + h * 528 + (lane & 31) * 16;
#pragma unroll
    for (int q = 0; q < 4; ++q) {
      v4f o0 = {c0[4 * q], c0[4 * q + 1], c0[4 * q + 2], c0[4 * q + 3]}, o1 = {c1[4 * q], c1[4 * q + 1], c1[4 * q + 2], c1[4 * q + 3]};
      LDS128F(o + 2 * q * 528) = o0;
      LDS128F(o + (8 + 2 * q) * 528) = o1;
    }
    SB();
  };
  __builtin_amdgcn_s_setprio(1);
  gemm2(hoA, L_REDA);
  TS(16);
  gemm2(hoA + 32 * HTILE, L_REDB);
  TS(17);
  BAR();
  {
    const int dq = tid & 15, l = tid >> 4;
#pragma unroll
    for (int pass = 0; pass < 2; ++pass) {
      const int lr = pass * L_REDB + dq * 528 + l * 16;
      v4f s = LDS128F(lr);
#pragma unroll
      for (int w = 1; w < 8; ++w) s += LDS128F(lr + w * RED_W);
      const v2u rv = LDS64(L_RES + pass * RES_P + dq * 272 + l * 8);
      v2u o = {pk(fma2(unpk(gb0), s.xy, unpk(rv.x))), pk(fma2(unpk(gb1), s.zw, unpk(rv.y)))};
      *(GAS v2u*)(out + (pass * 1024 + grp * 32 + l) * D + 64 * sl + 4 * dq) = o;
    }
  }
  TS(29); TSEND();
}

extern "C" void kernel_launch(void* const* d_in, const int* in_sizes, int n_in, void* d_out, int out_size, void* d_ws, size_t ws_size,
                              hipStream_t stream) {
  k_all<<<256, 512, 0, stream>>>((const u16*)d_in[0], (const u16*)d_in[1], (const u16*)d_in[2], (const u16*)d_in[3], (const u16*)d_in[4],
                                 (const u16*)d_in[5], (const u16*)d_in[6], (const u16*)d_in[7], (const u16*)d_in[8], (const u16*)d_in[10],
                                 (const u16*)d_in[11], (const u16*)d_in[12], (const u16*)d_in[13], (u16*)d_out, (char*)d_ws);
}
```

```cpp
#include <hip/hip_runtime.h>
#include <stdint.h>

typedef unsigned short u16;
typedef unsigned u32;
typedef unsigned long long u64;
typedef u32 v4u __attribute__((ext_vector_type(4)));
typedef u32 v2u __attribute__((ext_vector_type(2)));
typedef float v4f __attribute__((ext_vector_type(4)));
typedef float v2f __attribute__((ext_vector_type(2)));
typedef float v16f __attribute__((ext_vector_type(16)));
typedef __bf16 bf8 __attribute__((ext_vector_type(8)));
typedef __bf16 bf2 __attribute__((ext_vector_type(2)));
typedef short s4 __attribute__((ext_vector_type(4)));
#define GAS __attribute__((address_space(1)))
#define LAS __attribute__((address_space(3)))
#define DEV __device__ __forceinline__

constexpr int D = 448, F = 1792;

constexpr int OFF_STAT = 4096;
constexpr int OFF_MOD = 20480;
constexpr int OFF_UV = 32768;
constexpr int OFF_CFL = 61440;
constexpr int OFF_WFL = 65536;
constexpr int OFF_XT = 131072;
constexpr int OFF_W1T = 4 << 20;
constexpr int OFF_W2T = 6 << 20;
constexpr int OFF_X1 = 8 << 20;
constexpr int OFF_H = 12 << 20;
constexpr int OFF_DBG = 24 << 20;
constexpr int WS_BYTES = 32 << 20;
constexpr int XTILE = 57344, XHALF = 28672, HTILE = 114688;

constexpr int KG_W = 8704;
constexpr int L_KC = 0, KC_S = 4160, KC_CH = 4 * KC_S;
constexpr int L_DUMP0 = 33280;
constexpr int L_LNT = 69632;
constexpr int L_STX = 76800;
constexpr int L_KLIN = 76864, KLIN_CH = 2576;
constexpr int L_Z = 82048, Z_CH = 4608;
constexpr int L_XT = 91264;
constexpr int L_DUMP1 = 100480;
constexpr int L_XA = 0, L_XB = XTILE;
constexpr int RED_W = 8448, L_REDA = 0, L_REDB = 8 * RED_W;
constexpr int L_RES = 135168, RES_P = 4352;
constexpr int L_ST = 143872, ST_P = 1536;
constexpr int L_PF = 146944;
constexpr int LDS_BYTES = 147200;
constexpr int L_AR0 = 0, L_AR1 = 81920;
constexpr int L_ASC = 57344;
constexpr int L_ARED = 58368;
constexpr int L_AG = 60416;
constexpr int L_ARED2 = 62464;

constexpr u32 SPIN_CAP = 1u << 14;
constexpr u32 SENTW = 0xffbfffbfu;
#define CODE_PF 20480

DEV float blo(u32 v) { return __uint_as_float(v << 16); }
DEV float bhi(u32 v) { return __uint_as_float(v & 0xffff0000u); }
DEV v2f unpk(u32 v) { v2f r = {blo(v), bhi(v)}; return r; }
DEV u32 pk(float a, float b) { bf2 r = {(__bf16)a, (__bf16)b}; return __builtin_bit_cast(u32, r); }
DEV u32 pk(v2f a) { return pk(a.x, a.y); }
DEV float rbf(float a) { return blo(pk(a, a)); }
DEV u32 umax(u32 a, u32 b) { return a > b ? a : b; }
DEV u32 max4(u32 m, v4u v) { return umax(umax(umax(umax(m, v.x), v.y), v.z), v.w); }
DEV v2f fma2(v2f a, v2f b, v2f c) { return __builtin_elementwise_fma(a, b, c); }
DEV v2f bc(float a) { v2f r = {a, a}; return r; }
DEV v4f mfma16(v4u a, v4u b, v4f c) { return __builtin_amdgcn_mfma_f32_16x16x32_bf16(__builtin_bit_cast(bf8, a), __builtin_bit_cast(bf8, b), c, 0, 0, 0); }
DEV v16f mfma32(v4u a, v4u b, v16f c) { return __builtin_amdgcn_mfma_f32_32x32x16_bf16(__builtin_bit_cast(bf8, a), __builtin_bit_cast(bf8, b), c, 0, 0, 0); }
DEV float dot2(u32 a, u32 b, float c) { return __builtin_amdgcn_fdot2_f32_bf16(__builtin_bit_cast(bf2, a), __builtin_bit_cast(bf2, b), c, false); }
DEV void sumsq(v4u f, float& s, float& q) {
  s = dot2(f.x, 0x3f803f80u, s); q = dot2(f.x, f.x, q); s = dot2(f.y, 0x3f803f80u, s); q = dot2(f.y, f.y, q);
  s = dot2(f.z, 0x3f803f80u, s); q = dot2(f.z, f.z, q); s = dot2(f.w, 0x3f803f80u, s); q = dot2(f.w, f.w, q);
}
DEV v2u trd(LAS char* p) { return __builtin_bit_cast(v2u, __builtin_amdgcn_ds_read_tr16_b64_v4i16((LAS s4*)p)); }
DEV v4u trfrag(LAS char* p) { v2u a = trd(p), b = trd(p + 256); v4u r = {a.x, a.y, b.x, b.y}; return r; }
template <int CTRL, int RM> DEV float dppadd(float v) {
  return v + __builtin_bit_cast(float, __builtin_amdgcn_update_dpp(0, __builtin_bit_cast(int, v), CTRL, RM, 0xf, RM == 0xf));
}
DEV float rdl(float v, int l) { return __builtin_bit_cast(float, __builtin_amdgcn_readlane(__builtin_bit_cast(int, v), l)); }
DEV float swap32_add(float v) { auto r = __builtin_amdgcn_permlane32_swap(__float_as_uint(v), __float_as_uint(v), false, false); return __uint_as_float(r[0]) + __uint_as_float(r[1]); }
DEV float swap16_add(float v) { auto r = __builtin_amdgcn_permlane16_swap(__float_as_uint(v), __float_as_uint(v), false, false); return __uint_as_float(r[0]) + __uint_as_float(r[1]); }
DEV int opq(int v) { asm volatile("" : "+v"(v)); return v; }
DEV v2f cmul(v2f a, v2f w, v2f wn) { return fma2(a.yx, wn, a * w.xx); }
DEV v2f cneg(v2f w) { v2f r = {-w.y, w.y}; return r; }
DEV v2f cexpx(float er, float ei, float x) {
  const float e = __builtin_amdgcn_exp2f(er * x), ph = __builtin_amdgcn_fractf(ei * x);
  v2f c = {__builtin_amdgcn_cosf(ph), __builtin_amdgcn_sinf(ph)};
  return c * bc(e);
}

#define BAR() asm volatile("s_waitcnt lgkmcnt(0)\n\ts_barrier" ::: "memory")
#define VMW(n) asm volatile("s_waitcnt vmcnt(" #n ")" ::: "memory")
#define LGK0() __builtin_amdgcn_s_waitcnt(0xC07F)
#define MEMB() asm volatile("" ::: "memory")
#define SB() __builtin_amdgcn_sched_barrier(0)
#define SPIN _Pragma("unroll 1") for (u32 sp = 0; sp < SPIN_CAP; ++sp)
#define LDS128(off) (*(LAS v4u*)(lds + (off)))
#define LDS128F(off) (*(LAS v4f*)(lds + (off)))
#define LDS64(off) (*(LAS v2u*)(lds + (off)))
#define LDS64F(off) (*(LAS v2f*)(lds + (off)))
#define LDS32(off) (*(LAS u32*)(lds + (off)))
#define LDS32F(off) (*(LAS float*)(lds + (off)))
#define GLD(off) __builtin_amdgcn_raw_buffer_load_b128(rs, (off), 0, 16)
#define GLDS(voff, soff) __builtin_amdgcn_raw_buffer_load_b128(rs, (voff), (soff), 16)
#define GST(v, off) __builtin_amdgcn_raw_buffer_store_b128((v), rs, (off), 0, 16)

DEV void dma16(u32 ldsaddr, const char* gbase, u32 voff) {
  asm volatile("s_mov_b32 m0, %0\n\ts_nop 0\n\tglobal_load_lds_dwordx4 %1, %2" :: "s"(ldsaddr), "v"(voff), "s"(gbase) : "memory");
}
DEV void dma16_dev(u32 ldsaddr, const char* gbase, u32 voff) {
  asm volatile("s_mov_b32 m0, %0\n\ts_nop 0\n\tglobal_load_lds_dwordx4 %1, %2 sc1" :: "s"(ldsaddr), "v"(voff), "s"(gbase) : "memory");
}
template <int OFF> DEV void st_tid(u32 v) { asm volatile("ds_write_addtid_b32 %0 offset:%1" :: "v"(v), "i"(OFF) : "memory"); }

#define TS(i)
#define TSEND()

DEV bool tagok(v4u g, u32 tagL, u32 tagH) { return g.x == tagL && g.w == tagH; }

DEV void ln1_publish(LAS char* lds, __amdgpu_buffer_rsrc_t rs, v4u xr, u32 b, int tid, int lane, u32 wave, u32 tagL, u32 tagH) {
  if (lane < 56) {
    const int o = L_LNT + ((wave >> 1) * 224 + 4 * lane) * 8 + (wave & 1) * 4;
    LDS32(o) = xr.x; LDS32(o + 8) = xr.y; LDS32(o + 16) = xr.z; LDS32(o + 24) = xr.w;
  }
  float s = 0.f, q = 0.f;
  sumsq(xr, s, q);
  s = dppadd<0x111, 0xf>(s); q = dppadd<0x111, 0xf>(q); s = dppadd<0x112, 0xf>(s); q = dppadd<0x112, 0xf>(q);
  s = dppadd<0x114, 0xf>(s); q = dppadd<0x114, 0xf>(q); s = dppadd<0x118, 0xf>(s); q = dppadd<0x118, 0xf>(q);
  s = swap32_add(swap16_add(s)); q = swap32_add(swap16_add(q));
  const float mean = s * (1.f / D), var = rbf(fmaxf(q * (1.f / D) - mean * mean, 0.f));
  const float sd = __builtin_amdgcn_sqrtf(rbf(var + 1e-5f));
  if (lane == 15) LDS32(L_STX + wave * 4) = pk(mean, sd);
  BAR();
  const int lo = L_LNT + ((tid & 3) * 224 + (tid >> 2)) * 8, go = OFF_XT + (tid >> 2) * 16384 + (4 * b + (tid & 3)) * 16;
  {
    v2u g = LDS64(lo);
    v4u o = {tagL, g.x, g.y, tagH};
    GST(o, go);
  }
  if (wave < 6) {
    v2u g = LDS64(lo + 1024);
    v4u o = {tagL, g.x, g.y, tagH};
    GST(o, go + 128 * 16384);
  } else if (wave == 7 && lane < 4) {
    v2u g = LDS64(L_STX + lane * 8);
    v4u o = {tagL, g.x, g.y, tagH};
    GST(o, OFF_STAT + (4 * b + lane) * 16);
  }
}

DEV void conv_epi(LAS char* lds, __amdgpu_buffer_rsrc_t rs, int dump, int lz, int goff, float ga, float gd, float g2) {
  const v4f p0 = LDS128F(dump), p1 = LDS128F(dump + 4096);
  const v2u hh = LDS64(L_Z + 480 + lz), xx = LDS64(L_XT + lz);
  const v2f o0 = fma2(bc(ga), p0.xy + p1.xy, fma2(bc(gd), unpk(hh.x), unpk(xx.x)));
  const v2f o1 = fma2(bc(ga), p0.zw + p1.zw, fma2(bc(gd), unpk(hh.y), unpk(xx.y)));
  const u32 X0 = pk(o0), X1 = pk(o1);
  const u32 S0 = pk(unpk(X0) * bc(g2)), S1 = pk(unpk(X1) * bc(g2));
  auto r0 = __builtin_amdgcn_permlane16_swap(X0, S0, false, false);
  auto r1 = __builtin_amdgcn_permlane16_swap(X1, S1, false, false);
  v4u v = {r0[0], r1[0], r0[1], r1[1]};
  GST(v, goff);
}

__global__ void __launch_bounds__(512, 2) k_all(const u16* __restrict__ x, const u16* __restrict__ Are, const u16* __restrict__ Aim,
    const u16* __restrict__ Bre, const u16* __restrict__ Bim, const u16* __restrict__ Cre, const u16* __restrict__ Cim,
    const u16* __restrict__ logdt, const u16* __restrict__ Dskip, const u16* __restrict__ w1, const u16* __restrict__ w2,
    const u16* __restrict__ cvec, const u16* __restrict__ wmod, u16* __restrict__ out, char* ws) {
  __shared__ __attribute__((aligned(16))) char smem[LDS_BYTES];
  LAS char* lds = (LAS char*)smem;
  const u32 lds0 = (u32)(size_t)lds;
  u64 pc;
  asm volatile("s_getpc_b64 %0" : "=s"(pc));
  asm volatile("" :: "s"(x), "s"(Are), "s"(Aim), "s"(Bre), "s"(Bim), "s"(Cre), "s"(Cim), "s"(logdt), "s"(Dskip), "s"(w1), "s"(w2), "s"(cvec), "s"(wmod), "s"(out), "s"(ws));
  const int tid = threadIdx.x, lane = tid & 63;
  const u32 wave = __builtin_amdgcn_readfirstlane(tid >> 6);
  const u32 b = blockIdx.x, xcd = b & 7, j = b >> 3;
  TS(0);
  const __amdgpu_buffer_rsrc_t rs = __builtin_amdgcn_make_buffer_rsrc(ws, 0, WS_BYTES, 0x00020000);
  const v4u xr = __builtin_amdgcn_raw_buffer_load_b128(__builtin_amdgcn_make_buffer_rsrc((void*)(x + (8 * b + wave) * D), 0, 2 * D, 0x00020000), lane * 16, 0, 0);
  const v2u e0 = __builtin_amdgcn_raw_buffer_load_b64(rs, 0, 0, 0);
  SB();
  const v4u SENT = {SENTW, SENTW, SENTW, SENTW};
  auto gettag = [&](u32& tl, u32& th) {
    const u64 T = (((u64)__builtin_amdgcn_readfirstlane(e0.y) << 32) | __builtin_amdgcn_readfirstlane(e0.x)) + 0x9E3779B97F4A7C15ull;
    tl = (u32)T; th = (u32)(T >> 32);
  };
  u32 tagL, tagH;

  if (j >= 28) {
    const u32 a = (j - 28) * 8 + xcd;
    const int h = lane >> 5, trq = (lane & 15) >> 2, trc = ((lane >> 4) & 1) * 32 + (lane & 3) * 8;
    const u32 wv = wave < 7 ? wave : 0;
    auto stage = [&](u32 ldst, const char* src, u32 rstride) {
      if (wave < 7) {
        const u32 vo = ((lane >> 3) + 64 * wave) * rstride + (lane & 7) * 16;
#pragma unroll
        for (int i = 0; i < 8; ++i) dma16(lds0 + ldst + wave * 8192 + i * 1024, src + i * 8 * rstride, vo);
      }
    };
    auto gemv = [&](u32 lsrc, int atab, float& r0, float& r1) {
      v16f c0 = {0, 0, 0, 0, 0, 0, 0, 0, 0, 0, 0, 0, 0, 0, 0, 0}, c1 = c0;
      LAS char* pb = lds + opq(lsrc + wv * 8192 + (8 * h + trq) * 128 + trc);
#pragma unroll
      for (int k = 0; k < 4; ++k) {
        const v4u af = LDS128(atab + wv * 128 + k * 32 + h * 16);
        const v2u b0 = trd(pb + k * 2048), b1 = trd(pb + k * 2048 + 512), b2 = trd(pb + k * 2048 + 64), b3 = trd(pb + k * 2048 + 576);
        c0 = mfma32(af, (v4u){b0.x, b0.y, b1.x, b1.y}, c0); c1 = mfma32(af, (v4u){b2.x, b2.y, b3.x, b3.y}, c1);
      }
      r0 = h ? c1[0] : c0[0]; r1 = h ? c1[1] : c0[1];
    };
    const u32 cv = cvec[64 * wv + lane];
    u32 lc = a < 21 ? a : (a < 28 ? a + 7 : a - 7);
    stage(L_AR0, (const char*)wmod + lc * 128, 6 * D * 2);
    SB();
    {
      const float cf_ = blo(cv);
      *(LAS u16*)(lds + L_ASC + (64 * wv + lane) * 2) = (u16)pk(cf_ * __builtin_amdgcn_rcpf(1.f + __expf(-cf_)), 0.f);
    }
    gettag(tagL, tagH);
    TS(1);
    ln1_publish(lds, rs, xr, b, tid, lane, wave, tagL, tagH);
    TS(2);
    VMW(1);
    if (a >= 4) stage(L_AR1, (const char*)w1 + (a - 4) * 128, F * 2);
    _Pragma("unroll 1") for (int round = 0;; ++round) {
      float r0, r1;
      gemv(L_AR0, L_ASC, r0, r1);
      LDS32F(L_ARED + wave * 256 + lane * 4) = r0;
      BAR();
      const bool more = round == 0 && a < 10;
      if (more) stage(L_AR0, (const char*)wmod + (a < 3 ? 25 + a : 32 + a) * 128, 6 * D * 2);
      if (tid < 16) {
        v4f s = LDS128F(L_ARED + tid * 16);
#pragma unroll
        for (int w = 1; w < 7; ++w) s += LDS128F(L_ARED + w * 256 + tid * 16);
        v4u o = {tagL, pk(s[0], s[1]), pk(s[2], s[3]), tagH};
        GST(o, OFF_MOD + (16 * lc + tid) * 16);
      }
      if (!more) break;
      lc = a < 3 ? 25 + a : 32 + a;
      VMW(0);
      BAR();
    }
    TS(3);
    const int hc = OFF_H + a * HTILE + tid * 16;
#pragma unroll
    for (int i = 0; i < 8; ++i) GST(SENT, hc + i * 8192);
    SB();
    TS(4);
    if (a >= 4) {
      if (wave < 4) {
        const int gi = 336 + (tid < 224 ? tid : 0);
        v4u g;
        SPIN {
          MEMB();
          g = GLD(OFF_MOD + gi * 16);
          if (__all(tagok(g, tagL, tagH))) break;
          __builtin_amdgcn_s_sleep(8);
        }
        v2u f = {g.y, g.z};
        if (tid >= 112) { f.x = pk(unpk(f.x) + bc(1.f)); f.y = pk(unpk(f.y) + bc(1.f)); }
        if (tid < 224) LDS64(L_AG + tid * 8) = f;
      }
      VMW(0);
      BAR();
      TS(5);
      float r0, r1;
      gemv(L_AR1, L_AG + ((lane & 1) ? 0 : 2 * D), r0, r1);
      { v2f o = {r0, r1}; LDS64F(L_ARED2 + wave * 512 + lane * 8) = o; }
      BAR();
      if (tid < 64) {
        v2f s = LDS64F(L_ARED2 + tid * 8);
#pragma unroll
        for (int w = 1; w < 7; ++w) s += LDS64F(L_ARED2 + w * 512 + tid * 8);
        v4u o = {tagL, __float_as_uint(s[0]), __float_as_uint(s[1]), tagH};
        GST(o, OFF_UV + (64 * (a - 4) + tid) * 16);
      }
    }
    SB();
    TS(6);
#pragma unroll
    for (int i = 8; i < 14; ++i) GST(SENT, hc + i * 8192);
#pragma unroll
    for (int i = 0; i < 14; ++i) GST(SENT, hc + 32 * HTILE + i * 8192);
    VMW(0);
    BAR();
    if (tid == 0) { v4u f = {tagL, 0, 0, tagH}; GST(f, OFF_CFL + a * 128); }
    TS(29); TSEND();
    return;
  }

  const u32 p = xcd * 28 + j;
  const u32 jq = (j * 37) >> 8;
  const u32 grp = xcd * 4 + jq, sl = j - 7 * jq;
  const u32 chw = wave >> 2;

  const u32 pi = ((2 * p + chw) * 256 + (tid & 255)) * 2;
  auto ldp = [&](const u16* q) { return (u32)*(const GAS u16*)((const GAS char*)q + pi); };
  const u32 pAr = ldp(Are), pAi = ldp(Aim), pBr = ldp(Bre), pBi = ldp(Bim), pCr = ldp(Cre), pCi = ldp(Cim);
  const u32 pLd2 = *(const GAS u32*)(logdt + 2 * p), dk2 = *(const GAS u32*)(Dskip + 2 * p);
  SB();
  const bool isw1 = xcd < 4;
  u32 wt_dst = 0;
  if (wave < 7) {
    const char* src; u32 rstride;
    if (isw1) {
      const u32 kap = p >> 2, fg = (p & 3) * 7 + wave;
      src = (const char*)w1 + kap * (16 * F * 2) + fg * 128; rstride = F * 2;
      wt_dst = OFF_W1T + (fg * 56 + kap) * 1024;
    } else {
      const u32 kk = p - 112;
      src = (const char*)w2 + kk * (16 * D * 2) + wave * 128; rstride = D * 2;
      wt_dst = OFF_W2T + (wave * 224 + 2 * kk) * 1024;
    }
    const u32 vo = (lane >> 3) * rstride + (lane & 7) * 16;
    dma16(lds0 + wave * KG_W, src, vo);
    dma16(lds0 + wave * KG_W + 1024, src + 8 * rstride, vo);
  } else if (CODE_PF) {
    u32 vo = (j & 3) * 8192 + lane * 128; vo = vo < CODE_PF ? vo : 0;
    asm volatile("s_mov_b32 m0, %0\n\ts_nop 0\n\tglobal_load_lds_dword %1, %2" :: "s"(lds0 + L_PF), "v"(vo), "s"(pc & ~255ull) : "memory");
  }
  SB();
  {
    const int o = OFF_X1 + (tid >> 3) * XHALF + p * 128 + (tid & 7) * 16;
    GST(SENT, o); GST(SENT, o + 64 * XHALF);
  }
  SB();
  int kap[7];
#pragma unroll
  for (int e = 0; e < 7; ++e) {
    const int l = 4 * tid + e - 3;
    const int kb = (int)(pk((float)l, (float)l) & 0xffffu) - 0x4280;
    kap[e] = opq(L_KLIN + 4 * (l < 128 ? (l < 0 ? 643 : l) : kb));
  }
  int rd_src, rd_dst, rd_dk;
  {
    const int ch = tid >= 200, rem = tid - 200 * ch, jj = rem >> 3, iq = rem & 7;
    int cj, sh, base;
    if (jj < 16) { cj = jj; sh = iq >= 4; base = sh * 128; }
    else if (jj < 20) { cj = 512 + 4 * (jj - 16); sh = 2; base = 256; }
    else { cj = 1024 + 512 * ((jj - 20) >> 1) + 8 * (jj & 1); sh = 3; base = 384; }
    const bool okj = jj < 16 ? (iq < 4 || !(jj & 1)) : jj < 24;
    rd_src = opq(ch * 4 * KG_W + 144 * jj + 16 * iq);
    rd_dst = opq(L_KLIN + ch * KLIN_CH + 4 * ((okj || iq == 0) ? base + ((64 * iq + cj) >> sh) : 641));
    rd_dk = opq(okj ? 64 >> sh : 0);
    if (jj == 24 && iq == 0) rd_dk = -1;
  }
  SB();
  gettag(tagL, tagH);
  ln1_publish(lds, rs, xr, b, tid, lane, wave, tagL, tagH);
  TS(1);

  const int h = lane >> 5, trq = (lane & 15) >> 2, trc = ((lane >> 4) & 1) * 32 + (lane & 3) * 8;
  v4u wt0, wt1;
  if (wave < 7) {
    VMW(3);
    const int r0 = isw1 ? 8 * h + trq : 4 * h + trq, r1 = isw1 ? 512 : 1024;
    LAS char* pb = lds + wave * KG_W + r0 * 128 + trc;
    v2u a0 = trd(pb), a1 = trd(pb + r1), b0 = trd(pb + 64), b1 = trd(pb + 64 + r1);
    wt0 = (v4u){a0.x, a0.y, a1.x, a1.y}; wt1 = (v4u){b0.x, b0.y, b1.x, b1.y};
  }
  MEMB();

  {
    const float dt = rbf(expf(chw ? bhi(pLd2) : blo(pLd2)));
    const float er = (dt * blo(pAr)) * 1.4426950408889634f, ei = (dt * blo(pAi)) * 0.15915494309189535f;
    const v2f dB = {dt * blo(pBr), dt * blo(pBi)}, Cc = {blo(pCr), blo(pCi)};
    v2f X = cmul(Cc, dB, cneg(dB));
    X.y = -X.y;
    const v2f e1 = cexpx(er, ei, 1.f), e16 = cexpx(er, -ei, 16.f), e512 = cexpx(er, ei, 512.f);
    const v2f e1n = cneg(e1), e16n = cneg(e16), e512n = cneg(e512);
    asm volatile("s_mov_b32 m0, %0\n\ts_nop 0" :: "s"(lds0 + wave * KG_W) : "memory");
    st_tid<0>(0x00003f80u);
    v2f Y = e1, Y4, Y8, Y12;
#define YROW(jj) st_tid<jj * 272>(pk(Y)); if (jj == 4) Y4 = Y; if (jj == 8) Y8 = Y; if (jj == 12) Y12 = Y; if (jj < 15) Y = cmul(Y, e1, e1n);
    YROW(1) YROW(2) YROW(3) YROW(4) YROW(5) YROW(6) YROW(7) YROW(8) YROW(9) YROW(10) YROW(11) YROW(12) YROW(13) YROW(14) YROW(15)
    st_tid<16 * 272>(pk(e512));
    st_tid<17 * 272>(pk(cmul(Y4, e512, e512n)));
    Y = cmul(Y8, e512, e512n);  st_tid<18 * 272>(pk(Y));
    st_tid<19 * 272>(pk(cmul(Y12, e512, e512n)));
    v2f E = cmul(e512, e512, e512n); st_tid<20 * 272>(pk(E));
    Y = cmul(Y, e512, e512n);   st_tid<21 * 272>(pk(Y));
    E = cmul(E, e512, e512n);   st_tid<22 * 272>(pk(E));
    Y = cmul(Y, e512, e512n);   st_tid<23 * 272>(pk(Y));
    E = cmul(E, e512, e512n);   st_tid<24 * 272>(pk(E));
    if (wave < 7) {
      GST(wt0, lane * 16 + wt_dst);
      GST(wt1, lane * 16 + wt_dst + (isw1 ? 28 * 1024 : 1024));
    }
    const int r = lane & 31;
    v4u yf[8];
    LAS char* py = lds + wave * KG_W + (r < 24 ? r : 24) * 272 + 16 * h;
    LAS char* px = lds + wave * KG_W + r * 272 + 16 * h;
#pragma unroll
    for (int m = 0; m < 8; ++m) yf[m] = *(LAS v4u*)(py + 32 * m);
#define XROW(i) st_tid<(i) * 272>(pk(X)); if ((i) < 31) X = cmul(X, e16, e16n);
#define XROW4(i) XROW(i) XROW(i + 1) XROW(i + 2) XROW(i + 3)
    XROW4(0) XROW4(4) XROW4(8) XROW4(12) XROW4(16) XROW4(20) XROW4(24) XROW4(28)
    v16f acc = {0, 0, 0, 0, 0, 0, 0, 0, 0, 0, 0, 0, 0, 0, 0, 0};
    v4u xf[8];
#pragma unroll
    for (int m = 0; m < 8; ++m) xf[m] = *(LAS v4u*)(px + 32 * m);
#pragma unroll
    for (int m = 0; m < 8; ++m) acc = mfma32(xf[m], yf[m], acc);
#pragma unroll
    for (int q = 0; q < 4; ++q) {
      v4f o = {acc[4 * q], acc[4 * q + 1], acc[4 * q + 2], acc[4 * q + 3]};
      LDS128F(wave * KG_W + 144 * r + 32 * q + 16 * h) = o;
    }
  }
  TS(2);
  BAR();
  TS(3);
  if (wave == 7) {
    const v4u z0 = {0, 0, 0, 0};
    if (lane < 60) LDS128(L_Z + (lane >= 30) * (Z_CH - 480) + lane * 16) = z0;
    if (lane < 2) LDS32(L_KLIN + lane * KLIN_CH + 4 * 643) = 0;
  } else if (tid < 400) {
    const v4f s = (LDS128F(rd_src) + LDS128F(rd_src + KG_W)) + (LDS128F(rd_src + 2 * KG_W) + LDS128F(rd_src + 3 * KG_W));
    LDS32F(rd_dst) = s[0];
    if (rd_dk >= 0) { LDS32F(rd_dst + rd_dk) = s[1]; LDS32F(rd_dst + 2 * rd_dk) = s[2]; LDS32F(rd_dst + 3 * rd_dk) = s[3]; }
  }
  VMW(0);
  BAR();
  TS(19);
  if (tid == 448) { v4u f = {tagL, 0, 0, tagH};
    GST(f, OFF_WFL + p * 128); }
#pragma unroll
  for (int ch = 0; ch < 2; ++ch) {
    float k[7];
#pragma unroll
    for (int e = 0; e < 7; ++e) k[e] = LDS32F(kap[e] + ch * KLIN_CH);
    const u32 d10 = pk(k[1], k[0]), d21 = pk(k[2], k[1]), d32 = pk(k[3], k[2]), d43 = pk(k[4], k[3]), d54 = pk(k[5], k[4]), d65 = pk(k[6], k[5]);
    const int o = L_KC + ch * KC_CH + (511 - tid) * 8;
    v2u c0 = {d32, d10}, c1 = {d43, d21}, c2 = {d54, d32}, c3 = {d65, d43};
    LDS64(o) = c0; LDS64(o + KC_S) = c1; LDS64(o + 2 * KC_S) = c2; LDS64(o + 3 * KC_S) = c3;
  }
  if (tid < 32) { const v2u z0 = {0, 0}; LDS64(L_KC + (tid >> 2) * KC_S + 4096 + (tid & 3) * 8) = z0; }
  TS(4);

  float ga_[2], gd_[2], g2_[2];
  {
    v4u gx0, gx1, gs0, gs1, gm;
    const int l5 = lane & 31;
    const int mo = l5 < 4 ? OFF_MOD + ((l5 == 3 ? 4 : l5) * 112 + (p >> 1)) * 16 : OFF_WFL + (wave * 28 + l5 - 4) * 128;
    SPIN {
      MEMB();
      gx0 = GLDS(tid * 16, OFF_XT + p * 16384); gx1 = GLDS(tid * 16, OFF_XT + p * 16384 + 8192);
      gs0 = GLDS(tid * 16, OFF_STAT); gs1 = GLDS(tid * 16, OFF_STAT + 8192);
      gm = GLD(mo);
      const u32 bad = ((gx0.x ^ tagL) | (gx1.x ^ tagL) | (gs0.x ^ tagL)) | ((gs1.x ^ tagL) | (gm.x ^ tagL) | (gx0.w ^ tagH)) |
                      ((gx1.w ^ tagH) | (gs0.w ^ tagH) | (gs1.w ^ tagH)) | (gm.w ^ tagH);
      if (!__any(bad != 0)) break;
      __builtin_amdgcn_s_sleep(4);
    }
    const int mv = (p & 1) ? gm.z : gm.y;
    const u32 sa2 = __builtin_amdgcn_readlane(mv, 0), ca2 = __builtin_amdgcn_readlane(mv, 1), ga2 = __builtin_amdgcn_readlane(mv, 2), cb2 = __builtin_amdgcn_readlane(mv, 3);
    const v2f sa = unpk(sa2), g1 = unpk(pk(unpk(ca2) + bc(1.f))), g2 = unpk(pk(unpk(cb2) + bc(1.f))), ga = unpk(ga2), gd = ga * unpk(dk2);
    ga_[0] = ga.x; ga_[1] = ga.y; gd_[0] = gd.x; gd_[1] = gd.y; g2_[0] = g2.x; g2_[1] = g2.y;
#pragma unroll
    for (int q = 0; q < 2; ++q) {
      const v4u gx = q ? gx1 : gx0, gs = q ? gs1 : gs0;
      const v2f h0 = fma2((unpk(gx.y) - bc(blo(gs.y))) * bc(__builtin_amdgcn_rcpf(bhi(gs.y))), g1, sa);
      const v2f h1 = fma2((unpk(gx.z) - bc(blo(gs.z))) * bc(__builtin_amdgcn_rcpf(bhi(gs.z))), g1, sa);
      const int lo = (q * 512 + tid) * 4;
      LDS32(L_Z + 480 + lo) = pk(h0.x, h1.x);
      LDS32(L_Z + Z_CH + 480 + lo) = pk(h0.y, h1.y);
      LDS32(L_XT + lo) = __builtin_amdgcn_perm(gx.z, gx.y, 0x05040100u);
      LDS32(L_XT + Z_CH + lo) = __builtin_amdgcn_perm(gx.z, gx.y, 0x07060302u);
    }
  }
  BAR();
  TS(5);

  v4u w1f[28];
  const int w1o = OFF_W1T + (sl * 8 + wave) * 28 * 1024;
  const int c16 = lane & 15, g4 = lane >> 4;
  const int epi_l = 32 * c16 + 8 * g4;
  const int epi_g = OFF_X1 + (c16 >> 1) * XTILE + ((g4 & 1) ? 0 : XHALF) + p * 128 + (c16 & 1) * 32 + (g4 >> 1) * 16;
  auto ldw1 = [&](int i0, int i1) {
    const int l16 = opq(lane * 16);
#pragma unroll
    for (int i = i0; i < i1; ++i) { w1f[i] = GLDS(l16 + (i & 3) * 1024, w1o + (i >> 2) * 4096); SB(); }
  };
  if (wave >= 4) {
    const int ch = (wave >> 1) & 1, kh = wave & 1;
    LAS char* pa = lds + opq(L_KC + ch * KC_CH + (c16 & 3) * KC_S + 4104 - 8 * (c16 >> 2) - 16 * g4 - 256 * kh - 64 * 59);
    LAS char* pbz = lds + opq(L_Z + ch * Z_CH + 496 + 32 * c16 - 16 * g4 - 256 * kh - 192);
    v4u A[2][8], B[2][8];
    auto rd = [&](int s, int rho, int na, int n) {
      if (n >= 3 * na) return;
      const int i = n / 3, m = n % 3;
      if (m == 0) B[s][na - 1 - i] = *(LAS v4u*)(pbz + 64 * (8 * (na - 1 - i) + 3 - rho));
      else { const v2u u = *(LAS v2u*)(pa + 64 * (59 - 8 * i - rho) + 8 * (m - 1)); if (m == 1) { A[s][i].x = u.x; A[s][i].y = u.y; } else { A[s][i].z = u.x; A[s][i].w = u.y; } }
      SB();
    };
    v4f acc[4];
    __builtin_amdgcn_s_setprio(1);
#pragma unroll
    for (int n = 0; n < 12; ++n) rd(0, 0, 4, n);
#pragma unroll
    for (int t = 0; t < 4; ++t) acc[t] = (v4f){0, 0, 0, 0};
#pragma unroll
    for (int rho = 0; rho < 4; ++rho) {
      LGK0(); SB();
      int n = 0;
#pragma unroll
      for (int a = 0; a < 4; ++a) {
#pragma unroll
        for (int dl = 3 - a; dl >= 0; --dl) {
          acc[a + dl] = mfma16(A[rho & 1][a], B[rho & 1][dl], acc[a + dl]);
          if (rho < 3) { rd((rho + 1) & 1, rho + 1, 4, n); rd((rho + 1) & 1, rho + 1, 4, n + 1); }
          else { rd(0, 0, 8, n); rd(0, 0, 8, n + 1); }
          n += 2; SB();
        }
      }
    }
#pragma unroll
    for (int n = 20; n < 24; ++n) rd(0, 0, 8, n);
#pragma unroll
    for (int t = 0; t < 4; ++t) LDS128F(L_DUMP0 + ((wave - 4) * 4 + t) * 1024 + lane * 16) = acc[t];
    TS(20);
    BAR();
    TS(6);
#pragma unroll
    for (int t = 0; t < 4; ++t) acc[t] = (v4f){0, 0, 0, 0};
#pragma unroll
    for (int rho = 0; rho < 4; ++rho) {
      LGK0(); SB();
      int n = 0, cnt = 0;
#pragma unroll
      for (int a = 0; a < 8; ++a) {
#pragma unroll
        for (int dl = 7 - a; dl >= 0 && dl >= 4 - a; --dl) {
          acc[a + dl - 4] = mfma16(A[rho & 1][a], B[rho & 1][dl], acc[a + dl - 4]);
          if (rho < 3) { rd((rho + 1) & 1, rho + 1, 8, n++); if (cnt < 6) rd((rho + 1) & 1, rho + 1, 8, n++); }
          ++cnt; SB();
        }
      }
    }
#pragma unroll
    for (int t = 0; t < 4; ++t) LDS128F(L_DUMP1 + ((wave - 4) * 4 + t) * 1024 + lane * 16) = acc[t];
    __builtin_amdgcn_s_setprio(0);
    TS(21);
    BAR();
  } else {
    if (b == 0 && tid == 0) { v2u e = {tagL, tagH}; __builtin_amdgcn_raw_buffer_store_b64(e, rs, 0, 0, 16); }
    ldw1(0, 8);
    TS(20);
    BAR();
    TS(6);
    const int ch = wave >> 1, t0 = (wave & 1) * 2;
    const float ga = ch ? ga_[1] : ga_[0], gd = ch ? gd_[1] : gd_[0], g2 = ch ? g2_[1] : g2_[0];
    const int dm = L_DUMP0 + (ch * 8 + t0) * 1024 + lane * 16, lz = ch * Z_CH + t0 * 512 + epi_l, go = epi_g + ch * 64 + t0 * 8 * XTILE;
    conv_epi(lds, rs, dm, lz, go, ga, gd, g2);
    conv_epi(lds, rs, dm + 1024, lz + 512, go + 8 * XTILE, ga, gd, g2);
    SB();
    TS(7);
    ldw1(8, 28);
    TS(21);
    BAR();
  }
  TS(8);
  {
    const int t = wave & 3;
    conv_epi(lds, rs, L_DUMP1 + (chw * 8 + t) * 1024 + lane * 16, chw * Z_CH + (4 + t) * 512 + epi_l, epi_g + chw * 64 + (4 + t) * 8 * XTILE,
             chw ? ga_[1] : ga_[0], chw ? gd_[1] : gd_[0], chw ? g2_[1] : g2_[0]);
  }
  SB();
  TS(9);

  const int uvo = h ? OFF_CFL + grp * 128 : OFF_UV + (256 * sl + 32 * wave + lane) * 16;
  const int gbo = OFF_MOD + (560 + 16 * sl + (tid & 15)) * 16;
  v4u uv = GLD(uvo), gbv = GLD(gbo);
  SB();
  const int trb = (8 * h + trq) * 64 + trc;
  const char* xg = ws + OFF_X1 + grp * XTILE + wave * 7168;
  const u32 xl = lane * 16;
  auto dma_x = [&](int pass, int i) {
    const int half = i >= 7, q = i - 7 * half, o = half * XHALF + (q >> 2) * 4096;
    const char* src = xg + pass * 32 * XTILE + o;
    if ((q & 3) == 0) asm volatile("s_mov_b32 m0, %0\n\ts_nop 0" :: "s"(lds0 + pass * XTILE + wave * 7168 + o) : "memory");
    asm volatile("global_load_lds_dwordx4 %0, %1 offset:%2 sc1" :: "v"(xl), "s"(src), "i"((q & 3) * 1024) : "memory");
  };
  auto accept_x = [&](int pass) {
    const int lx = pass * XTILE + wave * 7168;
    float s, q;
    SPIN {
      u32 m = 0;
      v4u t[7], f[7];
#pragma unroll
      for (int i = 0; i < 7; ++i) t[i] = LDS128(lx + i * 1024 + lane * 16);
#pragma unroll
      for (int i = 0; i < 7; ++i) f[i] = trfrag(lds + lx + XHALF + trb + i * 1024);
#pragma unroll
      for (int i = 0; i < 7; ++i) m = max4(m, t[i]);
      s = 0.f; q = 0.f;
#pragma unroll
      for (int i = 0; i < 7; ++i) sumsq(f[i], s, q);
      if (!__any(m == SENTW || q != q)) break;
      MEMB();
#pragma unroll
      for (int i = 0; i < 7; ++i) m = max4(m, LDS128(lx + XHALF + i * 1024 + lane * 16));
      if (!__any(m == SENTW)) break;
      __builtin_amdgcn_s_sleep(4);
#pragma unroll
      for (int i = 0; i < 14; ++i) dma_x(pass, i);
      VMW(0);
    }
    s = swap32_add(s); q = swap32_add(q);
    if (!h) { v2f o = {s, q}; LDS64F(L_ST + pass * ST_P + lane * 48 + wave * 8) = o; }
  };
  if (wave < 4) {
#pragma unroll
    for (int i = 0; i < 14; ++i) dma_x(0, i);
    VMW(0);
    asm volatile("" : "+v"(gbv));
    accept_x(0);
  } else {
    ldw1(0, 14);
  }
  TS(10);
  BAR();
  TS(11);
  if (wave >= 4) ldw1(14, 28);
  SPIN {
    if (__all(tagok(uv, tagL, tagH) && tagok(gbv, tagL, tagH))) break;
    __builtin_amdgcn_s_sleep(4); MEMB();
    uv = GLD(uvo); gbv = GLD(gbo);
  }
  v4u uvf;
  {
    const float u = __uint_as_float(uv.y), v = __uint_as_float(uv.z), uh = rbf(u), vh = rbf(v);
    uvf = (v4u){h ? 0u : pk(uh, u - uh), h ? 0u : pk(vh, v - vh), 0u, 0u};
  }
  const u32 gb0 = gbv.y, gb1 = gbv.z;

  v4u w2f[28];
  const int w2o = OFF_W2T + (sl * 8 + wave) * 28 * 1024;
  const v16f zero16 = {0, 0, 0, 0, 0, 0, 0, 0, 0, 0, 0, 0, 0, 0, 0, 0};
  auto gemm1 = [&](int pass, auto extra) {
    const int lx = pass * XTILE;
    LAS char* pb = lds + lx + trb;
    const v2u res = trd(lds + lx + XHALF + (64 * sl + 8 * wave + 4 * h + trq) * 64 + trc);
    v4u bf[4];
#pragma unroll
    for (int k = 0; k < 3; ++k) bf[k] = trfrag(pb + k * 1024);
    LDS64(L_RES + pass * RES_P + (2 * wave + h) * 272 + (lane & 31) * 8) = res;
    SB();
    v16f acc = zero16;
#pragma unroll
    for (int k = 0; k < 28; ++k) {
      if (k + 3 < 28) bf[(k + 3) & 3] = trfrag(pb + (k + 3) * 1024);
      acc = mfma32(w1f[k], bf[k & 3], acc);
      extra(k);
      SB();
    }
    return acc;
  };
  auto epi1 = [&](int pass, v16f acc) {
    const v4f a0 = LDS128F(L_ST + pass * ST_P + (lane & 31) * 48 + h * 16);
    const float mean = swap32_add(a0[0] + a0[2]) * (1.f / D);
    const float ex2 = swap32_add(a0[1] + a0[3]) * (1.f / D);
    const float var = rbf(fmaxf(ex2 - mean * mean, 0.f));
    const float sd = rbf(__builtin_amdgcn_sqrtf(rbf(var + 1e-5f)));
    const v4u bs = {h ? 0u : pk(-mean, -mean), h ? 0u : pk(sd, sd), 0u, 0u};
    acc = mfma32(uvf, bs, acc);
    const v2f rl = bc(__builtin_amdgcn_rcpf(sd) * -1.4426950408889634f), sd2 = bc(sd);
    u32 hp[8];
#pragma unroll
    for (int e = 0; e < 8; ++e) {
      const v2f a = {acc[2 * e], acc[2 * e + 1]}, t = a * rl;
      const v2f ex = {__builtin_amdgcn_exp2f(t.x), __builtin_amdgcn_exp2f(t.y)}, dn = fma2(ex, sd2, sd2);
      const v2f rc = {__builtin_amdgcn_rcpf(dn.x), __builtin_amdgcn_rcpf(dn.y)};
      hp[e] = pk(a * rc);
    }
    const int ho = opq(OFF_H + (grp + 32 * pass) * HTILE + (16 * sl + 2 * wave) * 1024 + lane * 16);
    v4u h0 = {hp[0], hp[1], hp[2], hp[3]}, h1 = {hp[4], hp[5], hp[6], hp[7]};
    GST(h0, ho); GST(h1, ho + 1024);
    SB();
  };
  const int l16w = opq(lane * 16);
  auto ldw2 = [&](int i) { w2f[i] = GLDS(l16w + (i & 3) * 1024, w2o + (i >> 2) * 4096); };
  v16f acc;
  if (wave < 4) {
    acc = gemm1(0, [&](int k) { if (!(k & 1)) dma_x(1, k >> 1); });
    TS(12);
    epi1(0, acc);
    VMW(2);
    accept_x(1);
    TS(13);
    BAR();
    acc = gemm1(1, [&](int k) { if (!(k & 1)) ldw2(k >> 1); });
  } else {
    acc = gemm1(0, [&](int k) { if (k >= 14 && !(k & 1)) ldw2((k - 14) >> 1); });
    TS(12);
    BAR();
    epi1(0, acc);
    TS(13);
    acc = gemm1(1, [&](int k) { if (!(k & 3)) ldw2(7 + (k >> 2)); });
  }
  TS(14);
  epi1(1, acc);
#pragma unroll
  for (int i = 14; i < 28; ++i) { ldw2(i); SB(); }
  BAR();
  TS(15);

  const int hoA = OFF_H + grp * HTILE + wave * 14 * 1024;
  auto gemm2 = [&](int ho, int lr) {
    v4u hf[14];
    const int l16 = opq(lane * 16);
#pragma unroll
    for (int i = 0; i < 14; ++i) { hf[i] = GLDS(l16 + (i & 3) * 1024, ho + (i >> 2) * 4096); SB(); }
    __builtin_amdgcn_s_setprio(0);
    v16f c0 = zero16, c1 = zero16;
#pragma unroll
    for (int i = 0; i < 14; ++i) { c0 = mfma32(w2f[2 * i], hf[i], c0); c1 = mfma32(w2f[2 * i + 1], hf[i], c1); SB(); }
    if (__any(c0[0] != c0[0])) {
      _Pragma("unroll 1") for (int i = 0; i < 14; ++i) {
        SPIN {
          MEMB();
          const v4u t = GLDS(lane * 16, ho + i * 1024);
          if (!__any(t.x == SENTW || t.y == SENTW || t.z == SENTW || t.w == SENTW)) break;
          __builtin_amdgcn_s_sleep(4);
        }
      }
      MEMB();
      c0 = zero16; c1 = zero16;
#pragma unroll
      for (int i = 0; i < 14; ++i) {
        const v4u t = GLDS(lane * 16, ho + i * 1024);
        c0 = mfma32(w2f[2 * i], t, c0); c1 = mfma32(w2f[2 * i + 1], t, c1);
        SB();
      }
    }
    const int o = lr + wave * RED_W + h * 528 + (lane & 31) * 16;
#pragma unroll
    for (int q = 0; q < 4; ++q) {
      v4f o0 = {c0[4 * q], c0[4 * q + 1], c0[4 * q + 2], c0[4 * q + 3]}, o1 = {c1[4 * q], c1[4 * q + 1], c1[4 * q + 2], c1[4 * q + 3]};
      LDS128F(o + 2 * q * 528) = o0;
      LDS128F(o + (8 + 2 * q) * 528) = o1;
    }
    SB();
  };
  __builtin_amdgcn_s_setprio(1);
  gemm2(hoA, L_REDA);
  TS(16);
  gemm2(hoA + 32 * HTILE, L_REDB);
  TS(17);
  BAR();
  {
    const int dq = tid & 15, l = tid >> 4;
#pragma unroll
    for (int pass = 0; pass < 2; ++pass) {
      const int lr = pass * L_REDB + dq * 528 + l * 16;
      v4f s = LDS128F(lr);
#pragma unroll
      for (int w = 1; w < 8; ++w) s += LDS128F(lr + w * RED_W);
      const v2u rv = LDS64(L_RES + pass * RES_P + dq * 272 + l * 8);
      v2u o = {pk(fma2(unpk(gb0), s.xy, unpk(rv.x))), pk(fma2(unpk(gb1), s.zw, unpk(rv.y)))};
      *(GAS v2u*)(out + (pass * 1024 + grp * 32 + l) * D + 64 * sl + 4 * dq) = o;
    }
  }
  TS(29); TSEND();
}

__global__ void __launch_bounds__(512) k_post2(char* ws) {
  __shared__ char sm[147200];
  asm volatile("v_mov_b32 v255, 0" ::: "v255");
  sm[threadIdx.x] = 1; if (sm[(threadIdx.x + 1) & 63] == 77) ws[OFF_DBG] = 1;
}
extern "C" void kernel_launch(void* const* d_in, const int* in_sizes, int n_in, void* d_out, int out_size, void* d_ws, size_t ws_size,
                              hipStream_t stream) {
  k_all<<<256, 512, 0, stream>>>((const u16*)d_in[0], (const u16*)d_in[1], (const u16*)d_in[2], (const u16*)d_in[3], (const u16*)d_in[4],
                                 (const u16*)d_in[5], (const u16*)d_in[6], (const u16*)d_in[7], (const u16*)d_in[8], (const u16*)d_in[10],
                                 (const u16*)d_in[11], (const u16*)d_in[12], (const u16*)d_in[13], (u16*)d_out, (char*)d_ws);
  k_post2<<<256, 512, 0, stream>>>((char*)d_ws);
}
```

```cpp
#include <hip/hip_runtime.h>
#include <stdint.h>

typedef unsigned short u16;
typedef unsigned u32;
typedef unsigned long long u64;
typedef u32 v4u __attribute__((ext_vector_type(4)));
typedef u32 v2u __attribute__((ext_vector_type(2)));
typedef float v4f __attribute__((ext_vector_type(4)));
typedef float v2f __attribute__((ext_vector_type(2)));
typedef float v16f __attribute__((ext_vector_type(16)));
typedef __bf16 bf8 __attribute__((ext_vector_type(8)));
typedef __bf16 bf2 __attribute__((ext_vector_type(2)));
typedef short s4 __attribute__((ext_vector_type(4)));
#define GAS __attribute__((address_space(1)))
#define LAS __attribute__((address_space(3)))
#define DEV __device__ __forceinline__

constexpr int D = 448, F = 1792;

constexpr int OFF_STAT = 4096;
constexpr int OFF_MOD = 20480;
constexpr int OFF_UV = 32768;
constexpr int OFF_CFL = 61440;
constexpr int OFF_WFL = 65536;
constexpr int OFF_XT = 131072;
constexpr int OFF_W1T = 4 << 20;
constexpr int OFF_W2T = 6 << 20;
constexpr int OFF_X1 = 8 << 20;
constexpr int OFF_H = 12 << 20;
constexpr int OFF_DBG = 24 << 20;
constexpr int WS_BYTES = 32 << 20;
constexpr int XTILE = 57344, XHALF = 28672, HTILE = 114688;

constexpr int KG_W = 8704;
constexpr int L_KC = 0, KC_S = 4160, KC_CH = 4 * KC_S;
constexpr int L_DUMP0 = 33280;
constexpr int L_LNT = 69632;
constexpr int L_STX = 76800;
constexpr int L_KLIN = 68096, KLIN_CH = 2576;
constexpr int L_Z = 82048, Z_CH = 4608;
constexpr int L_XT = 91264;
constexpr int L_DUMP1 = 100480;
constexpr int L_XA = 0, L_XB = XTILE;
constexpr int RED_W = 8448, L_REDA = 0, L_REDB = 8 * RED_W;
constexpr int L_RES = 135168, RES_P = 4352;
constexpr int L_ST = 143872, ST_P = 1536;
constexpr int L_PF = 146944;
constexpr int LDS_BYTES = 147200;
constexpr int L_AR0 = 0, L_AR1 = 81920;
constexpr int L_ASC = 57344;
constexpr int L_ARED = 58368;
constexpr int L_AG = 60416;
constexpr int L_ARED2 = 62464;

constexpr u32 SPIN_CAP = 1u << 14;
constexpr u32 SENTW = 0xffbfffbfu;
#define CODE_PF $

DEV float blo(u32 v) { return __uint_as_float(v << 16); }
DEV float bhi(u32 v) { return __uint_as_float(v & 0xffff0000u); }
DEV v2f unpk(u32 v) { v2f r = {blo(v), bhi(v)}; return r; }
DEV u32 pk(float a, float b) { bf2 r = {(__bf16)a, (__bf16)b}; return __builtin_bit_cast(u32, r); }
DEV u32 pk(v2f a) { return pk(a.x, a.y); }
DEV float rbf(float a) { return blo(pk(a, a)); }
DEV u32 umax(u32 a, u32 b) { return a > b ? a : b; }
DEV u32 max4(u32 m, v4u v) { return umax(umax(umax(umax(m, v.x), v.y), v.z), v.w); }
DEV v2f fma2(v2f a, v2f b, v2f c) { return __builtin_elementwise_fma(a, b, c); }
DEV v2f bc(float a) { v2f r = {a, a}; return r; }
DEV v4f mfma16(v4u a, v4u b, v4f c) { return __builtin_amdgcn_mfma_f32_16x16x32_bf16(__builtin_bit_cast(bf8, a), __builtin_bit_cast(bf8, b), c, 0, 0, 0); }
DEV v16f mfma32(v4u a, v4u b, v16f c) { return __builtin_amdgcn_mfma_f32_32x32x16_bf16(__builtin_bit_cast(bf8, a), __builtin_bit_cast(bf8, b), c, 0, 0, 0); }
DEV float dot2(u32 a, u32 b, float c) { return __builtin_amdgcn_fdot2_f32_bf16(__builtin_bit_cast(bf2, a), __builtin_bit_cast(bf2, b), c, false); }
DEV void sumsq(v4u f, float& s, float& q) {
  s = dot2(f.x, 0x3f803f80u, s); q = dot2(f.x, f.x, q); s = dot2(f.y, 0x3f803f80u, s); q = dot2(f.y, f.y, q);
  s = dot2(f.z, 0x3f803f80u, s); q = dot2(f.z, f.z, q); s = dot2(f.w, 0x3f803f80u, s); q = dot2(f.w, f.w, q);
}
DEV v2u trd(LAS char* p) { return __builtin_bit_cast(v2u, __builtin_amdgcn_ds_read_tr16_b64_v4i16((LAS s4*)p)); }
DEV v4u trfrag(LAS char* p) { v2u a = trd(p), b = trd(p + 256); v4u r = {a.x, a.y, b.x, b.y}; return r; }
template <int CTRL, int RM> DEV float dppadd(float v) {
  return v + __builtin_bit_cast(float, __builtin_amdgcn_update_dpp(0, __builtin_bit_cast(int, v), CTRL, RM, 0xf, RM == 0xf));
}
DEV float rdl(float v, int l) { return __builtin_bit_cast(float, __builtin_amdgcn_readlane(__builtin_bit_cast(int, v), l)); }
DEV float swap32_add(float v) { auto r = __builtin_amdgcn_permlane32_swap(__float_as_uint(v), __float_as_uint(v), false, false); return __uint_as_float(r[0]) + __uint_as_float(r[1]); }
DEV float swap16_add(float v) { auto r = __builtin_amdgcn_permlane16_swap(__float_as_uint(v), __float_as_uint(v), false, false); return __uint_as_float(r[0]) + __uint_as_float(r[1]); }
DEV int opq(int v) { asm volatile("" : "+v"(v)); return v; }
DEV float opf(float v) { asm("" : "+v"(v)); return v; }
DEV float pin(float v) { asm volatile("" : "+v"(v)); return v; }
DEV u32 pks(float a, float b) { return pk(opf(a), opf(b)); }
DEV v2f cmul(v2f a, v2f w, v2f wn) { return fma2(a.yx, wn, a * w.xx); }
DEV v2f cneg(v2f w) { v2f r = {-w.y, w.y}; return r; }
DEV v2f cexpx(float er, float ei, float x) {
  const float e = __builtin_amdgcn_exp2f(er * x), ph = __builtin_amdgcn_fractf(ei * x);
  v2f c = {__builtin_amdgcn_cosf(ph), __builtin_amdgcn_sinf(ph)};
  return c * bc(e);
}

#define BAR() asm volatile("s_waitcnt lgkmcnt(0)\n\ts_barrier" ::: "memory")
#define VMW(n) asm volatile("s_waitcnt vmcnt(" #n ")" ::: "memory")
#define LGK0() __builtin_amdgcn_s_waitcnt(0xC07F)
#define MEMB() asm volatile("" ::: "memory")
#define SB() __builtin_amdgcn_sched_barrier(0)
#define SPIN _Pragma("unroll 1") for (u32 sp = 0; sp < SPIN_CAP; ++sp)
#define LDS128(off) (*(LAS v4u*)(lds + (off)))
#define LDS128F(off) (*(LAS v4f*)(lds + (off)))
#define LDS64(off) (*(LAS v2u*)(lds + (off)))
#define LDS64F(off) (*(LAS v2f*)(lds + (off)))
#define LDS32(off) (*(LAS u32*)(lds + (off)))
#define LDS32F(off) (*(LAS float*)(lds + (off)))
#define GLD(off) __builtin_amdgcn_raw_buffer_load_b128(rs, (off), 0, 16)
#define GLDS(voff, soff) __builtin_amdgcn_raw_buffer_load_b128(rs, (voff), (soff), 16)
#define GLD8(off) __builtin_amdgcn_raw_buffer_load_b64(rs, (off), 0, 16)
#define GST8(v, off) __builtin_amdgcn_raw_buffer_store_b64((v), rs, (off), 0, 16)
#define GST(v, off) __builtin_amdgcn_raw_buffer_store_b128((v), rs, (off), 0, 16)

DEV void dma16(u32 ldsaddr, const char* gbase, u32 voff) {
  asm volatile("s_mov_b32 m0, %0\n\ts_nop 0\n\tglobal_load_lds_dwordx4 %1, %2" :: "s"(ldsaddr), "v"(voff), "s"(gbase) : "memory");
}
DEV void dma16_dev(u32 ldsaddr, const char* gbase, u32 voff) {
  asm volatile("s_mov_b32 m0, %0\n\ts_nop 0\n\tglobal_load_lds_dwordx4 %1, %2 sc1" :: "s"(ldsaddr), "v"(voff), "s"(gbase) : "memory");
}
DEV void dma4_dev(u32 ldsaddr, const char* gbase, u32 voff) {
  asm volatile("s_mov_b32 m0, %0\n\ts_nop 0\n\tglobal_load_lds_dword %1, %2 sc1" :: "s"(ldsaddr), "v"(voff), "s"(gbase) : "memory");
}
DEV void set_m0(u32 v) { asm volatile("s_mov_b32 m0, %0\n\ts_nop 0" :: "s"(v) : "memory"); }
template <int OFF> DEV void dma16_dev_o(const char* gbase, u32 voff) {
  asm volatile("global_load_lds_dwordx4 %0, %1 offset:%2 sc1" :: "v"(voff), "s"(gbase), "i"(OFF) : "memory");
}
template <class T> DEV void tie(T& v) { asm volatile("" : "+v"(v)); }
template <int OFF> DEV void st_tid(u32 v) { asm volatile("ds_write_addtid_b32 %0 offset:%1" :: "v"(v), "i"(OFF) : "memory"); }

#define TS(i)
#define TSEND()

template <int V> struct Role { static constexpr int value = V; };
DEV bool tagok(v4u g, u32 tagL, u32 tagH) { return g.x == tagL && g.w == tagH; }

#define STV_P
#define STV_A
DEV void ln1_publish(LAS char* lds, __amdgpu_buffer_rsrc_t rs, v4u xr, u32 b, int lane, u32 wave, u32 tagL, u32 tagH STV_P) {
  if (wave < 7) {
    LDS128(L_LNT + wave * 1024 + lane * 16) = xr;
    const int lo = L_LNT + wave * 1024 + (lane & 3) * 256 + (lane >> 2) * 8;
    const v2u ra = LDS64(lo), rb = LDS64(lo + 128);
    float s = 0.f, q = 0.f;
    sumsq(xr, s, q);
    s = dppadd<0xb1, 0xf>(s); q = dppadd<0xb1, 0xf>(q);
    s = dppadd<0x4e, 0xf>(s); q = dppadd<0x4e, 0xf>(q);
    s = dppadd<0x141, 0xf>(s); q = dppadd<0x141, 0xf>(q);
    if ((lane & 7) == 0) { v2f o = {s, q}; LDS64F(L_STX + wave * 64 + lane) = o; }
    const int go = OFF_XT + (32 * wave + 2 * (lane >> 2)) * 16384 + (4 * b + (lane & 3)) * 16;
    const v4u o0 = {tagL, ra.x, rb.x, tagH}, o1 = {tagL, ra.y, rb.y, tagH};
    GST(o0, go); GST(o1, go + 16384);
  }
  BAR();
  if (wave == 7) {
    v2f t = LDS64F(L_STX + lane * 8);
    if (lane >= 56) t = (v2f){0.f, 0.f};
    float s = dppadd<0x128, 0xf>(t.x), q = dppadd<0x128, 0xf>(t.y);
    s = swap32_add(swap16_add(s)); q = swap32_add(swap16_add(q));
    const float mean = s * (1.f / D), var = rbf(fmaxf(q * (1.f / D) - mean * mean, 0.f));
    const u32 ms = pk(mean, __builtin_amdgcn_sqrtf(rbf(var + 1e-5f)));
    const v4u o = {tagL, ms, (u32)__builtin_amdgcn_update_dpp(0, (int)ms, 0xb1, 0xf, 0xf, true), tagH};
    if (lane < 8 && !(lane & 1)) GST(o, OFF_STAT + 64 * b + lane * 8);
  }
}

DEV void conv_epi(LAS char* lds, __amdgpu_buffer_rsrc_t rs, int dump, int lz, int goff, float ga, float gd, float g2) {
  const v4f p0 = LDS128F(dump), p1 = LDS128F(dump + 4096);
  const v2u hh = LDS64(lz + 480), xx = LDS64(lz + (L_XT - L_Z));
  const float h4[4] = {blo(hh.x), bhi(hh.x), blo(hh.y), bhi(hh.y)}, x4[4] = {blo(xx.x), bhi(xx.x), blo(xx.y), bhi(xx.y)};
  float o[4];
#pragma unroll
  for (int e = 0; e < 4; ++e) o[e] = fmaf(ga, opf(p0[e]) + opf(p1[e]), fmaf(gd, h4[e], x4[e]));
  u32 X0 = pks(o[0], o[1]), X1 = pks(o[2], o[3]);
  asm("" : "+v"(X0), "+v"(X1));
  const u32 S0 = pks(blo(X0) * g2, bhi(X0) * g2), S1 = pks(blo(X1) * g2, bhi(X1) * g2);
  auto r0 = __builtin_amdgcn_permlane16_swap(X0, S0, false, false);
  auto r1 = __builtin_amdgcn_permlane16_swap(X1, S1, false, false);
  v4u v = {r0[0], r1[0], r0[1], r1[1]};
  GST(v, goff);
}

__global__ void __launch_bounds__(512, 2) k_all(const u16* __restrict__ x, const u16* __restrict__ Are, const u16* __restrict__ Aim,
    const u16* __restrict__ Bre, const u16* __restrict__ Bim, const u16* __restrict__ Cre, const u16* __restrict__ Cim,
    const u16* __restrict__ logdt, const u16* __restrict__ Dskip, const u16* __restrict__ w1, const u16* __restrict__ w2,
    const u16* __restrict__ cvec, const u16* __restrict__ wmod, u16* __restrict__ out, char* ws) {
  __shared__ __attribute__((aligned(16))) char smem[LDS_BYTES];
  LAS char* lds = (LAS char*)smem;
  const u32 lds0 = (u32)(size_t)lds;
  u64 pc;
  asm volatile("s_getpc_b64 %0" : "=s"(pc));
  asm volatile("" :: "s"(x), "s"(Are), "s"(Aim), "s"(Bre), "s"(Bim), "s"(Cre), "s"(Cim), "s"(logdt), "s"(Dskip), "s"(w1), "s"(w2), "s"(cvec), "s"(wmod), "s"(out), "s"(ws));
  const int tid = threadIdx.x, lane = tid & 63;
  const u32 wave = __builtin_amdgcn_readfirstlane(tid >> 6);
  const u32 b = blockIdx.x, xcd = b & 7, j = b >> 3;
  TS(0);
  const __amdgpu_buffer_rsrc_t rs = __builtin_amdgcn_make_buffer_rsrc(ws, 0, WS_BYTES, 0x00020000);
  auto first_loads = [&](v4u& xr, v2u& e0) {
    const u64 xa = (u64)(x + 8 * b * D);
    const v4u srd = {(u32)xa, (u32)(xa >> 32) & 0xffffu, 16 * D, 0x00020000u};
    const u32 vo = wave < 7 ? (lane >> 3) * (2 * D) + 128 * wave + (lane & 7) * 16 : 16 * D;
    asm volatile("buffer_load_dwordx4 %0, %1, %2, 0 offen ; ALOAD" : "=v"(xr) : "v"(vo), "s"(srd) : "memory");
    asm volatile("global_load_dwordx2 %0, %1, %2 ; ALOAD" : "=v"(e0) : "v"(0), "s"(ws) : "memory");
  };
  const v4u SENT = {SENTW, SENTW, SENTW, SENTW};
  auto gettag = [&](v2u e0, u32& tl, u32& th) {
    const u64 T = (((u64)(u32)__builtin_amdgcn_readfirstlane(e0.y) << 32) | (u32)__builtin_amdgcn_readfirstlane(e0.x)) + 0x9E3779B97F4A7C15ull;
    tl = (u32)T; th = (u32)(T >> 32);
  };
  u32 tagL, tagH;

  if (j >= 28) {
    v4u xr; v2u e0;
    first_loads(xr, e0);
    const u32 a = (j - 28) * 8 + xcd;
    const int h = lane >> 5, trq = (lane & 15) >> 2, trc = ((lane >> 4) & 1) * 32 + (lane & 3) * 8;
    const u32 wv = wave < 7 ? wave : 0;
    auto stage = [&](u32 ldst, const char* src, u32 rstride) {
      if (wave < 7) {
        const u32 vo = ((lane >> 3) + 64 * wave) * rstride + (lane & 7) * 16;
#pragma unroll
        for (int i = 0; i < 8; ++i) dma16(lds0 + ldst + wave * 8192 + i * 1024, src + i * 8 * rstride, vo);
      }
    };
    auto gemv = [&](u32 lsrc, int atab, float& r0, float& r1) {
      v16f c0 = {0, 0, 0, 0, 0, 0, 0, 0, 0, 0, 0, 0, 0, 0, 0, 0}, c1 = c0;
      LAS char* pb = lds + opq(lsrc + wv * 8192 + (8 * h + trq) * 128 + trc);
#pragma unroll
      for (int k = 0; k < 4; ++k) {
        const v4u af = LDS128(atab + wv * 128 + k * 32 + h * 16);
        const v2u b0 = trd(pb + k * 2048), b1 = trd(pb + k * 2048 + 512), b2 = trd(pb + k * 2048 + 64), b3 = trd(pb + k * 2048 + 576);
        c0 = mfma32(af, (v4u){b0.x, b0.y, b1.x, b1.y}, c0); c1 = mfma32(af, (v4u){b2.x, b2.y, b3.x, b3.y}, c1);
      }
      r0 = h ? c1[0] : c0[0]; r1 = h ? c1[1] : c0[1];
    };
    u32 cv;
    asm volatile("global_load_ushort %0, %1, %2 ; ALOAD" : "=v"(cv) : "v"((64 * wv + lane) * 2), "s"(cvec) : "memory");
    u32 lc = a < 21 ? a : (a < 28 ? a + 7 : a - 7);
    stage(L_AR0, (const char*)wmod + lc * 128, 6 * D * 2);
    if (wave == 7) VMW(0);
    asm volatile("s_waitcnt vmcnt(8) ; TIE" : "+v"(xr), "+v"(e0), "+v"(cv) :: "memory");
    {
      const float cf_ = blo(cv);
      *(LAS u16*)(lds + L_ASC + (64 * wv + lane) * 2) = (u16)pk(cf_ * __builtin_amdgcn_rcpf(1.f + __expf(-cf_)), 0.f);
    }
    gettag(e0, tagL, tagH);
    TS(1);
    ln1_publish(lds, rs, xr, b, lane, wave, tagL, tagH STV_A);
    TS(2);
    VMW(2);
    if (a >= 4) stage(L_AR1, (const char*)w1 + (a - 4) * 128, F * 2);
    auto modround = [&](u32 lc_) {
      float r0, r1;
      gemv(L_AR0, L_ASC, r0, r1);
      LDS32F(L_ARED + wave * 256 + lane * 4) = r0;
      BAR();
      if (tid < 16) {
        v4f s = LDS128F(L_ARED + tid * 16);
#pragma unroll
        for (int w = 1; w < 7; ++w) s += LDS128F(L_ARED + w * 256 + tid * 16);
        v4u o = {tagL, pk(s[0], s[1]), pk(s[2], s[3]), tagH};
        GST(o, OFF_MOD + (16 * lc_ + tid) * 16);
      }
    };
    modround(lc);
    TS(3);
    lc = a < 3 ? 25 + a : 32 + a;
    if (a < 10) stage(L_AR0, (const char*)wmod + lc * 128, 6 * D * 2);
    if (a < 3) { VMW(0); BAR(); modround(lc); }
    const int hc = OFF_H + a * HTILE + tid * 16;
#pragma unroll
    for (int i = 0; i < 10; ++i) GST(SENT, hc + i * 8192);
    SB();
    TS(4);
    if (a >= 4) {
      if (wave < 4) {
        const int gi = 336 + (tid < 224 ? tid : 0);
        v4u g;
        SPIN {
          MEMB();
          g = GLD(OFF_MOD + gi * 16);
          if (__all(tagok(g, tagL, tagH))) break;
          __builtin_amdgcn_s_sleep(8);
        }
        v2u f = {g.y, g.z};
        if (tid >= 112) { f.x = pk(unpk(f.x) + bc(1.f)); f.y = pk(unpk(f.y) + bc(1.f)); }
        if (tid < 224) LDS64(L_AG + tid * 8) = f;
      }
      VMW(0);
      BAR();
      TS(5);
      float r0, r1;
      gemv(L_AR1, L_AG + ((lane & 1) ? 0 : 2 * D), r0, r1);
      { v2f o = {r0, r1}; LDS64F(L_ARED2 + wave * 512 + lane * 8) = o; }
      BAR();
      if (tid < 64) {
        v2f s = LDS64F(L_ARED2 + tid * 8);
#pragma unroll
        for (int w = 1; w < 7; ++w) s += LDS64F(L_ARED2 + w * 512 + tid * 8);
        v4u o = {tagL, __float_as_uint(s[0]), __float_as_uint(s[1]), tagH};
        GST(o, OFF_UV + (64 * (a - 4) + tid) * 16);
      }
    }
    SB();
    TS(6);
#pragma unroll
    for (int i = 10; i < 14; ++i) GST(SENT, hc + i * 8192);
#pragma unroll
    for (int i = 0; i < 14; ++i) GST(SENT, hc + 32 * HTILE + i * 8192);
    VMW(0);
    BAR();
    if (tid == 0) { v4u f = {tagL, 0, 0, tagH}; GST(f, OFF_CFL + a * 128); }
    TS(7);
    if (a >= 3 && a < 10) modround(lc);
    TS(29); TSEND();
    return;
  }

  v4u xr; v2u e0;
  first_loads(xr, e0);
  const u32 p = xcd * 28 + j;
  const u32 jq = (j * 37) >> 8;
  const u32 grp = xcd * 4 + jq, sl = j - 7 * jq;
  const u32 chw = wave >> 2;

  const u32 pi = ((2 * p + chw) * 256 + (tid & 255)) * 2;
  auto ldp = [&](const u16* q) { return (u32)*(const GAS u16*)((const GAS char*)q + pi); };
  u32 pAr = ldp(Are), pAi = ldp(Aim), pBr = ldp(Bre), pBi = ldp(Bim), pCr = ldp(Cre), pCi = ldp(Cim);
  u32 pLd2 = *(const GAS u32*)(logdt + 2 * p), dk2 = *(const GAS u32*)(Dskip + 2 * p);
  SB();
  const bool isw1 = xcd < 4;
  u32 wt_dst = 0;
  if (wave < 7) {
    const char* src; u32 rstride;
    if (isw1) {
      const u32 kap = p >> 2, fg = (p & 3) * 7 + wave;
      src = (const char*)w1 + kap * (16 * F * 2) + fg * 128; rstride = F * 2;
      wt_dst = OFF_W1T + (fg * 56 + kap) * 1024;
    } else {
      const u32 kk = p - 112;
      src = (const char*)w2 + kk * (16 * D * 2) + wave * 128; rstride = D * 2;
      wt_dst = OFF_W2T + (wave * 224 + 2 * kk) * 1024;
    }
    const u32 vo = (lane >> 3) * rstride + (lane & 7) * 16;
    dma16(lds0 + wave * KG_W, src, vo);
    dma16(lds0 + wave * KG_W + 1024, src + 8 * rstride, vo);
  } else {
    const u32 a = (lane >> 4) * 8 + xcd, lc = a < 21 ? a : (a < 28 ? a + 7 : a - 7);
    asm volatile("s_mov_b32 m0, %0\n\ts_nop 0\n\tglobal_load_lds_dword %1, %2" :: "s"(lds0 + L_PF), "v"((16 * j + (lane & 15)) * (12 * D) + lc * 128), "s"(wmod) : "memory");
    u32 vo = (j & 7) * 8192 + lane * 128; vo = vo < CODE_PF ? vo : 0;
    asm volatile("s_mov_b32 m0, %0\n\ts_nop 0\n\tglobal_load_lds_dword %1, %2" :: "s"(lds0 + L_PF), "v"(vo), "s"(pc & ~255ull) : "memory");
  }
  SB();
  asm volatile("s_waitcnt vmcnt(0) ; TIE" : "+v"(xr), "+v"(e0), "+v"(pAr), "+v"(pAi), "+v"(pBr), "+v"(pBi), "+v"(pCr), "+v"(pCi), "+v"(pLd2), "+v"(dk2) :: "memory");
  TS(27);
  gettag(e0, tagL, tagH);
  const int h = lane >> 5, trq = (lane & 15) >> 2, trc = ((lane >> 4) & 1) * 32 + (lane & 3) * 8;
  if (wave < 7) {
    const int r0 = isw1 ? 8 * h + trq : 4 * h + trq, r1 = isw1 ? 512 : 1024;
    LAS char* pb = lds + wave * KG_W + r0 * 128 + trc;
    v2u a0 = trd(pb), a1 = trd(pb + r1), b0 = trd(pb + 64), b1 = trd(pb + 64 + r1);
    GST(((v4u){a0.x, a0.y, a1.x, a1.y}), lane * 16 + wt_dst);
    GST(((v4u){b0.x, b0.y, b1.x, b1.y}), lane * 16 + wt_dst + (isw1 ? 28 * 1024 : 1024));
  }
  SB();
  ln1_publish(lds, rs, xr, b, lane, wave, tagL, tagH STV_A);
  TS(1);
  const int l5 = lane & 31;

  {
    const float dt = rbf(expf(chw ? bhi(pLd2) : blo(pLd2)));
    const float er = (dt * blo(pAr)) * 1.4426950408889634f, ei = (dt * blo(pAi)) * 0.15915494309189535f;
    const v2f dB = {dt * blo(pBr), dt * blo(pBi)}, Cc = {blo(pCr), blo(pCi)};
    v2f X = cmul(Cc, dB, cneg(dB));
    X.y = -X.y;
    const v2f e1 = cexpx(er, ei, 1.f), e16 = cexpx(er, -ei, 16.f), e512 = cexpx(er, ei, 512.f);
    const v2f e1n = cneg(e1), e16n = cneg(e16), e512n = cneg(e512);
    set_m0(lds0 + wave * KG_W);
    st_tid<0>(0x00003f80u);
    v2f Y = e1, Y4, Y8, Y12;
#define YROW(jj) st_tid<jj * 272>(pk(Y)); if (jj == 4) Y4 = Y; if (jj == 8) Y8 = Y; if (jj == 12) Y12 = Y; if (jj < 15) Y = cmul(Y, e1, e1n);
    YROW(1) YROW(2) YROW(3) YROW(4) YROW(5) YROW(6) YROW(7) YROW(8) YROW(9) YROW(10) YROW(11) YROW(12) YROW(13) YROW(14) YROW(15)
    st_tid<16 * 272>(pk(e512));
    st_tid<17 * 272>(pk(cmul(Y4, e512, e512n)));
    Y = cmul(Y8, e512, e512n);  st_tid<18 * 272>(pk(Y));
    st_tid<19 * 272>(pk(cmul(Y12, e512, e512n)));
    v2f E = cmul(e512, e512, e512n); st_tid<20 * 272>(pk(E));
    Y = cmul(Y, e512, e512n);   st_tid<21 * 272>(pk(Y));
    E = cmul(E, e512, e512n);   st_tid<22 * 272>(pk(E));
    Y = cmul(Y, e512, e512n);   st_tid<23 * 272>(pk(Y));
    E = cmul(E, e512, e512n);   st_tid<24 * 272>(pk(E));
    const int r = lane & 31;
    v4u yf[8];
    LAS char* py = lds + wave * KG_W + (r < 24 ? r : 24) * 272 + 16 * h;
    LAS char* px = lds + wave * KG_W + r * 272 + 16 * h;
#pragma unroll
    for (int m = 0; m < 8; ++m) yf[m] = *(LAS v4u*)(py + 32 * m);
    TS(25);
    VMW(2);
    asm volatile("s_barrier" ::: "memory");
    if (tid == 448) GST8(((v2u){tagL, tagH}), OFF_WFL + p * 128);
    TS(24);

  {
    const int o = OFF_X1 + (tid >> 3) * XHALF + p * 128 + (tid & 7) * 16;
    GST(SENT, o); GST(SENT, o + 64 * XHALF);
  }
  SB();
#define XROW(i) st_tid<(i) * 272>(pk(X)); if ((i) < 31) X = cmul(X, e16, e16n);
#define XROW4(i) XROW(i) XROW(i + 1) XROW(i + 2) XROW(i + 3)
    XROW4(0) XROW4(4) XROW4(8) XROW4(12) XROW4(16) XROW4(20) XROW4(24) XROW4(28)
    TS(26);
    v16f acc = {0, 0, 0, 0, 0, 0, 0, 0, 0, 0, 0, 0, 0, 0, 0, 0};
    v4u xf[8];
#pragma unroll
    for (int m = 0; m < 8; ++m) xf[m] = *(LAS v4u*)(px + 32 * m);
#pragma unroll
    for (int m = 0; m < 8; ++m) acc = mfma32(xf[m], yf[m], acc);
#pragma unroll
    for (int q = 0; q < 4; ++q) {
      v4f o = {acc[4 * q], acc[4 * q + 1], acc[4 * q + 2], acc[4 * q + 3]};
      LDS128F(wave * KG_W + 144 * r + 32 * q + 16 * h) = o;
    }
  }
  TS(2);
  BAR();
  TS(3);
  auto rest = [&](auto role) {
  constexpr int CV = decltype(role)::value;
  v4u w1f[32];
  const int w1o = OFF_W1T + (sl * 8 + wave) * 28 * 1024;
  auto ldw1 = [&](int i0, int i1) {
    const int l16 = opq(lane * 16);
#pragma unroll
    for (int i = i0; i < i1; ++i) { w1f[i] = GLDS(l16 + (i & 3) * 1024, w1o + (i >> 2) * 4096); SB(); }
  };
  auto l2pf = [&](u32 off) {
    dma4_dev(lds0 + L_PF, ws + off, lane * 128);
  };
  v4u gx0, gx1, gs0, gs1, gm;
  const int mo = OFF_MOD + (((lane & 3) == 3 ? 4 : (lane & 3)) * 112 + (p >> 1)) * 16;
  auto sweep = [&]() {
    gx0 = GLDS(tid * 16, OFF_XT + p * 16384); gx1 = GLDS(tid * 16, OFF_XT + p * 16384 + 8192);
    gs0 = GLDS(tid * 16, OFF_STAT); gs1 = GLDS(tid * 16, OFF_STAT + 8192);
  };
  sweep();
  gm = GLD(mo);
  SB();
  {
    const int jl = lane >> 3, iq = lane & 7, wq = wave & 3;
    const int src = CV * 4 * KG_W + 144 * (8 * wq + (wq == 3 ? 0 : jl)) + 16 * iq;
    const v4f s = (LDS128F(src) + LDS128F(src + KG_W)) + (LDS128F(src + 2 * KG_W) + LDS128F(src + 3 * KG_W));
    const int kl = L_KLIN + CV * KLIN_CH;
    if (wq < 2) {
      const int jj = 8 * wq + jl, hi = iq >> 2;
      const int dst = kl + 512 * hi + ((256 * iq + 4 * jj) >> hi), dk = 64 >> hi;
      if (!(hi & jj)) { LDS32F(dst) = s[0]; LDS32F(dst + dk) = s[1]; LDS32F(dst + 2 * dk) = s[2]; LDS32F(dst + 3 * dk) = s[3]; }
    } else if (wq == 2) {
      const int hi = jl >> 2;
      const int dst = kl + (hi ? 4 * (512 + 8 * iq + 32 * (jl & 2) + (jl & 1)) : 4 * (384 + 16 * iq + jl)), dk = 16 >> hi;
      LDS32F(dst) = s[0]; LDS32F(dst + dk) = s[1]; LDS32F(dst + 2 * dk) = s[2]; LDS32F(dst + 3 * dk) = s[3];
    } else {
      if (lane < 2) LDS32F(kl + 4 * (640 + 3 * lane)) = lane ? 0.f : s[0];
      const v4u z0 = {0, 0, 0, 0};
      if (lane < 30) LDS128(L_Z + CV * Z_CH + lane * 16) = z0;
    }
  }
  int kap[7];
  {
    const v2f f0 = bc((float)(4 * tid)) + (v2f){-3.f, -2.f};
    u32 b0 = pk(f0), b1 = pk(f0 + bc(2.f)), b2 = pk(f0 + bc(4.f)), b3 = pk(f0 + bc(6.f));
    asm("" : "+v"(b0), "+v"(b1), "+v"(b2), "+v"(b3));
    kap[0] = (b0 & 0xffffu) * 4; kap[1] = (b0 >> 16) * 4; kap[2] = (b1 & 0xffffu) * 4; kap[3] = (b1 >> 16) * 4;
    kap[4] = (b2 & 0xffffu) * 4; kap[5] = (b2 >> 16) * 4; kap[6] = (b3 & 0xffffu) * 4;
    if (!CV && wave == 0) {
#pragma unroll
      for (int e = 0; e < 7; ++e) { const int l = 4 * tid + e - 3; if (l < 128) kap[e] = L_KLIN + 4 * (l < 0 ? 643 : l); }
    }
  }
  BAR();
  TS(19);
  const int gfo = OFF_WFL + (wave * 28 + (l5 < 28 ? l5 : 0)) * 128;
  v2u gf = GLD8(gfo);
  SB();
#pragma unroll
  for (int ch = 0; ch < 2; ++ch) {
    float k[7];
#pragma unroll
    for (int e = 0; e < 7; ++e) k[e] = LDS32F(kap[e] + ch * KLIN_CH);
    const u32 d10 = pk(k[1], k[0]), d21 = pk(k[2], k[1]), d32 = pk(k[3], k[2]), d43 = pk(k[4], k[3]), d54 = pk(k[5], k[4]), d65 = pk(k[6], k[5]);
    const int o = L_KC + ch * KC_CH + (511 - tid) * 8;
    v2u c0 = {d32, d10}, c1 = {d43, d21}, c2 = {d54, d32}, c3 = {d65, d43};
    LDS64(o) = c0; LDS64(o + KC_S) = c1; LDS64(o + 2 * KC_S) = c2; LDS64(o + 3 * KC_S) = c3;
  }
  if (tid < 32) { const v2u z0 = {0, 0}; LDS64(L_KC + (tid >> 2) * KC_S + 4096 + (tid & 3) * 8) = z0; }
  SB();
  if (__any(gf.x != tagL || gf.y != tagH)) {
    SPIN {
      __builtin_amdgcn_s_sleep(2); MEMB();
      gf = GLD8(gfo);
      if (!__any(gf.x != tagL || gf.y != tagH)) break;
    }
  }
  BAR();
  TS(4);
  const int c16 = lane & 15, g4 = lane >> 4;
  if constexpr (CV) {
    LAS char* pa = lds + opq(L_KC + ((wave >> 1) & 1) * KC_CH + (c16 & 3) * KC_S + 4104 - 8 * (c16 >> 2) - 16 * g4 - 256 * (wave & 1) - 64 * 59);
#pragma unroll
    for (int i = 0; i < 32; ++i) {
      const int o = 64 * (59 - 8 * (i & 7) - (i >> 3));
      const v2u u = *(LAS v2u*)(pa + o); SB();
      const v2u v = *(LAS v2u*)(pa + o + 8); SB();
      w1f[i] = (v4u){u.x, u.y, v.x, v.y};
    }
  } else ldw1(0, 10);
  TS(22);

  float ga_[2], gd_[2], g2_[2];
  {
    auto badx = [&]() { return ((gx0.x ^ tagL) | (gx1.x ^ tagL) | (gs0.x ^ tagL)) | ((gs1.x ^ tagL) | (gx0.w ^ tagH) | (gx1.w ^ tagH)) | ((gs0.w ^ tagH) | (gs1.w ^ tagH)); };
    if (__any((badx() | (gm.x ^ tagL) | (gm.w ^ tagH)) != 0)) {
      if (__any(badx() != 0)) {
        SPIN { __builtin_amdgcn_s_sleep(2); MEMB(); sweep(); if (!__any(badx() != 0)) break; }
      }
      SPIN { MEMB(); gm = GLD(mo); if (__all(tagok(gm, tagL, tagH))) break; __builtin_amdgcn_s_sleep(2); }
    }
    TS(23);
    const int mv = (p & 1) ? gm.z : gm.y;
    const u32 sa2 = __builtin_amdgcn_readlane(mv, 0), ca2 = __builtin_amdgcn_readlane(mv, 1), ga2 = __builtin_amdgcn_readlane(mv, 2), cb2 = __builtin_amdgcn_readlane(mv, 3);
    const v2f sa = unpk(sa2), g1 = unpk(pk(unpk(ca2) + bc(1.f)));
#pragma unroll
    for (int q = 0; q < 2; ++q) {
      const v4u gx = q ? gx1 : gx0, gs = q ? gs1 : gs0;
      const v2f h0 = fma2((unpk(gx.y) - bc(blo(gs.y))) * bc(__builtin_amdgcn_rcpf(bhi(gs.y))), g1, sa);
      const v2f h1 = fma2((unpk(gx.z) - bc(blo(gs.z))) * bc(__builtin_amdgcn_rcpf(bhi(gs.z))), g1, sa);
      const int lo = opq(L_Z + (q * 512 + tid) * 4);
      LDS32(lo + 480) = pk(h0.x, h1.x);
      LDS32(lo + 480 + Z_CH) = pk(h0.y, h1.y);
      LDS32(lo + (L_XT - L_Z)) = __builtin_amdgcn_perm(gx.z, gx.y, 0x05040100u);
      LDS32(lo + (L_XT - L_Z) + Z_CH) = __builtin_amdgcn_perm(gx.z, gx.y, 0x07060302u);
    }
    SB();
    asm volatile("s_waitcnt lgkmcnt(0)\n\ts_barrier" ::: "memory");
    SB();
    const v2f g2 = unpk(pk(unpk(cb2) + bc(1.f))), ga = unpk(ga2), gd = ga * unpk(dk2);
    ga_[0] = ga.x; ga_[1] = ga.y; gd_[0] = gd.x; gd_[1] = gd.y; g2_[0] = g2.x; g2_[1] = g2.y;
  }
  TS(5);

  const int epi_l = L_Z + 32 * c16 + 8 * g4;
  const int epi_g = OFF_X1 + (c16 >> 1) * XTILE + ((g4 & 1) ? 0 : XHALF) + p * 128 + (c16 & 1) * 32 + (g4 >> 1) * 16;
  auto epi = [&](int dump, int ch, int t, int t0) {
    conv_epi(lds, rs, dump + (ch * 8 + t) * 1024 + lane * 16, opq(ch * Z_CH + (t0 + t) * 512 + epi_l), epi_g + ch * 64 + (t0 + t) * 8 * XTILE,
             ch ? ga_[1] : ga_[0], ch ? gd_[1] : gd_[0], ch ? g2_[1] : g2_[0]);
  };
  if constexpr (CV) {
    LAS char* pbz = lds + opq(L_Z + ((wave >> 1) & 1) * Z_CH + 496 + 32 * c16 - 16 * g4 - 256 * (wave & 1) - 192);
    v4u B[16];
    auto rdb = [&](int slot, int rho, int dl) { B[slot] = *(LAS v4u*)(pbz + 64 * (8 * dl + 3 - rho)); SB(); };
    v4f acc[4];
#pragma unroll
    for (int n = 0; n < 16; ++n) rdb(n, n >> 2, 3 - (n & 3));
    __builtin_amdgcn_s_setprio(1);
#pragma unroll
    for (int t = 0; t < 4; ++t) acc[t] = (v4f){0, 0, 0, 0};
    {
      int n = 0;
#pragma unroll
      for (int rho = 0; rho < 4; ++rho) {
#pragma unroll
        for (int a = 0; a < 4; ++a) {
#pragma unroll
          for (int dl = 3 - a; dl >= 0; --dl) {
            acc[a + dl] = mfma16(w1f[8 * rho + a], B[4 * rho + 3 - dl], acc[a + dl]); tie(acc[a + dl]); SB();
            if (rho >= 2 && n < 8) { rdb(n, 0, 7 - n); ++n; }
          }
        }
      }
    }
#pragma unroll
    for (int t = 0; t < 4; ++t) LDS128F(L_DUMP0 + ((wave - 4) * 4 + t) * 1024 + lane * 16) = acc[t];
    TS(20);
    BAR();
    TS(6);
    __builtin_amdgcn_s_setprio(0);
    epi(L_DUMP0, CV, wave & 3, 0);
    SB();
    __builtin_amdgcn_s_setprio(1);
    TS(7);
#pragma unroll
    for (int t = 0; t < 4; ++t) acc[t] = (v4f){0, 0, 0, 0};
#pragma unroll
    for (int rho = 0; rho < 4; ++rho) {
      LGK0(); SB();
      int n = 0, cnt = 0;
#pragma unroll
      for (int a = 0; a < 8; ++a) {
#pragma unroll
        for (int dl = 7 - a; dl >= 0 && dl >= 4 - a; --dl) {
          acc[a + dl - 4] = mfma16(w1f[8 * rho + a], B[8 * (rho & 1) + 7 - dl], acc[a + dl - 4]); tie(acc[a + dl - 4]); SB();
          if (rho < 3 && n < 8) { rdb(8 * ((rho + 1) & 1) + n, rho + 1, 7 - n); ++n; }
          if ((rho == 1 || rho == 2) && cnt % 3 == 2) ldw1(8 * (rho - 1) + cnt / 3, 8 * (rho - 1) + cnt / 3 + 1);
          ++cnt;
        }
      }
    }
#pragma unroll
    for (int t = 0; t < 4; ++t) LDS128F(L_DUMP1 + ((wave - 4) * 4 + t) * 1024 + lane * 16) = acc[t];
    __builtin_amdgcn_s_setprio(0);
    TS(21);
    BAR();
  } else {
    if (b == 0 && tid == 0) { v2u e = {tagL, tagH}; __builtin_amdgcn_raw_buffer_store_b64(e, rs, 0, 0, 16); }
    if (tid == 0) GST8(((v2u){tagL, tagH}), OFF_WFL + p * 128 + 64);
    ldw1(10, 20);
    TS(20);
    BAR();
    TS(6);
    epi(L_DUMP0, CV, wave & 3, 0);
    SB();
    TS(7);
    ldw1(20, 28);
    SPIN {
      const v2u g = GLD8(OFF_WFL + (wave * 56 + (lane < 56 ? lane : 0)) * 128 + 64);
      if (!__any(g.x != tagL || g.y != tagH)) break;
      __builtin_amdgcn_s_sleep(2); MEMB();
    }
    TS(21);
    BAR();
  }
  TS(8);

  const int uvo = h ? OFF_CFL + grp * 128 : OFF_UV + (256 * sl + 32 * wave + lane) * 16;
  const int gbo = OFF_MOD + (560 + 16 * sl + (tid & 15)) * 16;
  v4u uv;
  const int trb = (8 * h + trq) * 64 + trc;
  const char* xg = ws + OFF_X1 + grp * XTILE + wave * 7168;
  const u32 xl = lane * 16;
  auto dma_x = [&](int pass, int i) {
    const int half = i >= 7, q = i - 7 * half, o = half * XHALF + (q >> 2) * 4096;
    const char* src = xg + pass * 32 * XTILE + o;
    if ((q & 3) == 0) { set_m0(lds0 + pass * XTILE + wave * 7168 + o); dma16_dev_o<0>(src, xl); }
    else if ((q & 3) == 1) dma16_dev_o<1024>(src, xl);
    else if ((q & 3) == 2) dma16_dev_o<2048>(src, xl);
    else dma16_dev_o<3072>(src, xl);
  };
  auto accept_x = [&](int pass) {
    const int lx = pass * XTILE + wave * 7168;
    float s, q;
    SPIN {
      u32 m = 0;
      v4u t[7], f[7];
#pragma unroll
      for (int i = 0; i < 7; ++i) t[i] = LDS128(lx + i * 1024 + lane * 16);
#pragma unroll
      for (int i = 0; i < 7; ++i) f[i] = trfrag(lds + lx + XHALF + trb + i * 1024);
#pragma unroll
      for (int i = 0; i < 7; ++i) m = max4(m, t[i]);
      s = 0.f; q = 0.f;
#pragma unroll
      for (int i = 0; i < 7; ++i) sumsq(f[i], s, q);
      if (!__any(m == SENTW || q != q)) break;
      MEMB();
#pragma unroll
      for (int i = 0; i < 7; ++i) m = max4(m, LDS128(lx + XHALF + i * 1024 + lane * 16));
      if (!__any(m == SENTW)) break;
      __builtin_amdgcn_s_sleep(4);
#pragma unroll
      for (int i = 0; i < 14; ++i) dma_x(pass, i);
      VMW(0);
    }
    s = swap32_add(s); q = swap32_add(q);
    if (!h) { v2f o = {s, q}; LDS64F(L_ST + pass * ST_P + lane * 48 + wave * 8) = o; }
  };
  v4u w2f[28];
  const int w2o = OFF_W2T + (sl * 8 + wave) * 28 * 1024;
  const int l16w = opq(lane * 16);
  auto ldw2 = [&](int i) { w2f[i] = GLDS(l16w + (i & 3) * 1024, w2o + (i >> 2) * 4096); SB(); };
  if constexpr (!CV) {
    uv = GLD(uvo);
    SB();
#pragma unroll
    for (int i = 0; i < 14; ++i) dma_x(0, i);
    TS(9);
    VMW(0);
    tie(uv);
    accept_x(0);
  } else {
    epi(L_DUMP1, (wave >> 1) & 1, (wave & 1) * 2, 4);
    epi(L_DUMP1, (wave >> 1) & 1, (wave & 1) * 2 + 1, 4);
    SB();
    TS(9);
    uv = GLD(uvo);
    ldw1(16, 28);
  }
  TS(10);
  BAR();
  TS(11);
  if (!__all(tagok(uv, tagL, tagH))) {
    SPIN { __builtin_amdgcn_s_sleep(4); MEMB(); uv = GLD(uvo); if (__all(tagok(uv, tagL, tagH))) break; }
  }
  v4u uvf;
  {
    const float u = __uint_as_float(uv.y), v = __uint_as_float(uv.z), uh = rbf(u), vh = rbf(v);
    uvf = (v4u){h ? 0u : pk(uh, u - uh), h ? 0u : pk(vh, v - vh), 0u, 0u};
  }

  const v16f zero16 = {0, 0, 0, 0, 0, 0, 0, 0, 0, 0, 0, 0, 0, 0, 0, 0};
  float sd, rl;
  auto gemm1 = [&](int pass, auto extra) {
    const int lx = pass * XTILE;
    LAS char* pb = lds + opq(lx + trb);
    v4u bf[4];
#pragma unroll
    for (int k = 0; k < 3; ++k) bf[k] = trfrag(pb + k * 1024);
    SB();
    v16f acc = zero16;
    v2u res; v4f a0; float mean, ex2; v4u bs;
    __builtin_amdgcn_s_setprio(1);
#pragma unroll
    for (int k = 0; k < 28; ++k) {
      if (k + 3 < 28) bf[(k + 3) & 3] = trfrag(pb + (k + 3) * 1024);
      acc = mfma32(w1f[k], bf[k & 3], acc); tie(acc);
      extra(k);
      if (k == 1) {
        res = trd(lds + lx + XHALF + (64 * sl + 8 * wave + 4 * h + trq) * 64 + trc);
        a0 = LDS128F(L_ST + pass * ST_P + (lane & 31) * 48 + h * 16);
      }
      if (k == 7) LDS64(L_RES + pass * RES_P + (2 * wave + h) * 272 + (lane & 31) * 8) = res;
      if (k == 9) mean = pin(swap32_add(pin(a0[0]) + opf(a0[2])) * (1.f / D));
      if (k == 11) ex2 = pin(swap32_add(pin(a0[1]) + opf(a0[3])) * (1.f / D));
      if (k == 13) ex2 = pin(rbf(fmaxf(pin(ex2) - mean * mean, 0.f)));
      if (k == 15) ex2 = pin(__builtin_amdgcn_sqrtf(rbf(pin(ex2) + 1e-5f)));
      if (k == 17) { sd = rbf(pin(ex2)); rl = pin(__builtin_amdgcn_rcpf(sd) * -1.4426950408889634f); }
      if (k == 19) { mean = pin(mean); bs = (v4u){h ? 0u : pk(-mean, -mean), h ? 0u : pk(sd, sd), 0u, 0u}; tie(bs); }
      SB();
    }
    acc = mfma32(uvf, bs, acc); tie(acc);
    SB();
    __builtin_amdgcn_s_setprio(0);
    return acc;
  };
  auto epi1 = [&](int pass, v16f acc) {
    tie(acc);
    u32 hp[8];
#pragma unroll
    for (int e = 0; e < 8; ++e) {
      const float a0 = opf(acc[2 * e]), a1 = opf(acc[2 * e + 1]);
      const float e0_ = __builtin_amdgcn_exp2f(a0 * rl), e1_ = __builtin_amdgcn_exp2f(a1 * rl);
      hp[e] = pks(a0 * __builtin_amdgcn_rcpf(fmaf(e0_, sd, sd)), a1 * __builtin_amdgcn_rcpf(fmaf(e1_, sd, sd)));
    }
    const int ho = opq(OFF_H + (grp + 32 * pass) * HTILE + (16 * sl + 2 * wave) * 1024 + lane * 16);
    v4u h0 = {hp[0], hp[1], hp[2], hp[3]}, h1 = {hp[4], hp[5], hp[6], hp[7]};
    GST(h0, ho); GST(h1, ho + 1024);
    SB();
  };
  v16f acc;
  if constexpr (!CV) {
    acc = gemm1(0, [&](int k) { if (!(k & 1)) dma_x(1, k >> 1); });
    TS(12);
    epi1(0, acc);
    VMW(2);
    accept_x(1);
    TS(13);
    BAR();
    acc = gemm1(1, [&](int k) { if (!(k & 1)) ldw2(k >> 1); });
  } else {
    acc = gemm1(0, [&](int k) { if (!(k & 1)) ldw2(k >> 1); });
    TS(12);
    BAR();
    epi1(0, acc);
    TS(13);
    acc = gemm1(1, [&](int k) { if (!(k & 1)) ldw2(14 + (k >> 1)); });
  }
  TS(14);
  epi1(1, acc);
  if constexpr (!CV) {
#pragma unroll
    for (int i = 14; i < 28; ++i) ldw2(i);
  }
  BAR();
  TS(15);

  const int hoA = OFF_H + grp * HTILE + wave * 14 * 1024;
  auto gemm2 = [&](int ho, int lr) {
    v4u hf[14];
    const int l16 = opq(lane * 16);
#pragma unroll
    for (int i = 0; i < 14; ++i) { hf[i] = GLDS(l16 + (i & 3) * 1024, ho + (i >> 2) * 4096); SB(); }
    __builtin_amdgcn_s_setprio(0);
    v16f c0 = zero16, c1 = zero16;
#pragma unroll
    for (int i = 0; i < 14; ++i) { c0 = mfma32(w2f[2 * i], hf[i], c0); c1 = mfma32(w2f[2 * i + 1], hf[i], c1); tie(c0); tie(c1); SB(); }
    if (__any(c0[0] != c0[0])) {
      _Pragma("unroll 1") for (int i = 0; i < 14; ++i) {
        SPIN {
          MEMB();
          const v4u t = GLDS(lane * 16, ho + i * 1024);
          if (!__any(t.x == SENTW || t.y == SENTW || t.z == SENTW || t.w == SENTW)) break;
          __builtin_amdgcn_s_sleep(4);
        }
      }
      MEMB();
      c0 = zero16; c1 = zero16;
#pragma unroll
      for (int i = 0; i < 14; ++i) {
        const v4u t = GLDS(lane * 16, ho + i * 1024);
        c0 = mfma32(w2f[2 * i], t, c0); c1 = mfma32(w2f[2 * i + 1], t, c1);
        SB();
      }
    }
    const int o = lr + wave * RED_W + h * 528 + (lane & 31) * 16;
#pragma unroll
    for (int q = 0; q < 4; ++q) {
      v4f o0 = {c0[4 * q], c0[4 * q + 1], c0[4 * q + 2], c0[4 * q + 3]}, o1 = {c1[4 * q], c1[4 * q + 1], c1[4 * q + 2], c1[4 * q + 3]};
      LDS128F(o + 2 * q * 528) = o0;
      LDS128F(o + (8 + 2 * q) * 528) = o1;
    }
    SB();
  };
  __builtin_amdgcn_s_setprio(1);
  gemm2(hoA, L_REDA);
  TS(16);
  v4u gbv = GLD(gbo);
  SB();
  gemm2(hoA + 32 * HTILE, L_REDB);
  TS(17);
  if (!__all(tagok(gbv, tagL, tagH))) {
    SPIN { __builtin_amdgcn_s_sleep(4); MEMB(); gbv = GLD(gbo); if (__all(tagok(gbv, tagL, tagH))) break; }
  }
  const u32 gb0 = gbv.y, gb1 = gbv.z;
  BAR();
  {
    const int dq = tid & 15, l = tid >> 4;
#pragma unroll
    for (int pass = 0; pass < 2; ++pass) {
      const int lr = pass * L_REDB + dq * 528 + l * 16;
      v4f s = LDS128F(lr);
#pragma unroll
      for (int w = 1; w < 8; ++w) s += LDS128F(lr + w * RED_W);
      const v2u rv = LDS64(L_RES + pass * RES_P + dq * 272 + l * 8);
      v2u o = {pk(fma2(unpk(gb0), s.xy, unpk(rv.x))), pk(fma2(unpk(gb1), s.zw, unpk(rv.y)))};
      *(GAS v2u*)(out + (pass * 1024 + grp * 32 + l) * D + 64 * sl + 4 * dq) = o;
    }
  }
  };
  if (wave < 4) rest(Role<0>{}); else rest(Role<1>{});
  TS(29); TSEND();
}

extern "C" void kernel_launch(void* const* d_in, const int* in_sizes, int n_in, void* d_out, int out_size, void* d_ws, size_t ws_size,
                              hipStream_t stream) {
  k_all<<<256, 512, 0, stream>>>((const u16*)d_in[0], (const u16*)d_in[1], (const u16*)d_in[2], (const u16*)d_in[3], (const u16*)d_in[4],
                                 (const u16*)d_in[5], (const u16*)d_in[6], (const u16*)d_in[7], (const u16*)d_in[8], (const u16*)d_in[10],
                                 (const u16*)d_in[11], (const u16*)d_in[12], (const u16*)d_in[13], (u16*)d_out, (char*)d_ws);
}
```

```cpp
#include <hip/hip_runtime.h>
#include <stdint.h>

typedef unsigned short u16;
typedef unsigned u32;
typedef unsigned long long u64;
typedef u32 v4u __attribute__((ext_vector_type(4)));
typedef u32 v2u __attribute__((ext_vector_type(2)));
typedef float v4f __attribute__((ext_vector_type(4)));
typedef float v2f __attribute__((ext_vector_type(2)));
typedef float v16f __attribute__((ext_vector_type(16)));
typedef __bf16 bf8 __attribute__((ext_vector_type(8)));
typedef __bf16 bf2 __attribute__((ext_vector_type(2)));
typedef short s4 __attribute__((ext_vector_type(4)));
#define GAS __attribute__((address_space(1)))
#define LAS __attribute__((address_space(3)))
#define DEV __device__ __forceinline__

constexpr int D = 448, F = 1792;

constexpr int OFF_STAT = 4096;
constexpr int OFF_MOD = 20480;
constexpr int OFF_UV = 32768;
constexpr int OFF_CFL = 61440;
constexpr int OFF_WFL = 65536;
constexpr int OFF_XFL = 98304;
constexpr int OFF_XT = 131072;
constexpr int OFF_W1T = 4 << 20;
constexpr int OFF_W2T = 6 << 20;
constexpr int OFF_X1 = 8 << 20;
constexpr int OFF_H = 12 << 20;
constexpr int OFF_DBG = 24 << 20;
constexpr int WS_BYTES = 32 << 20;
constexpr int XTILE = 57344, XHALF = 28672, HTILE = 114688;

constexpr int KG_W = 8704;
constexpr int L_KC = 0, KC_S = 4160, KC_CH = 4 * KC_S;
constexpr int L_LNT = 69632;
constexpr int L_STX = 76800;
constexpr int L_KLIN = 68096, KLIN_CH = 2592;
constexpr int NWS = 13, L_WS = 57344;
constexpr int L_Z = 110592, Z_CH = 4608;
constexpr int L_XT = 119808;
constexpr int L_DUMP = 129024;
constexpr int L_XA = 0, L_XB = XTILE;
constexpr int RED_S = 576, RED_W = 16 * RED_S, L_REDA = 0, L_REDB = 8 * RED_W;
constexpr int L_RES = 147456, RES_P = 4608;
constexpr int L_ST = 156672, ST_P = 1536;
constexpr int L_PF = 159744;
constexpr int L_FL = 160000;
constexpr int F_D0 = L_FL, F_D1 = L_FL + 16, F_EB = L_FL + 32, F_XA = L_FL + 48, F_XB = L_FL + 64, F_PA = L_FL + 80, F_TR = L_FL + 96,
              F_WS = L_FL + 112, F_RA = L_FL + 128, F_XL = L_FL + 160;
constexpr int L_WA = 160192;
constexpr int LDS_BYTES = 160256;
constexpr int L_AR0 = 0, L_AR1 = 81920;
constexpr int L_ASC = 57344;
constexpr int L_ARED = 58368;
constexpr int L_AG = 60416;
constexpr int L_ARED2 = 62464;

constexpr u32 SPIN_CAP = 1u << 14;
constexpr u32 SENTW = 0xffbfffbfu;
#define CODE_PF 36864
#define NPRE 10
#define N1 28
#define NB2 6
#define NB1 6
#define NC1 21
#define NCA 14
#define TAIL 0
#define TRIG 3
#define TRIGN 8
#define VPOS 12

DEV float blo(u32 v) { return __uint_as_float(v << 16); }
DEV float bhi(u32 v) { return __uint_as_float(v & 0xffff0000u); }
DEV v2f unpk(u32 v) { v2f r = {blo(v), bhi(v)}; return r; }
DEV u32 pk(float a, float b) { bf2 r = {(__bf16)a, (__bf16)b}; return __builtin_bit_cast(u32, r); }
DEV u32 pk(v2f a) { return pk(a.x, a.y); }
DEV float rbf(float a) { return blo(pk(a, a)); }
DEV u32 umax(u32 a, u32 b) { return a > b ? a : b; }
DEV u32 max4(u32 m, v4u v) { return umax(umax(umax(umax(m, v.x), v.y), v.z), v.w); }
DEV v2f fma2(v2f a, v2f b, v2f c) { return __builtin_elementwise_fma(a, b, c); }
DEV v2f bc(float a) { v2f r = {a, a}; return r; }
DEV v4f mfma16(v4u a, v4u b, v4f c) { return __builtin_amdgcn_mfma_f32_16x16x32_bf16(__builtin_bit_cast(bf8, a), __builtin_bit_cast(bf8, b), c, 0, 0, 0); }
DEV v16f mfma32(v4u a, v4u b, v16f c) { return __builtin_amdgcn_mfma_f32_32x32x16_bf16(__builtin_bit_cast(bf8, a), __builtin_bit_cast(bf8, b), c, 0, 0, 0); }
DEV float dot2(u32 a, u32 b, float c) { return __builtin_amdgcn_fdot2_f32_bf16(__builtin_bit_cast(bf2, a), __builtin_bit_cast(bf2, b), c, false); }
DEV void sumsq(v4u f, float& s, float& q) {
  s = dot2(f.x, 0x3f803f80u, s); q = dot2(f.x, f.x, q); s = dot2(f.y, 0x3f803f80u, s); q = dot2(f.y, f.y, q);
  s = dot2(f.z, 0x3f803f80u, s); q = dot2(f.z, f.z, q); s = dot2(f.w, 0x3f803f80u, s); q = dot2(f.w, f.w, q);
}
DEV v2u trd(LAS char* p) { return __builtin_bit_cast(v2u, __builtin_amdgcn_ds_read_tr16_b64_v4i16((LAS s4*)p)); }
DEV v4u trfrag(LAS char* p) { v2u a = trd(p), b = trd(p + 256); v4u r = {a.x, a.y, b.x, b.y}; return r; }
template <int CTRL, int RM> DEV float dppadd(float v) {
  return v + __builtin_bit_cast(float, __builtin_amdgcn_update_dpp(0, __builtin_bit_cast(int, v), CTRL, RM, 0xf, RM == 0xf));
}
DEV float rdl(float v, int l) { return __builtin_bit_cast(float, __builtin_amdgcn_readlane(__builtin_bit_cast(int, v), l)); }
DEV float swap32_add(float v) { auto r = __builtin_amdgcn_permlane32_swap(__float_as_uint(v), __float_as_uint(v), false, false); return __uint_as_float(r[0]) + __uint_as_float(r[1]); }
DEV float swap16_add(float v) { auto r = __builtin_amdgcn_permlane16_swap(__float_as_uint(v), __float_as_uint(v), false, false); return __uint_as_float(r[0]) + __uint_as_float(r[1]); }
DEV int opq(int v) { asm volatile("" : "+v"(v)); return v; }
DEV float opf(float v) { asm("" : "+v"(v)); return v; }
DEV float pin(float v) { asm volatile("" : "+v"(v)); return v; }
DEV u32 pks(float a, float b) { return pk(opf(a), opf(b)); }
DEV v2f cmul(v2f a, v2f w, v2f wn) { return fma2(a.yx, wn, a * w.xx); }
DEV v2f cneg(v2f w) { v2f r = {-w.y, w.y}; return r; }
DEV v2f cexpx(float er, float ei, float x) {
  const float e = __builtin_amdgcn_exp2f(er * x), ph = __builtin_amdgcn_fractf(ei * x);
  v2f c = {__builtin_amdgcn_cosf(ph), __builtin_amdgcn_sinf(ph)};
  return c * bc(e);
}

#define BAR() asm volatile("s_waitcnt lgkmcnt(0)\n\ts_barrier" ::: "memory")
#define VMW(n) asm volatile("s_waitcnt vmcnt(" #n ")" ::: "memory")
#define LGK0() __builtin_amdgcn_s_waitcnt(0xC07F)
#define MEMB() asm volatile("" ::: "memory")
#define SB() __builtin_amdgcn_sched_barrier(0)
#define SPIN _Pragma("unroll 1") for (u32 sp = 0; sp < SPIN_CAP; ++sp)
#define LDS128(off) (*(LAS v4u*)(lds + (off)))
#define LDS128F(off) (*(LAS v4f*)(lds + (off)))
#define LDS64(off) (*(LAS v2u*)(lds + (off)))
#define LDS64F(off) (*(LAS v2f*)(lds + (off)))
#define LDS32(off) (*(LAS u32*)(lds + (off)))
#define LDS32F(off) (*(LAS float*)(lds + (off)))
#define GLD(off) __builtin_amdgcn_raw_buffer_load_b128(rs, (off), 0, 16)
#define GLDS(voff, soff) __builtin_amdgcn_raw_buffer_load_b128(rs, (voff), (soff), 16)
#define GLD8(off) __builtin_amdgcn_raw_buffer_load_b64(rs, (off), 0, 16)
#define GST8(v, off) __builtin_amdgcn_raw_buffer_store_b64((v), rs, (off), 0, 16)
#define GST(v, off) __builtin_amdgcn_raw_buffer_store_b128((v), rs, (off), 0, 16)

DEV void dma16(u32 ldsaddr, const char* gbase, u32 voff) {
  asm volatile("s_mov_b32 m0, %0\n\ts_nop 0\n\tglobal_load_lds_dwordx4 %1, %2" :: "s"(ldsaddr), "v"(voff), "s"(gbase) : "memory");
}
DEV void dma16_dev(u32 ldsaddr, const char* gbase, u32 voff) {
  asm volatile("s_mov_b32 m0, %0\n\ts_nop 0\n\tglobal_load_lds_dwordx4 %1, %2 sc1" :: "s"(ldsaddr), "v"(voff), "s"(gbase) : "memory");
}
DEV void dma4_dev(u32 ldsaddr, const char* gbase, u32 voff) {
  asm volatile("s_mov_b32 m0, %0\n\ts_nop 0\n\tglobal_load_lds_dword %1, %2 sc1" :: "s"(ldsaddr), "v"(voff), "s"(gbase) : "memory");
}
DEV void set_m0(u32 v) { asm volatile("s_mov_b32 m0, %0\n\ts_nop 0" :: "s"(v) : "memory"); }
template <int OFF> DEV void dma16_dev_o(const char* gbase, u32 voff) {
  asm volatile("global_load_lds_dwordx4 %0, %1 offset:%2 sc1" :: "v"(voff), "s"(gbase), "i"(OFF) : "memory");
}
template <class T> DEV void tie(T& v) { asm volatile("" : "+v"(v)); }
template <int OFF> DEV void st_tid(u32 v) { asm volatile("ds_write_addtid_b32 %0 offset:%1" :: "v"(v), "i"(OFF) : "memory"); }

#define TS(i)
#define TSEND()

template <int V> struct Role { static constexpr int value = V; };
DEV bool tagok(v4u g, u32 tagL, u32 tagH) { return g.x == tagL && g.w == tagH; }

struct Ln1 { int lw, lr, go; };
DEV Ln1 ln1_addr(u32 b, int lane, u32 wave) {
  Ln1 a = {L_LNT + (int)wave * 1024 + lane * 16, L_LNT + (int)wave * 1024 + (lane & 3) * 256 + (lane >> 2) * 8,
           OFF_XT + (32 * (int)wave + 2 * (lane >> 2)) * 16384 + (4 * (int)b + (lane & 3)) * 16};
  tie(a.lw); tie(a.lr); tie(a.go);
  return a;
}
DEV void ln1_publish(LAS char* lds, __amdgpu_buffer_rsrc_t rs, v4u xr, Ln1 ad, u32 b, int lane, u32 wave, u32 tagL, u32 tagH) {
  if (wave < 7) {
    LDS128(ad.lw) = xr;
    const v2u ra = LDS64(ad.lr), rb = LDS64(ad.lr + 128);
    float s = 0.f, q = 0.f;
    sumsq(xr, s, q);
    s = dppadd<0xb1, 0xf>(s); q = opf(dppadd<0xb1, 0xf>(q));
    s = dppadd<0x4e, 0xf>(s); q = opf(dppadd<0x4e, 0xf>(q));
    s = dppadd<0x141, 0xf>(s); q = opf(dppadd<0x141, 0xf>(q));
    MEMB();
    if ((lane & 7) == 0) { const v4u o = {__float_as_uint(s), __float_as_uint(q), tagL, tagH}; LDS128(L_STX + wave * 128 + lane * 2) = o; }
    MEMB();
    const v4u o0 = {tagL, ra.x, rb.x, tagH}, o1 = {tagL, ra.y, rb.y, tagH};
    GST(o0, ad.go); GST(o1, ad.go + 16384);
  } else {
    v4u t;
    _Pragma("unroll 1") for (u32 sp = 0; sp < (1u << 22); ++sp) {
      t = *(volatile LAS v4u*)(lds + L_STX + (lane < 56 ? lane : 0) * 16);
      if (!__any(t.z != tagL || t.w != tagH)) break;
      __builtin_amdgcn_s_sleep(1);
    }
    float s = lane < 56 ? __uint_as_float(t.x) : 0.f, q = lane < 56 ? __uint_as_float(t.y) : 0.f;
    s = dppadd<0x128, 0xf>(s); q = dppadd<0x128, 0xf>(q);
    s = swap32_add(swap16_add(s)); q = swap32_add(swap16_add(q));
    const float mean = s * (1.f / D), var = rbf(fmaxf(q * (1.f / D) - mean * mean, 0.f));
    const u32 ms = pk(mean, __builtin_amdgcn_sqrtf(rbf(var + 1e-5f)));
    const v4u o = {tagL, ms, (u32)__builtin_amdgcn_update_dpp(0, (int)ms, 0xb1, 0xf, 0xf, true), tagH};
    if (lane < 8 && !(lane & 1)) GST(o, OFF_STAT + 64 * b + lane * 8);
  }
  MEMB();
}

DEV void conv_epi(LAS char* lds, __amdgpu_buffer_rsrc_t rs, int dump, int lz, int goff, float ga, float gd, float g2) {
  const v4f p0 = LDS128F(dump), p1 = LDS128F(dump + 4096);
  const v2u hh = LDS64(lz + 480), xx = LDS64(lz + (L_XT - L_Z));
  const float h4[4] = {blo(hh.x), bhi(hh.x), blo(hh.y), bhi(hh.y)}, x4[4] = {blo(xx.x), bhi(xx.x), blo(xx.y), bhi(xx.y)};
  float o[4];
#pragma unroll
  for (int e = 0; e < 4; ++e) o[e] = fmaf(ga, opf(p0[e]) + opf(p1[e]), fmaf(gd, h4[e], x4[e]));
  u32 X0 = pks(o[0], o[1]), X1 = pks(o[2], o[3]);
  asm("" : "+v"(X0), "+v"(X1));
  const u32 S0 = pks(blo(X0) * g2, bhi(X0) * g2), S1 = pks(blo(X1) * g2, bhi(X1) * g2);
  auto r0 = __builtin_amdgcn_permlane16_swap(X0, S0, false, false);
  auto r1 = __builtin_amdgcn_permlane16_swap(X1, S1, false, false);
  v4u v = {r0[0], r1[0], r0[1], r1[1]};
  GST(v, goff);
}

__global__ void __launch_bounds__(512, 2) k_all(const u16* __restrict__ x, const u16* __restrict__ Are, const u16* __restrict__ Aim,
    const u16* __restrict__ Bre, const u16* __restrict__ Bim, const u16* __restrict__ Cre, const u16* __restrict__ Cim,
    const u16* __restrict__ logdt, const u16* __restrict__ Dskip, const u16* __restrict__ w1, const u16* __restrict__ w2,
    const u16* __restrict__ cvec, const u16* __restrict__ wmod, u16* __restrict__ out, char* ws) {
  __shared__ __attribute__((aligned(16))) char smem[LDS_BYTES];
  LAS char* lds = (LAS char*)smem;
  const u32 lds0 = (u32)(size_t)lds;
  u64 pc;
  asm volatile("s_getpc_b64 %0" : "=s"(pc));
  asm volatile("" :: "s"(x), "s"(Are), "s"(Aim), "s"(Bre), "s"(Bim), "s"(Cre), "s"(Cim), "s"(logdt), "s"(Dskip), "s"(w1), "s"(w2), "s"(cvec), "s"(wmod), "s"(out), "s"(ws));
  const int tid = threadIdx.x, lane = tid & 63;
  const u32 wave = __builtin_amdgcn_readfirstlane(tid >> 6);
  const u32 b = blockIdx.x, xcd = b & 7, j = b >> 3;
  TS(0);
  const __amdgpu_buffer_rsrc_t rs = __builtin_amdgcn_make_buffer_rsrc(ws, 0, WS_BYTES, 0x00020000);
  auto first_loads = [&](v4u& xr, u64& e0) {
    const u64 xa = (u64)(x + 8 * b * D);
    const v4u srd = {(u32)xa, (u32)(xa >> 32) & 0xffffu, 16 * D, 0x00020000u};
    const u32 vo = wave < 7 ? (lane >> 3) * (2 * D) + 128 * wave + (lane & 7) * 16 : 16 * D;
    asm volatile("buffer_load_dwordx4 %0, %1, %2, 0 offen ; ALOAD" : "=v"(xr) : "v"(vo), "s"(srd) : "memory");
    asm volatile("s_load_dwordx2 %0, %1, 0x0 ; SLOAD" : "=s"(e0) : "s"(ws) : "memory");
  };
  const v4u SENT = {SENTW, SENTW, SENTW, SENTW};
  auto gettag = [&](u64 e0, u32& tl, u32& th) {
    const u64 T = (((u64)(u32)__builtin_amdgcn_readfirstlane((u32)(e0 >> 32)) << 32) | (u32)__builtin_amdgcn_readfirstlane((u32)e0)) + 0x9E3779B97F4A7C15ull; tl = (u32)T; th = (u32)(T >> 32); };
  u32 tagL, tagH;

  if (j >= 28) {
    v4u xr; u64 e0;
    first_loads(xr, e0);
    const u32 a = (j - 28) * 8 + xcd;
    const int h = lane >> 5, trq = (lane & 15) >> 2, trc = ((lane >> 4) & 1) * 32 + (lane & 3) * 8;
    const u32 wv = wave < 7 ? wave : 0;
    auto stage = [&](u32 ldst, const char* src, u32 rstride) {
      if (wave < 7) {
        const u32 vo = ((lane >> 3) + 64 * wave) * rstride + (lane & 7) * 16;
#pragma unroll
        for (int i = 0; i < 8; ++i) dma16(lds0 + ldst + wave * 8192 + i * 1024, src + i * 8 * rstride, vo);
      }
    };
    auto gemv = [&](u32 lsrc, int atab, float& r0, float& r1) {
      v16f c0 = {0, 0, 0, 0, 0, 0, 0, 0, 0, 0, 0, 0, 0, 0, 0, 0}, c1 = c0;
      LAS char* pb = lds + opq(lsrc + wv * 8192 + (8 * h + trq) * 128 + trc);
#pragma unroll
      for (int k = 0; k < 4; ++k) {
        const v4u af = LDS128(atab + wv * 128 + k * 32 + h * 16);
        const v2u b0 = trd(pb + k * 2048), b1 = trd(pb + k * 2048 + 512), b2 = trd(pb + k * 2048 + 64), b3 = trd(pb + k * 2048 + 576);
        c0 = mfma32(af, (v4u){b0.x, b0.y, b1.x, b1.y}, c0); c1 = mfma32(af, (v4u){b2.x, b2.y, b3.x, b3.y}, c1);
      }
      r0 = h ? c1[0] : c0[0]; r1 = h ? c1[1] : c0[1];
    };
    u32 cv;
    asm volatile("global_load_ushort %0, %1, %2 ; ALOAD" : "=v"(cv) : "v"((64 * wv + lane) * 2), "s"(cvec) : "memory");
    const u32 lc = a < 21 ? a : (a < 28 ? a + 7 : a - 7);
    stage(L_AR0, (const char*)wmod + lc * 128, 6 * D * 2);
    if (a >= 3) VMW(8);
    if (a < 3) stage(L_AR1, (const char*)wmod + (25 + a) * 128, 6 * D * 2);
    const Ln1 lna = ln1_addr(b, lane, wave);
    if (wave == 7) VMW(0);
    asm volatile("s_waitcnt vmcnt(16) lgkmcnt(0) ; TIE" : "+v"(xr), "+s"(e0), "+v"(cv) :: "memory");
    {
      const float cf_ = blo(cv);
      *(LAS u16*)(lds + L_ASC + (64 * wv + lane) * 2) = (u16)pk(cf_ * __builtin_amdgcn_rcpf(1.f + __expf(-cf_)), 0.f);
    }
    gettag(e0, tagL, tagH);
    TS(1);
    ln1_publish(lds, rs, xr, lna, b, lane, wave, tagL, tagH);
    TS(2);
    if (a < 3) VMW(10); else VMW(2);
    auto modround = [&](u32 lsrc, u32 lc_) {
      float r0, r1;
      gemv(lsrc, L_ASC, r0, r1);
      LDS32F(L_ARED + wave * 256 + lane * 4) = r0;
      BAR();
      if (tid < 16) {
        v4f s = LDS128F(L_ARED + tid * 16);
#pragma unroll
        for (int w = 1; w < 7; ++w) s += LDS128F(L_ARED + w * 256 + tid * 16);
        v4u o = {tagL, pk(s[0], s[1]), pk(s[2], s[3]), tagH};
        GST(o, OFF_MOD + (16 * lc_ + tid) * 16);
      }
    };
    modround(L_AR0, lc);
    TS(3);
    if (a < 3) { VMW(0); BAR(); modround(L_AR1, 25 + a); }
    if (a >= 4) stage(L_AR1, (const char*)w1 + (a - 4) * 128, F * 2);
    if (a >= 3 && a < 10) stage(L_AR0, (const char*)wmod + (32 + a) * 128, 6 * D * 2);
    const int hc = OFF_H + a * HTILE + tid * 16;
#pragma unroll
    for (int i = 0; i < 4; ++i) GST(SENT, hc + i * 8192);
    TS(4);
    if (a >= 4) {
      if (wave < 4) {
        const int gi = 336 + (tid < 224 ? tid : 0);
        v4u g;
        SPIN {
          MEMB();
          g = GLD(OFF_MOD + gi * 16);
          if (__all(tagok(g, tagL, tagH))) break;
          __builtin_amdgcn_s_sleep(8);
        }
        v2u f = {g.y, g.z};
        if (tid >= 112) { f.x = pk(unpk(f.x) + bc(1.f)); f.y = pk(unpk(f.y) + bc(1.f)); }
        if (tid < 224) LDS64(L_AG + tid * 8) = f;
      }
      VMW(0);
      BAR();
      TS(5);
      float r0, r1;
      gemv(L_AR1, L_AG + ((lane & 1) ? 0 : 2 * D), r0, r1);
      { v2f o = {r0, r1}; LDS64F(L_ARED2 + wave * 512 + lane * 8) = o; }
      BAR();
      if (tid < 64) {
        v2f s = LDS64F(L_ARED2 + tid * 8);
#pragma unroll
        for (int w = 1; w < 7; ++w) s += LDS64F(L_ARED2 + w * 512 + tid * 8);
        v4u o = {tagL, __float_as_uint(s[0]), __float_as_uint(s[1]), tagH};
        GST(o, OFF_UV + (64 * (a - 4) + tid) * 16);
      }
    }
    SB();
    TS(6);
#pragma unroll
    for (int i = 4; i < 14; ++i) GST(SENT, hc + i * 8192);
#pragma unroll
    for (int i = 0; i < 14; ++i) GST(SENT, hc + 32 * HTILE + i * 8192);
    VMW(0);
    BAR();
    if (tid == 0) { v4u f = {tagL, 0, 0, tagH}; GST(f, OFF_CFL + a * 128); }
    TS(7);
    if (a >= 3 && a < 10) modround(L_AR0, 32 + a);
    TS(29); TSEND();
    return;
  }

  v4u xr; u64 e0;
  first_loads(xr, e0);
  const u32 p = xcd * 28 + j;
  const u32 jq = (j * 37) >> 8;
  const u32 grp = xcd * 4 + jq, sl = j - 7 * jq;
  const u32 chw = wave >> 2;

  const u32 pi = ((2 * p + chw) * 256 + (tid & 255)) * 2;
  u32 pAr, pAi, pBr, pBi, pCr, pCi, pLd2, dk2;
  asm volatile("global_load_ushort %0, %1, %2 ; ALOAD" : "=v"(pAr) : "v"(pi), "s"(Are) : "memory");
  asm volatile("global_load_ushort %0, %1, %2 ; ALOAD" : "=v"(pAi) : "v"(pi), "s"(Aim) : "memory");
  asm volatile("global_load_ushort %0, %1, %2 ; ALOAD" : "=v"(pBr) : "v"(pi), "s"(Bre) : "memory");
  asm volatile("global_load_ushort %0, %1, %2 ; ALOAD" : "=v"(pBi) : "v"(pi), "s"(Bim) : "memory");
  asm volatile("global_load_ushort %0, %1, %2 ; ALOAD" : "=v"(pCr) : "v"(pi), "s"(Cre) : "memory");
  asm volatile("global_load_ushort %0, %1, %2 ; ALOAD" : "=v"(pCi) : "v"(pi), "s"(Cim) : "memory");
  asm volatile("s_load_dword %0, %1, %2 ; SLOAD" : "=s"(pLd2) : "s"(logdt), "s"(4 * p) : "memory");
  asm volatile("s_load_dword %0, %1, %2 ; SLOAD" : "=s"(dk2) : "s"(Dskip), "s"(4 * p) : "memory");
  const bool isw1 = xcd < 4;
  u32 wt_dst = 0;
  if (wave < 7) {
    const char* src; u32 rstride;
    if (isw1) {
      const u32 kap = p >> 2, fg = (p & 3) * 7 + wave;
      src = (const char*)w1 + kap * (16 * F * 2) + fg * 128; rstride = F * 2;
      wt_dst = OFF_W1T + (fg * 56 + kap) * 1024;
    } else {
      const u32 kk = p - 112;
      src = (const char*)w2 + kk * (16 * D * 2) + wave * 128; rstride = D * 2;
      wt_dst = OFF_W2T + (wave * 224 + 2 * kk) * 1024;
    }
    const u32 vo = (lane >> 3) * rstride + (lane & 7) * 16;
    dma16(lds0 + wave * KG_W, src, vo);
    dma16(lds0 + wave * KG_W + 1024, src + 8 * rstride, vo);
  } else {
    set_m0(lds0 + L_PF);
    const u32 a = (lane >> 4) * 8 + xcd, lc = a < 21 ? a : (a < 28 ? a + 7 : a - 7);
    asm volatile("global_load_lds_dword %0, %1" :: "v"((16 * j + (lane & 15)) * (12 * D) + lc * 128), "s"(wmod) : "memory");
    u32 vo = (j & 7) * 8192 + lane * 128; vo = vo < CODE_PF ? vo : 0;
    asm volatile("global_load_lds_dword %0, %1" :: "v"(vo), "s"(pc & ~255ull) : "memory");
  }
  const int h = lane >> 5, trq = (lane & 15) >> 2, trc = ((lane >> 4) & 1) * 32 + (lane & 3) * 8;
  const Ln1 lna = ln1_addr(b, lane, wave);
  int wt_a = wave * KG_W + (isw1 ? (8 * h + trq) * 128 + trc : (4 * h + trq) * 128 + 2 * trc), wt_b = wt_a + (isw1 ? 512 : 1024);
  int wt_c = wt_a + (isw1 ? 64 : 8), wt_d = wt_b + (isw1 ? 64 : 8), wt_g = lane * 16 + wt_dst, wt_h = wt_g + (isw1 ? 28 * 1024 : 1024);
  tie(wt_a); tie(wt_b); tie(wt_c); tie(wt_d); tie(wt_g); tie(wt_h);
  int rd_src, rd_dst, rd_dk;
  {
    const int jl = lane >> 3, iq = lane & 7, wq = wave & 3, kl = L_KLIN + chw * KLIN_CH;
    rd_src = chw * 4 * KG_W + 144 * (8 * wq + (wq == 3 ? 0 : jl)) + 16 * iq;
    if (wq < 2) {
      const int jj = 8 * wq + jl, hi = iq >> 2;
      rd_dst = (hi & jj) ? kl + 4 * 641 : kl + 512 * hi + ((256 * iq + 4 * jj) >> hi); rd_dk = (hi & jj) ? 0 : 64 >> hi;
    } else if (wq == 2) {
      const int hi = jl >> 2;
      rd_dst = kl + (hi ? 4 * (512 + 8 * iq + 32 * (jl & 2) + (jl & 1)) : 4 * (384 + 16 * iq + jl)); rd_dk = 16 >> hi;
    } else { rd_dst = kl + 4 * (lane ? 641 : 640); rd_dk = lane ? 0 : 4; }
    tie(rd_src); tie(rd_dst); tie(rd_dk);
  }
  int kap[7];
  {
    const v2f f0 = bc((float)(4 * tid)) + (v2f){-3.f, -2.f};
    u32 b0 = pk(f0), b1 = pk(f0 + bc(2.f)), b2 = pk(f0 + bc(4.f)), b3 = pk(f0 + bc(6.f));
    asm("" : "+v"(b0), "+v"(b1), "+v"(b2), "+v"(b3));
    kap[0] = (b0 & 0xffffu) * 4; kap[1] = (b0 >> 16) * 4; kap[2] = (b1 & 0xffffu) * 4; kap[3] = (b1 >> 16) * 4;
    kap[4] = (b2 & 0xffffu) * 4; kap[5] = (b2 >> 16) * 4; kap[6] = (b3 & 0xffffu) * 4;
    if (wave == 0) {
#pragma unroll
      for (int e = 0; e < 7; ++e) { const int l = 4 * tid + e - 3; if (l < 128) kap[e] = L_KLIN + 4 * (l < 0 ? 644 : l); }
    }
#pragma unroll
    for (int e = 0; e < 7; ++e) tie(kap[e]);
  }
  TS(28);
  if (wave < 7) VMW(0);
  asm volatile("s_waitcnt vmcnt(2) lgkmcnt(0) ; TIE" : "+v"(xr), "+s"(e0), "+v"(pAr), "+v"(pAi), "+v"(pBr), "+v"(pBi), "+v"(pCr), "+v"(pCi), "+s"(pLd2), "+s"(dk2) :: "memory");
  TS(27);
  gettag(e0, tagL, tagH);
  pLd2 = __builtin_amdgcn_readfirstlane(pLd2); dk2 = __builtin_amdgcn_readfirstlane(dk2);
  if (wave < 7) {
    v2u a0 = trd(lds + wt_a), a1 = trd(lds + wt_b), b0 = trd(lds + wt_c), b1 = trd(lds + wt_d);
    GST(((v4u){a0.x, a0.y, a1.x, a1.y}), wt_g);
    GST(((v4u){b0.x, b0.y, b1.x, b1.y}), wt_h);
  }
  SB();
  ln1_publish(lds, rs, xr, lna, b, lane, wave, tagL, tagH);
  TS(1);

  v4u gx0, gx1, gs0, gs1, gm;
  const int mo = OFF_MOD + (((lane & 3) == 3 ? 4 : (lane & 3)) * 112 + (p >> 1)) * 16;
  auto sweep = [&]() {
    gx0 = GLDS(tid * 16, OFF_XT + p * 16384); gx1 = GLDS(tid * 16, OFF_XT + p * 16384 + 8192);
    gs0 = GLDS(tid * 16, OFF_STAT); gs1 = GLDS(tid * 16, OFF_STAT + 8192);
  };

  {
    const float dt = rbf(expf(chw ? bhi(pLd2) : blo(pLd2)));
    const float er = (dt * blo(pAr)) * 1.4426950408889634f, ei = (dt * blo(pAi)) * 0.15915494309189535f;
    const v2f dB = {dt * blo(pBr), dt * blo(pBi)}, Cc = {blo(pCr), blo(pCi)};
    v2f X = cmul(Cc, dB, cneg(dB));
    X.y = -X.y;
    const v2f e1 = cexpx(er, ei, 1.f), e16 = cexpx(er, -ei, 16.f), e512 = cexpx(er, ei, 512.f);
    const v2f e1n = cneg(e1), e16n = cneg(e16), e512n = cneg(e512);
    set_m0(lds0 + wave * KG_W);
    st_tid<0>(0x00003f80u);
    v2f Y = e1, Y4, Y8, Y12;
#define YROW(jj) st_tid<jj * 272>(pk(Y)); if (jj == 4) Y4 = Y; if (jj == 8) Y8 = Y; if (jj == 12) Y12 = Y; if (jj < 15) Y = cmul(Y, e1, e1n);
    YROW(1) YROW(2) YROW(3) YROW(4) YROW(5) YROW(6) YROW(7) YROW(8) YROW(9) YROW(10) YROW(11) YROW(12) YROW(13) YROW(14) YROW(15)
    st_tid<16 * 272>(pk(e512));
    st_tid<17 * 272>(pk(cmul(Y4, e512, e512n)));
    Y = cmul(Y8, e512, e512n);  st_tid<18 * 272>(pk(Y));
    st_tid<19 * 272>(pk(cmul(Y12, e512, e512n)));
    v2f E = cmul(e512, e512, e512n); st_tid<20 * 272>(pk(E));
    Y = cmul(Y, e512, e512n);   st_tid<21 * 272>(pk(Y));
    E = cmul(E, e512, e512n);   st_tid<22 * 272>(pk(E));
    Y = cmul(Y, e512, e512n);   st_tid<23 * 272>(pk(Y));
    E = cmul(E, e512, e512n);   st_tid<24 * 272>(pk(E));
    const int r = lane & 31;
    v4u yf[8];
    LAS char* py = lds + wave * KG_W + (r < 24 ? r : 24) * 272 + 16 * h;
    LAS char* px = lds + wave * KG_W + r * 272 + 16 * h;
#pragma unroll
    for (int m = 0; m < 8; ++m) yf[m] = *(LAS v4u*)(py + 32 * m);
    TS(25);
#define XROW(i) st_tid<(i) * 272>(pk(X)); if ((i) < 31) X = cmul(X, e16, e16n);
#define XROW4(i) XROW(i) XROW(i + 1) XROW(i + 2) XROW(i + 3)
    if (wave == 7) VMW(1); else VMW(2);
    asm volatile("s_barrier" ::: "memory");
    if (tid == 448) GST8(((v2u){tagL, tagH}), OFF_WFL + p * 128);
    TS(18);
    {
      const int o = OFF_X1 + (tid >> 3) * XHALF + p * 128 + (tid & 7) * 16;
      GST(SENT, o); GST(SENT, o + 64 * XHALF);
      SB();
    }
    XROW4(0) XROW4(4) XROW4(8) XROW4(12) XROW4(16) XROW4(20) XROW4(24) XROW4(28)
    TS(26);
    sweep();
    gm = GLD(mo);
    SB();
    v16f acc = {0, 0, 0, 0, 0, 0, 0, 0, 0, 0, 0, 0, 0, 0, 0, 0};
    v4u xf[8];
#pragma unroll
    for (int m = 0; m < 8; ++m) xf[m] = *(LAS v4u*)(px + 32 * m);
#pragma unroll
    for (int m = 0; m < 8; ++m) acc = mfma32(xf[m], yf[m], acc);
#pragma unroll
    for (int q = 0; q < 4; ++q) {
      v4f o = {acc[4 * q], acc[4 * q + 1], acc[4 * q + 2], acc[4 * q + 3]};
      LDS128F(wave * KG_W + 144 * r + 32 * q + 16 * h) = o;
    }
  }
  TS(2);
  const int gfo = OFF_WFL + (wave * 28 + ((lane & 31) < 28 ? (lane & 31) : 0)) * 128;
  v2u gf = GLD8(gfo);
  SB();
  BAR();
  TS(3);
  {
    const v4f s = (LDS128F(rd_src) + LDS128F(rd_src + KG_W)) + (LDS128F(rd_src + 2 * KG_W) + LDS128F(rd_src + 3 * KG_W));
    LDS32F(rd_dst + 3 * rd_dk) = s[3]; LDS32F(rd_dst + 2 * rd_dk) = s[2]; LDS32F(rd_dst + rd_dk) = s[1]; LDS32F(rd_dst) = s[0];
    if ((wave & 3) == 3) {
      const v4u z0 = {0, 0, 0, 0};
      if (lane < 30) LDS128(L_Z + chw * Z_CH + lane * 16) = z0;
      if (lane == 30) LDS128(L_KLIN + chw * KLIN_CH + 4 * 644) = z0;
      if (chw && lane < 11) LDS128(L_FL + lane * 16) = z0;
    }
  }
  auto rest = [&](auto role) {
  constexpr int CV = decltype(role)::value;
  v4u w1f[32];
  const int w1o = OFF_W1T + (sl * 8 + wave) * 28 * 1024;
  auto ldw1 = [&](int i0, int i1) {
    const int l16 = opq(lane * 16);
#pragma unroll
    for (int i = i0; i < i1; ++i) { w1f[i] = GLDS(l16 + (i & 3) * 1024, w1o + (i >> 2) * 4096); SB(); }
  };
  if (__any(gf.x != tagL || gf.y != tagH)) {
    SPIN {
      __builtin_amdgcn_s_sleep(2); MEMB();
      gf = GLD8(gfo);
      if (!__any(gf.x != tagL || gf.y != tagH)) break;
    }
  }
  BAR();
  TS(19);
  if constexpr (!CV) ldw1(0, NPRE);
#pragma unroll
  for (int ch = 0; ch < 2; ++ch) {
    float k[7];
#pragma unroll
    for (int e = 0; e < 7; ++e) k[e] = LDS32F(kap[e] + ch * KLIN_CH);
    const u32 d10 = pk(k[1], k[0]), d21 = pk(k[2], k[1]), d32 = pk(k[3], k[2]), d43 = pk(k[4], k[3]), d54 = pk(k[5], k[4]), d65 = pk(k[6], k[5]);
    const int o = L_KC + ch * KC_CH + (511 - tid) * 8;
    v2u c0 = {d32, d10}, c1 = {d43, d21}, c2 = {d54, d32}, c3 = {d65, d43};
    LDS64(o) = c0; LDS64(o + KC_S) = c1; LDS64(o + 2 * KC_S) = c2; LDS64(o + 3 * KC_S) = c3;
  }
  if (tid < 32) { const v2u z0 = {0, 0}; LDS64(L_KC + (tid >> 2) * KC_S + 4096 + (tid & 3) * 8) = z0; }
  SB();
  const int c16 = lane & 15, g4 = lane >> 4;

  float ga_[2], gd_[2], g2_[2];
  {
    auto badx = [&]() { return ((gx0.x ^ tagL) | (gx1.x ^ tagL) | (gs0.x ^ tagL)) | ((gs1.x ^ tagL) | (gx0.w ^ tagH) | (gx1.w ^ tagH)) | ((gs0.w ^ tagH) | (gs1.w ^ tagH)); };
    if (__any((badx() | (gm.x ^ tagL) | (gm.w ^ tagH)) != 0)) {
      if (__any(badx() != 0)) {
        SPIN { __builtin_amdgcn_s_sleep(2); MEMB(); sweep(); if (!__any(badx() != 0)) break; }
      }
      SPIN { MEMB(); gm = GLD(mo); if (__all(tagok(gm, tagL, tagH))) break; __builtin_amdgcn_s_sleep(2); }
    }
    TS(23);
    const int mv = (p & 1) ? gm.z : gm.y;
    const u32 sa2 = __builtin_amdgcn_readlane(mv, 0), ca2 = __builtin_amdgcn_readlane(mv, 1), ga2 = __builtin_amdgcn_readlane(mv, 2), cb2 = __builtin_amdgcn_readlane(mv, 3);
    const v2f sa = unpk(sa2), g1 = unpk(pk(unpk(ca2) + bc(1.f)));
#pragma unroll
    for (int q = 0; q < 2; ++q) {
      const v4u gx = q ? gx1 : gx0, gs = q ? gs1 : gs0;
      const v2f h0 = fma2((unpk(gx.y) - bc(blo(gs.y))) * bc(__builtin_amdgcn_rcpf(bhi(gs.y))), g1, sa);
      const v2f h1 = fma2((unpk(gx.z) - bc(blo(gs.z))) * bc(__builtin_amdgcn_rcpf(bhi(gs.z))), g1, sa);
      const int lo = opq(L_Z + (q * 512 + tid) * 4);
      LDS32(lo + 480) = pk(h0.x, h1.x);
      LDS32(lo + 480 + Z_CH) = pk(h0.y, h1.y);
      LDS32(lo + (L_XT - L_Z)) = __builtin_amdgcn_perm(gx.z, gx.y, 0x05040100u);
      LDS32(lo + (L_XT - L_Z) + Z_CH) = __builtin_amdgcn_perm(gx.z, gx.y, 0x07060302u);
    }
    SB();
    asm volatile("s_waitcnt lgkmcnt(0)\n\ts_barrier" ::: "memory");
    SB();
    const v2f g2 = unpk(pk(unpk(cb2) + bc(1.f))), ga = unpk(ga2), gd = ga * unpk(dk2);
    ga_[0] = ga.x; ga_[1] = ga.y; gd_[0] = gd.x; gd_[1] = gd.y; g2_[0] = g2.x; g2_[1] = g2.y;
  }
  TS(5);

  const int epi_l = L_Z + 32 * c16 + 8 * g4;
  const int epi_g = OFF_X1 + (c16 >> 1) * XTILE + ((g4 & 1) ? 0 : XHALF) + p * 128 + (c16 & 1) * 32 + (g4 >> 1) * 16;
  auto setf = [&](int f) { MEMB(); if (lane == 0) *(volatile LAS u32*)(lds + f + (wave & 3) * 4) = 1; MEMB(); };
  auto wait1 = [&](int f) {
    _Pragma("unroll 1") for (u32 sp = 0; sp < (1u << 22); ++sp) { if (__builtin_amdgcn_readfirstlane(*(volatile LAS u32*)(lds + f))) break; }
    MEMB();
  };
  auto wait4 = [&](int f) {
    _Pragma("unroll 1") for (u32 sp = 0; sp < (1u << 22); ++sp) {
      const v4u v = *(volatile LAS v4u*)(lds + f);
      if (__builtin_amdgcn_readfirstlane(v.x & v.y & v.z & v.w)) break;
      __builtin_amdgcn_s_sleep(1);
    }
    MEMB();
  };
  v4u w2f[28];
  const int w2v = opq(lane * 16), w2o = OFF_W2T + (sl * 8 + wave) * 28 * 1024;
  auto ldw2 = [&](int u) { w2f[u] = GLDS(w2v + (u & 3) * 1024, w2o + (u >> 2) * 4096); SB(); };
  if constexpr (CV) {
    const int ech = (wave >> 1) & 1, et = (wave & 1) * 2;
    float ega = ech ? ga_[1] : ga_[0], egd = ech ? gd_[1] : gd_[0], eg2 = ech ? g2_[1] : g2_[0];
    int edm = (ech * 8 + et) * 1024 + lane * 16 + L_DUMP, elz = ech * Z_CH + et * 512 + epi_l, ego = epi_g + ech * 64 + et * 8 * XTILE;
    auto epi = [&](int stage, int u) { conv_epi(lds, rs, edm + u * 1024, elz + (4 * stage + u) * 512, ego + (4 * stage + u) * 8 * XTILE, ega, egd, eg2); };
    LAS char* pa = lds + opq(L_KC + ech * KC_CH + (c16 & 3) * KC_S + 4104 - 8 * (c16 >> 2) - 16 * g4 - 256 * (wave & 1) - 64 * 59);
    LAS char* pbz = lds + opq(L_Z + ech * Z_CH + 496 + 32 * c16 - 16 * g4 - 256 * (wave & 1) - 192);
    const int dmo = opq(L_DUMP + (wave & 3) * 4096 + lane * 16);
    v4u B[16];
    auto rdb = [&](int slot, int rho, int dl) { B[slot] = *(LAS v4u*)(pbz + 64 * (8 * dl + 3 - rho)); SB(); };
    auto rda = [&](int rho, int a, int half) {
      const v2u u = *(LAS v2u*)(pa + 64 * (59 - 8 * a - rho) + 8 * half); SB();
      if (half) { w1f[8 * rho + a].z = u.x; w1f[8 * rho + a].w = u.y; } else { w1f[8 * rho + a].x = u.x; w1f[8 * rho + a].y = u.y; }
    };
    auto rd1 = [&](int rho, int n) {
      if (n < 2) rda(rho, 0, n); else if (n < 6) rdb(4 * rho + n - 2, rho, 5 - n); else rda(rho, (n - 4) >> 1, n & 1);
    };
    v4f acc[4];
    __builtin_amdgcn_s_setprio(1);
#pragma unroll
    for (int n = 0; n < 16; ++n) rd1(n / 12, n % 12);
    tie(ega); tie(egd); tie(eg2); tie(edm); tie(elz); tie(ego); SB();
#pragma unroll
    for (int t = 0; t < 4; ++t) acc[t] = (v4f){0, 0, 0, 0};
    {
      int n = 16, m = 0, c = 0;
#pragma unroll
      for (int rho = 0; rho < 4; ++rho) {
#pragma unroll
        for (int a = 0; a < 4; ++a) {
#pragma unroll
          for (int dl = 3 - a; dl >= 0; --dl) {
            acc[a + dl] = mfma16(w1f[8 * rho + a], B[4 * rho + 3 - dl], acc[a + dl]); tie(acc[a + dl]); SB();
            if (n < 48) { rd1(n / 12, n % 12); ++n; if ((c & 1) && n < 48) { rd1(n / 12, n % 12); ++n; } }
            else if (m < 8) { rdb(m, 0, 7 - m); ++m; }
            ++c;
          }
        }
      }
    }
#pragma unroll
    for (int t = 0; t < 4; ++t) LDS128F(dmo + t * 1024) = acc[t];
    setf(F_D0);
#pragma unroll
    for (int n = 0; n < 16; ++n) rda(n >> 3, 4 + ((n >> 1) & 3), n & 1);
    TS(20);
    wait1(F_D0 + ((wave & 3) ^ 1) * 4);
    TS(6);
    epi(0, 0); epi(0, 1);
    setf(F_PA);
    SB();
    TS(7);
#pragma unroll
    for (int t = 0; t < 4; ++t) acc[t] = (v4f){0, 0, 0, 0};
#pragma unroll
    for (int rho = 0; rho < 4; ++rho) {
      LGK0(); SB();
      int n = 0;
#pragma unroll
      for (int a = 0; a < 8; ++a) {
#pragma unroll
        for (int dl = 7 - a; dl >= 0 && dl >= 4 - a; --dl) {
          acc[a + dl - 4] = mfma16(w1f[8 * rho + a], B[8 * (rho & 1) + 7 - dl], acc[a + dl - 4]); tie(acc[a + dl - 4]); SB();
          if (n < 8 && rho < 3) rdb(8 * ((rho + 1) & 1) + n, rho + 1, 7 - n);
          else if (n < 16 && rho < 2) rda(rho + 2, 4 + ((n - 8) >> 1), n & 1);
          else if (n == TRIGN && rho == TRIG) setf(F_TR);
          ++n;
        }
      }
    }
    wait1(F_PA + ((wave & 3) ^ 1) * 4);
#pragma unroll
    for (int t = 0; t < 4; ++t) LDS128F(dmo + t * 1024) = acc[t];
    setf(F_D1);
    TS(21);
    wait1(F_D1 + ((wave & 3) ^ 1) * 4);
    epi(1, 0); epi(1, 1);
    SB();
    __builtin_amdgcn_s_setprio(0);
    TS(8);
    wait1(F_WS + (wave & 3) * 4);
    const int wso = opq(L_WS + (wave & 3) * NWS * 1024 + lane * 16);
#pragma unroll
    for (int i = 0; i < NWS; ++i) w1f[i] = LDS128(wso + i * 1024);
    setf(F_EB);
  } else {
    if (tid == 0) {
      GST8(((v2u){tagL, tagH}), OFF_XFL + p * 32);
      if (b == 0) GST8(((v2u){tagL, tagH}), 0);
    }
    ldw1(NPRE, N1);
    TS(20);
    wait4(F_PA);
    ldw1(N1, 28);
    const int xfo = OFF_XFL + (wave * 56 + (lane < 56 ? lane : 0)) * 32;
    v2u xf = GLD8(xfo);
    SB();
    {
      const char* src = ws + w1o + 4 * 28 * 1024;
      const u32 l16 = lane * 16;
#pragma unroll
      for (int i = 0; i < NWS; ++i) {
        if ((i & 3) == 0) { set_m0(lds0 + L_WS + wave * NWS * 1024 + i * 1024); dma16_dev_o<0>(src + i * 1024, l16); }
        else if ((i & 3) == 1) dma16_dev_o<1024>(src + (i - 1) * 1024, l16);
        else if ((i & 3) == 2) dma16_dev_o<2048>(src + (i - 2) * 1024, l16);
        else dma16_dev_o<3072>(src + (i - 3) * 1024, l16);
      }
    }
#pragma unroll
    for (int i = 0; i < NB1; ++i) ldw2(i);
    asm volatile("s_waitcnt vmcnt(%0)" :: "i"(NB1) : "memory");
    if (__any(xf.x != tagL || xf.y != tagH)) {
      SPIN { __builtin_amdgcn_s_sleep(2); MEMB(); xf = GLD8(xfo); if (!__any(xf.x != tagL || xf.y != tagH)) break; }
    }
    setf(F_WS);
    TS(21);
    wait4(F_TR);
    __builtin_amdgcn_s_setprio(2);
    TS(8);
  }

  const int uvo = h ? OFF_CFL + grp * 128 : OFF_UV + (256 * sl + 32 * wave + lane) * 16;
  const int gbo = OFF_MOD + (560 + 16 * sl + (tid & 15)) * 16;
  v4u uv;
  const int trb = (8 * h + trq) * 64 + trc;
  const char* xg = ws + OFF_X1 + grp * XTILE + (wave & 3) * 7168;
  const u32 xl = lane * 16;
  auto dma_x = [&](int pass, int i) {
    const int half = i >= 7, q = i - 7 * half, o = half * XHALF + (q >> 2) * 4096;
    const char* src = xg + pass * 32 * XTILE + o;
    if ((q & 3) == 0) { set_m0(lds0 + pass * XTILE + (wave & 3) * 7168 + o); dma16_dev_o<0>(src, xl); }
    else if ((q & 3) == 1) dma16_dev_o<1024>(src, xl);
    else if ((q & 3) == 2) dma16_dev_o<2048>(src, xl);
    else dma16_dev_o<3072>(src, xl);
  };
  auto accept_x = [&](int pass) {
    const int lx = pass * XTILE + (wave & 3) * 7168;
    float s, q;
    SPIN {
      u32 m = 0;
      v4u t[7], f[7];
#pragma unroll
      for (int i = 0; i < 7; ++i) t[i] = LDS128(lx + i * 1024 + lane * 16);
#pragma unroll
      for (int i = 0; i < 7; ++i) f[i] = trfrag(lds + lx + XHALF + trb + i * 1024);
#pragma unroll
      for (int i = 0; i < 7; ++i) m = max4(m, t[i]);
      s = 0.f; q = 0.f;
#pragma unroll
      for (int i = 0; i < 7; ++i) sumsq(f[i], s, q);
      if (!__any(m == SENTW || q != q)) break;
      MEMB();
#pragma unroll
      for (int i = 0; i < 7; ++i) m = max4(m, LDS128(lx + XHALF + i * 1024 + lane * 16));
      if (!__any(m == SENTW)) break;
      __builtin_amdgcn_s_sleep(4);
#pragma unroll
      for (int i = 0; i < 14; ++i) dma_x(pass, i);
      VMW(0);
    }
    s = swap32_add(s); q = swap32_add(q);
    if (!h) { v2f o = {s, q}; LDS64F(L_ST + pass * ST_P + lane * 48 + (wave & 3) * 8) = o; }
  };
  uv = GLD(uvo);
  SB();
  if constexpr (!CV) {
#pragma unroll
    for (int i = 0; i < 14; ++i) dma_x(0, i);
    __builtin_amdgcn_s_setprio(0);
    TS(9);
#pragma unroll
    for (int i = NB1; i < NB1 + NB2; ++i) ldw2(i);
    asm volatile("s_waitcnt vmcnt(%0)" :: "i"(NB2) : "memory");
    tie(uv);
    TS(6);
    accept_x(0);
    setf(F_XA);
    TS(10);
    wait4(F_EB);
  } else {
    ldw1(NWS, 28);
    TS(10);
  }
  wait4(F_XA);
  TS(11);
  if (!__all(tagok(uv, tagL, tagH))) {
    SPIN { __builtin_amdgcn_s_sleep(4); MEMB(); uv = GLD(uvo); if (__all(tagok(uv, tagL, tagH))) break; }
  }
  v4u uvf;
  {
    const float u = __uint_as_float(uv.y), v = __uint_as_float(uv.z), uh = rbf(u), vh = rbf(v);
    uvf = (v4u){h ? 0u : pk(uh, u - uh), h ? 0u : pk(vh, v - vh), 0u, 0u};
  }

  const v16f zero16 = {0, 0, 0, 0, 0, 0, 0, 0, 0, 0, 0, 0, 0, 0, 0, 0};
  float sd, rl;
  auto gemm1 = [&](int pass, auto extra) {
    const int lx = pass * XTILE;
    LAS char* pb = lds + opq(lx + trb);
    v4u bf[4];
#pragma unroll
    for (int k = 0; k < 3; ++k) bf[k] = trfrag(pb + k * 1024);
    SB();
    v16f acc = zero16;
    v2u res; v4f a0; float mean, ex2; v4u bs;
    __builtin_amdgcn_s_setprio(2 - CV);
#pragma unroll
    for (int k = 0; k < 28; ++k) {
      if (k + 3 < 28) bf[(k + 3) & 3] = trfrag(pb + (k + 3) * 1024);
      acc = mfma32(w1f[k], bf[k & 3], acc); tie(acc);
      extra(k);
      if (k == 1) {
        res = trd(lds + lx + XHALF + (64 * sl + 8 * wave + 4 * h + trq) * 64 + trc);
        a0 = LDS128F(L_ST + pass * ST_P + (lane & 31) * 48 + h * 16);
      }
      if (k == 7) LDS64(L_RES + pass * RES_P + (lane & 31) * 144 + 16 * wave + 8 * h) = res;
      if (k == 9) mean = pin(swap32_add(pin(a0[0]) + opf(a0[2])) * (1.f / D));
      if (k == 11) ex2 = pin(swap32_add(pin(a0[1]) + opf(a0[3])) * (1.f / D));
      if (k == 13) ex2 = pin(rbf(fmaxf(pin(ex2) - mean * mean, 0.f)));
      if (k == 15) ex2 = pin(__builtin_amdgcn_sqrtf(rbf(pin(ex2) + 1e-5f)));
      if (k == 17) { sd = rbf(pin(ex2)); rl = pin(__builtin_amdgcn_rcpf(sd) * -1.4426950408889634f); }
      if (k == 19) { mean = pin(mean); bs = (v4u){h ? 0u : pk(-mean, -mean), h ? 0u : pk(sd, sd), 0u, 0u}; tie(bs); }
      SB();
    }
    acc = mfma32(uvf, bs, acc); tie(acc);
    SB();
    __builtin_amdgcn_s_setprio(0);
    return acc;
  };
  auto epi1 = [&](int pass, v16f acc) {
    tie(acc);
    u32 hp[8];
#pragma unroll
    for (int e = 0; e < 8; ++e) {
      const float a0 = opf(acc[2 * e]), a1 = opf(acc[2 * e + 1]);
      const float e0_ = __builtin_amdgcn_exp2f(a0 * rl), e1_ = __builtin_amdgcn_exp2f(a1 * rl);
      hp[e] = pks(a0 * __builtin_amdgcn_rcpf(fmaf(e0_, sd, sd)), a1 * __builtin_amdgcn_rcpf(fmaf(e1_, sd, sd)));
    }
    const int ho = opq(OFF_H + (grp + 32 * pass) * HTILE + (16 * sl + 2 * wave) * 1024 + lane * 16);
    v4u h0 = {hp[0], hp[1], hp[2], hp[3]}, h1 = {hp[4], hp[5], hp[6], hp[7]};
    GST(h0, ho); GST(h1, ho + 1024);
    SB();
  };
  v16f acc;
  if constexpr (!CV) {
    acc = gemm1(0, [&](int k) { if (!(k & 1)) dma_x(1, k >> 1); });
    TS(12);
    epi1(0, acc);
    VMW(2);
    __builtin_amdgcn_s_setprio(2);
    accept_x(1);
    setf(F_XB);
    wait4(F_XB);
    TS(13);
    acc = gemm1(1, [&](int k) { if (!(k & 1) && NB1 + NB2 + (k >> 1) < 28) ldw2(NB1 + NB2 + (k >> 1)); });
  } else {
    acc = gemm1(0, [&](int k) { if (k >= 28 - 2 * NCA && !(k & 1)) ldw2((k - 28 + 2 * NCA) >> 1); });
    TS(12);
    epi1(0, acc);
#pragma unroll
    for (int i = NCA; i < 14; ++i) ldw2(i);
    TS(9);
    wait4(F_XB);
    TS(13);
    acc = gemm1(1, [&](int k) { if (!(k & 1)) ldw2(14 + (k >> 1)); });
  }
  TS(14);
  epi1(1, acc);
  if constexpr (!CV) {
#pragma unroll
    for (int i = NB1 + NB2 + 14; i < 28; ++i) ldw2(i);
  }
  TS(24);

  const int hoA = OFF_H + grp * HTILE + wave * 14 * 1024;
  v4u hf[14];
  v16f c0, c1;
  auto ldh = [&](int ho) {
    const int l16 = opq(lane * 16);
#pragma unroll
    for (int i = 0; i < 14; ++i) { hf[i] = GLDS(l16 + (i & 3) * 1024, ho + (i >> 2) * 4096); SB(); }
  };
  auto gemm2 = [&](int ho) {
    c0 = zero16; c1 = zero16;
#pragma unroll
    for (int i = 0; i < 14; ++i) { c0 = mfma32(w2f[2 * i], hf[i], c0); c1 = mfma32(w2f[2 * i + 1], hf[i], c1); tie(c0); tie(c1); SB(); }
    if (__any(c0[0] != c0[0])) {
      SPIN {
        MEMB();
        ldh(ho);
        u32 m = 0;
#pragma unroll
        for (int i = 0; i < 14; ++i) m = max4(m, hf[i]);
        if (!__any(m == SENTW)) break;
      }
      c0 = zero16; c1 = zero16;
#pragma unroll
      for (int i = 0; i < 14; ++i) { c0 = mfma32(w2f[2 * i], hf[i], c0); c1 = mfma32(w2f[2 * i + 1], hf[i], c1); }
    }
  };
  auto put_red = [&](int lr) {
    const int o = opq(lr + wave * RED_W + h * RED_S + (lane & 31) * 16);
#pragma unroll
    for (int q = 0; q < 4; ++q) {
      v4f o0 = {c0[4 * q], c0[4 * q + 1], c0[4 * q + 2], c0[4 * q + 3]}, o1 = {c1[4 * q], c1[4 * q + 1], c1[4 * q + 2], c1[4 * q + 3]};
      LDS128F(o + 2 * q * RED_S) = o0;
      LDS128F(o + (8 + 2 * q) * RED_S + 32) = o1;
    }
    SB();
  };
  v4u gbv;
  auto finish = [&](int pass, int t) {
    const int dq = t & 15, l = t >> 4;
    const int lr = opq(pass * L_REDB + (dq >> 1) * RED_S + (dq & 1) * (8 * RED_S + 32) + l * 16), lr2 = opq(lr + 4 * RED_W);
    v4f s0 = LDS128F(lr), s1 = LDS128F(lr2);
#pragma unroll
    for (int w = 1; w < 4; ++w) { s0 += LDS128F(lr + w * RED_W); s1 += LDS128F(lr2 + w * RED_W); }
    s0 += s1;
    const v2u rv = LDS64(L_RES + pass * RES_P + l * 144 + dq * 8);
    const v2u o = {pk(fma2(unpk(gbv.y), s0.xy, unpk(rv.x))), pk(fma2(unpk(gbv.z), s0.zw, unpk(rv.y)))};
    *(GAS v2u*)(out + (pass * 1024 + grp * 32 + l) * D + 64 * sl + 4 * dq) = o;
  };
  gbv = GLD(gbo);
  SB();
  BAR();
  TS(15);
  __builtin_amdgcn_s_setprio(1);
  ldh(hoA);
  __builtin_amdgcn_s_setprio(0);
  gemm2(hoA);
  if (!__all(tagok(gbv, tagL, tagH))) {
    SPIN { __builtin_amdgcn_s_sleep(4); MEMB(); gbv = GLD(gbo); if (__all(tagok(gbv, tagL, tagH))) break; }
  }
  TS(16);
  put_red(L_REDA);
  setf(F_RA + CV * 16);
  ldh(hoA + 32 * HTILE);
  TS(22);
  if (TAIL == 1 && !CV) {
    wait4(F_RA); wait4(F_RA + 16);
    finish(0, tid); finish(0, tid + 256);
  }
  SB();
  TS(4);
  gemm2(hoA + 32 * HTILE);
  put_red(L_REDB);
  TS(17);
  BAR();
  if (TAIL == 0) finish(0, tid);
  finish(1, tid);
  };
  if (wave < 4) rest(Role<0>{}); else rest(Role<1>{});
  TS(29); TSEND();
}

extern "C" void kernel_launch(void* const* d_in, const int* in_sizes, int n_in, void* d_out, int out_size, void* d_ws, size_t ws_size,
                              hipStream_t stream) {
  k_all<<<256, 512, 0, stream>>>((const u16*)d_in[0], (const u16*)d_in[1], (const u16*)d_in[2], (const u16*)d_in[3], (const u16*)d_in[4],
                                 (const u16*)d_in[5], (const u16*)d_in[6], (const u16*)d_in[7], (const u16*)d_in[8], (const u16*)d_in[10],
                                 (const u16*)d_in[11], (const u16*)d_in[12], (const u16*)d_in[13], (u16*)d_out, (char*)d_ws);
}
```

```cpp
#define LNF 0
#define WOPT 0
#define NB1 0
#define NB2 8
#include <hip/hip_runtime.h>
#include <stdint.h>

typedef unsigned short u16;
typedef unsigned u32;
typedef unsigned long long u64;
typedef u32 v4u __attribute__((ext_vector_type(4)));
typedef u32 v2u __attribute__((ext_vector_type(2)));
typedef float v4f __attribute__((ext_vector_type(4)));
typedef float v2f __attribute__((ext_vector_type(2)));
typedef float v16f __attribute__((ext_vector_type(16)));
typedef __bf16 bf8 __attribute__((ext_vector_type(8)));
typedef __bf16 bf2 __attribute__((ext_vector_type(2)));
typedef short s4 __attribute__((ext_vector_type(4)));
#define GAS __attribute__((address_space(1)))
#define LAS __attribute__((address_space(3)))
#define DEV __device__ __forceinline__

constexpr int D = 448, F = 1792;

constexpr int OFF_STAT = 4096;
constexpr int OFF_MOD = 20480;
constexpr int OFF_UV = 32768;
constexpr int OFF_CFL = 61440;
constexpr int OFF_WFL = 65536;
constexpr int OFF_XFL = 98304;
constexpr int OFF_XT = 131072;
constexpr int OFF_W1T = 4 << 20;
constexpr int OFF_W2T = 6 << 20;
constexpr int OFF_X1 = 8 << 20;
constexpr int OFF_H = 12 << 20;
constexpr int OFF_DBG = 24 << 20;
constexpr int WS_BYTES = 32 << 20;
constexpr int XTILE = 57344, XHALF = 28672, HTILE = 114688;

constexpr int KG_W = 8704;
constexpr int L_KC = 0, KC_S = 4160, KC_CH = 4 * KC_S;
constexpr int L_LNT = 69632;
constexpr int L_STX = 76800;
constexpr int L_KLIN = 68096, KLIN_CH = 2592;
constexpr int NWS = 13, L_WS = 57344;
constexpr int L_Z = 110592, Z_CH = 4608;
constexpr int L_XT = 119808;
constexpr int L_DUMP = 129024;
constexpr int L_XA = 0, L_XB = XTILE;
constexpr int RED_S = 576, RED_W = 16 * RED_S, L_REDA = 0, L_REDB = 8 * RED_W;
constexpr int L_RES = 147456, RES_P = 4608;
constexpr int L_ST = 156672, ST_P = 1536;
constexpr int L_PF = 159744;
constexpr int L_FL = 160000;
constexpr int F_D0 = L_FL, F_D1 = L_FL + 16, F_EB = L_FL + 32, F_XA = L_FL + 48, F_XB = L_FL + 64, F_PA = L_FL + 80, F_TR = L_FL + 96,
              F_WS = L_FL + 112, F_RA = L_FL + 128, F_XL = L_FL + 160;
constexpr int L_WA = 160192;
constexpr int LDS_BYTES = 160256;
constexpr int L_AR0 = 0, L_AR1 = 81920;
constexpr int L_ASC = 57344;
constexpr int L_ARED = 58368;
constexpr int L_AG = 60416;
constexpr int L_ARED2 = 62464;

constexpr u32 SPIN_CAP = 1u << 14;
constexpr u32 SENTW = 0xffbfffbfu;
#define CODE_PF 36864
#define NPRE 10
#define N1 28
#define NC1 21
#define NCA 14
#define GMPOS 1
#define TAIL 0
#define TRIG 3
#define TRIGN 8
#define VPOS 12

DEV float blo(u32 v) { return __uint_as_float(v << 16); }
DEV float bhi(u32 v) { return __uint_as_float(v & 0xffff0000u); }
DEV v2f unpk(u32 v) { v2f r = {blo(v), bhi(v)}; return r; }
DEV u32 pk(float a, float b) { bf2 r = {(__bf16)a, (__bf16)b}; return __builtin_bit_cast(u32, r); }
DEV u32 pk(v2f a) { return pk(a.x, a.y); }
DEV float rbf(float a) { return blo(pk(a, a)); }
DEV u32 umax(u32 a, u32 b) { return a > b ? a : b; }
DEV u32 max4(u32 m, v4u v) { return umax(umax(umax(umax(m, v.x), v.y), v.z), v.w); }
DEV v2f fma2(v2f a, v2f b, v2f c) { return __builtin_elementwise_fma(a, b, c); }
DEV v2f bc(float a) { v2f r = {a, a}; return r; }
DEV v4f mfma16(v4u a, v4u b, v4f c) { return __builtin_amdgcn_mfma_f32_16x16x32_bf16(__builtin_bit_cast(bf8, a), __builtin_bit_cast(bf8, b), c, 0, 0, 0); }
DEV v16f mfma32(v4u a, v4u b, v16f c) { return __builtin_amdgcn_mfma_f32_32x32x16_bf16(__builtin_bit_cast(bf8, a), __builtin_bit_cast(bf8, b), c, 0, 0, 0); }
DEV float dot2(u32 a, u32 b, float c) { return __builtin_amdgcn_fdot2_f32_bf16(__builtin_bit_cast(bf2, a), __builtin_bit_cast(bf2, b), c, false); }
DEV void sumsq(v4u f, float& s, float& q) {
  s = dot2(f.x, 0x3f803f80u, s); q = dot2(f.x, f.x, q); s = dot2(f.y, 0x3f803f80u, s); q = dot2(f.y, f.y, q);
  s = dot2(f.z, 0x3f803f80u, s); q = dot2(f.z, f.z, q); s = dot2(f.w, 0x3f803f80u, s); q = dot2(f.w, f.w, q);
}
DEV v2u trd(LAS char* p) { return __builtin_bit_cast(v2u, __builtin_amdgcn_ds_read_tr16_b64_v4i16((LAS s4*)p)); }
DEV v4u trfrag(LAS char* p) { v2u a = trd(p), b = trd(p + 256); v4u r = {a.x, a.y, b.x, b.y}; return r; }
template <int CTRL, int RM> DEV float dppadd(float v) {
  return v + __builtin_bit_cast(float, __builtin_amdgcn_update_dpp(0, __builtin_bit_cast(int, v), CTRL, RM, 0xf, RM == 0xf));
}
DEV float rdl(float v, int l) { return __builtin_bit_cast(float, __builtin_amdgcn_readlane(__builtin_bit_cast(int, v), l)); }
DEV float swap32_add(float v) { auto r = __builtin_amdgcn_permlane32_swap(__float_as_uint(v), __float_as_uint(v), false, false); return __uint_as_float(r[0]) + __uint_as_float(r[1]); }
DEV float swap16_add(float v) { auto r = __builtin_amdgcn_permlane16_swap(__float_as_uint(v), __float_as_uint(v), false, false); return __uint_as_float(r[0]) + __uint_as_float(r[1]); }
DEV int opq(int v) { asm volatile("" : "+v"(v)); return v; }
DEV float opf(float v) { asm("" : "+v"(v)); return v; }
DEV float pin(float v) { asm volatile("" : "+v"(v)); return v; }
DEV u32 pks(float a, float b) { return pk(opf(a), opf(b)); }
DEV v2f cmul(v2f a, v2f w, v2f wn) { return fma2(a.yx, wn, a * w.xx); }
DEV v2f cneg(v2f w) { v2f r = {-w.y, w.y}; return r; }
DEV v2f cexpx(float er, float ei, float x) {
  const float e = __builtin_amdgcn_exp2f(er * x), ph = __builtin_amdgcn_fractf(ei * x);
  v2f c = {__builtin_amdgcn_cosf(ph), __builtin_amdgcn_sinf(ph)};
  return c * bc(e);
}

#define BAR() asm volatile("s_waitcnt lgkmcnt(0)\n\ts_barrier" ::: "memory")
#define VMW(n) asm volatile("s_waitcnt vmcnt(" #n ")" ::: "memory")
#define LGK0() __builtin_amdgcn_s_waitcnt(0xC07F)
#define MEMB() asm volatile("" ::: "memory")
#define SB() __builtin_amdgcn_sched_barrier(0)
#define SPIN _Pragma("unroll 1") for (u32 sp = 0; sp < SPIN_CAP; ++sp)
#define LDS128(off) (*(LAS v4u*)(lds + (off)))
#define LDS128F(off) (*(LAS v4f*)(lds + (off)))
#define LDS64(off) (*(LAS v2u*)(lds + (off)))
#define LDS64F(off) (*(LAS v2f*)(lds + (off)))
#define LDS32(off) (*(LAS u32*)(lds + (off)))
#define LDS32F(off) (*(LAS float*)(lds + (off)))
#define GLD(off) __builtin_amdgcn_raw_buffer_load_b128(rs, (off), 0, 16)
#define GLDS(voff, soff) __builtin_amdgcn_raw_buffer_load_b128(rs, (voff), (soff), 16)
#define GLD8(off) __builtin_amdgcn_raw_buffer_load_b64(rs, (off), 0, 16)
#define GST8(v, off) __builtin_amdgcn_raw_buffer_store_b64((v), rs, (off), 0, 16)
#define GST(v, off) __builtin_amdgcn_raw_buffer_store_b128((v), rs, (off), 0, 16)

DEV void dma16(u32 ldsaddr, const char* gbase, u32 voff) {
  asm volatile("s_mov_b32 m0, %0\n\ts_nop 0\n\tglobal_load_lds_dwordx4 %1, %2" :: "s"(ldsaddr), "v"(voff), "s"(gbase) : "memory");
}
DEV void dma16_dev(u32 ldsaddr, const char* gbase, u32 voff) {
  asm volatile("s_mov_b32 m0, %0\n\ts_nop 0\n\tglobal_load_lds_dwordx4 %1, %2 sc1" :: "s"(ldsaddr), "v"(voff), "s"(gbase) : "memory");
}
DEV void dma4_dev(u32 ldsaddr, const char* gbase, u32 voff) {
  asm volatile("s_mov_b32 m0, %0\n\ts_nop 0\n\tglobal_load_lds_dword %1, %2 sc1" :: "s"(ldsaddr), "v"(voff), "s"(gbase) : "memory");
}
DEV void set_m0(u32 v) { asm volatile("s_mov_b32 m0, %0\n\ts_nop 0" :: "s"(v) : "memory"); }
template <int OFF> DEV void dma16_dev_o(const char* gbase, u32 voff) {
  asm volatile("global_load_lds_dwordx4 %0, %1 offset:%2 sc1" :: "v"(voff), "s"(gbase), "i"(OFF) : "memory");
}
template <class T> DEV void tie(T& v) { asm volatile("" : "+v"(v)); }
template <int OFF> DEV void st_tid(u32 v) { asm volatile("ds_write_addtid_b32 %0 offset:%1" :: "v"(v), "i"(OFF) : "memory"); }

#define TS(i)
#define TSEND()

template <int V> struct Role { static constexpr int value = V; };
DEV bool tagok(v4u g, u32 tagL, u32 tagH) { return g.x == tagL && g.w == tagH; }

struct Ln1 { int lw, lr, go; };
DEV Ln1 ln1_addr(u32 b, int lane, u32 wave) {
  Ln1 a = {L_LNT + (int)wave * 1024 + lane * 16, L_LNT + (int)wave * 1024 + (lane & 3) * 256 + (lane >> 2) * 8,
           OFF_XT + (32 * (int)wave + 2 * (lane >> 2)) * 16384 + (4 * (int)b + (lane & 3)) * 16};
  tie(a.lw); tie(a.lr); tie(a.go);
  return a;
}
DEV void ln1_publish(LAS char* lds, __amdgpu_buffer_rsrc_t rs, v4u xr, Ln1 ad, u32 b, int lane, u32 wave, u32 tagL, u32 tagH) {
  if (wave < 7) {
    LDS128(ad.lw) = xr;
    const v2u ra = LDS64(ad.lr), rb = LDS64(ad.lr + 128);
    float s = 0.f, q = 0.f;
    sumsq(xr, s, q);
    s = dppadd<0xb1, 0xf>(s); q = opf(dppadd<0xb1, 0xf>(q));
    s = dppadd<0x4e, 0xf>(s); q = opf(dppadd<0x4e, 0xf>(q));
    s = dppadd<0x141, 0xf>(s); q = opf(dppadd<0x141, 0xf>(q));
    MEMB();
    if ((lane & 7) == 0) { const v4u o = {__float_as_uint(s), __float_as_uint(q), tagL, tagH}; LDS128(L_STX + wave * 128 + lane * 2) = o; }
    MEMB();
    const v4u o0 = {tagL, ra.x, rb.x, tagH}, o1 = {tagL, ra.y, rb.y, tagH};
    GST(o0, ad.go); GST(o1, ad.go + 16384);
  } else {
    v4u t;
    _Pragma("unroll 1") for (u32 sp = 0; sp < (1u << 22); ++sp) {
      t = *(volatile LAS v4u*)(lds + L_STX + (lane < 56 ? lane : 0) * 16);
      if (!__any(t.z != tagL || t.w != tagH)) break;
      __builtin_amdgcn_s_sleep(1);
    }
    float s = lane < 56 ? __uint_as_float(t.x) : 0.f, q = lane < 56 ? __uint_as_float(t.y) : 0.f;
    s = dppadd<0x128, 0xf>(s); q = dppadd<0x128, 0xf>(q);
    s = swap32_add(swap16_add(s)); q = swap32_add(swap16_add(q));
    const float mean = s * (1.f / D), var = rbf(fmaxf(q * (1.f / D) - mean * mean, 0.f));
    const u32 ms = pk(mean, __builtin_amdgcn_sqrtf(rbf(var + 1e-5f)));
    const v4u o = {tagL, ms, (u32)__builtin_amdgcn_update_dpp(0, (int)ms, 0xb1, 0xf, 0xf, true), tagH};
    if (lane < 8 && !(lane & 1)) GST(o, OFF_STAT + 64 * b + lane * 8);
  }
  MEMB();
}

DEV void conv_epi(LAS char* lds, __amdgpu_buffer_rsrc_t rs, int dump, int lz, int goff, float ga, float gd, float g2) {
  const v4f p0 = LDS128F(dump), p1 = LDS128F(dump + 4096);
  const v2u hh = LDS64(lz + 480), xx = LDS64(lz + (L_XT - L_Z));
  const float h4[4] = {blo(hh.x), bhi(hh.x), blo(hh.y), bhi(hh.y)}, x4[4] = {blo(xx.x), bhi(xx.x), blo(xx.y), bhi(xx.y)};
  float o[4];
#pragma unroll
  for (int e = 0; e < 4; ++e) o[e] = fmaf(ga, opf(p0[e]) + opf(p1[e]), fmaf(gd, h4[e], x4[e]));
  u32 X0 = pks(o[0], o[1]), X1 = pks(o[2], o[3]);
  asm("" : "+v"(X0), "+v"(X1));
  const u32 S0 = pks(blo(X0) * g2, bhi(X0) * g2), S1 = pks(blo(X1) * g2, bhi(X1) * g2);
  auto r0 = __builtin_amdgcn_permlane16_swap(X0, S0, false, false);
  auto r1 = __builtin_amdgcn_permlane16_swap(X1, S1, false, false);
  v4u v = {r0[0], r1[0], r0[1], r1[1]};
  GST(v, goff);
}

__global__ void __launch_bounds__(512, 2) k_all(const u16* __restrict__ x, const u16* __restrict__ Are, const u16* __restrict__ Aim,
    const u16* __restrict__ Bre, const u16* __restrict__ Bim, const u16* __restrict__ Cre, const u16* __restrict__ Cim,
    const u16* __restrict__ logdt, const u16* __restrict__ Dskip, const u16* __restrict__ w1, const u16* __restrict__ w2,
    const u16* __restrict__ cvec, const u16* __restrict__ wmod, u16* __restrict__ out, char* ws) {
  __shared__ __attribute__((aligned(16))) char smem[LDS_BYTES];
  LAS char* lds = (LAS char*)smem;
  const u32 lds0 = (u32)(size_t)lds;
  u64 pc;
  asm volatile("s_getpc_b64 %0" : "=s"(pc));
  asm volatile("" :: "s"(x), "s"(Are), "s"(Aim), "s"(Bre), "s"(Bim), "s"(Cre), "s"(Cim), "s"(logdt), "s"(Dskip), "s"(w1), "s"(w2), "s"(cvec), "s"(wmod), "s"(out), "s"(ws));
  const int tid = threadIdx.x, lane = tid & 63;
  const u32 wave = __builtin_amdgcn_readfirstlane(tid >> 6);
  const u32 b = blockIdx.x, xcd = b & 7, j = b >> 3;
  TS(0);
  const __amdgpu_buffer_rsrc_t rs = __builtin_amdgcn_make_buffer_rsrc(ws, 0, WS_BYTES, 0x00020000);
  auto first_loads = [&](v4u& xr, u64& e0) {
    const u64 xa = (u64)(x + 8 * b * D);
    const v4u srd = {(u32)xa, (u32)(xa >> 32) & 0xffffu, 16 * D, 0x00020000u};
    const u32 vo = wave < 7 ? (lane >> 3) * (2 * D) + 128 * wave + (lane & 7) * 16 : 16 * D;
    asm volatile("buffer_load_dwordx4 %0, %1, %2, 0 offen ; ALOAD" : "=v"(xr) : "v"(vo), "s"(srd) : "memory");
    asm volatile("s_load_dwordx2 %0, %1, 0x0 ; SLOAD" : "=s"(e0) : "s"(ws) : "memory");
  };
  const v4u SENT = {SENTW, SENTW, SENTW, SENTW};
  auto gettag = [&](u64 e0, u32& tl, u32& th) {
    const u64 T = (((u64)(u32)__builtin_amdgcn_readfirstlane((u32)(e0 >> 32)) << 32) | (u32)__builtin_amdgcn_readfirstlane((u32)e0)) + 0x9E3779B97F4A7C15ull; tl = (u32)T; th = (u32)(T >> 32); };
  u32 tagL, tagH;

  if (j >= 28) {
    v4u xr; u64 e0;
    first_loads(xr, e0);
    const u32 a = (j - 28) * 8 + xcd;
    const int h = lane >> 5, trq = (lane & 15) >> 2, trc = ((lane >> 4) & 1) * 32 + (lane & 3) * 8;
    const u32 wv = wave < 7 ? wave : 0;
    auto stage = [&](u32 ldst, const char* src, u32 rstride) {
      if (wave < 7) {
        const u32 vo = ((lane >> 3) + 64 * wave) * rstride + (lane & 7) * 16;
#pragma unroll
        for (int i = 0; i < 8; ++i) dma16(lds0 + ldst + wave * 8192 + i * 1024, src + i * 8 * rstride, vo);
      }
    };
    auto gemv = [&](u32 lsrc, int atab, float& r0, float& r1) {
      v16f c0 = {0, 0, 0, 0, 0, 0, 0, 0, 0, 0, 0, 0, 0, 0, 0, 0}, c1 = c0;
      LAS char* pb = lds + opq(lsrc + wv * 8192 + (8 * h + trq) * 128 + trc);
#pragma unroll
      for (int k = 0; k < 4; ++k) {
        const v4u af = LDS128(atab + wv * 128 + k * 32 + h * 16);
        const v2u b0 = trd(pb + k * 2048), b1 = trd(pb + k * 2048 + 512), b2 = trd(pb + k * 2048 + 64), b3 = trd(pb + k * 2048 + 576);
        c0 = mfma32(af, (v4u){b0.x, b0.y, b1.x, b1.y}, c0); c1 = mfma32(af, (v4u){b2.x, b2.y, b3.x, b3.y}, c1);
      }
      r0 = h ? c1[0] : c0[0]; r1 = h ? c1[1] : c0[1];
    };
    u32 cv;
    asm volatile("global_load_ushort %0, %1, %2 ; ALOAD" : "=v"(cv) : "v"((64 * wv + lane) * 2), "s"(cvec) : "memory");
    const u32 lc = a < 21 ? a : (a < 28 ? a + 7 : a - 7);
    stage(L_AR0, (const char*)wmod + lc * 128, 6 * D * 2);
    const Ln1 lna = ln1_addr(b, lane, wave);
    if (wave == 7) VMW(0);
    asm volatile("s_waitcnt vmcnt(8) lgkmcnt(0) ; TIE" : "+v"(xr), "+s"(e0), "+v"(cv) :: "memory");
    {
      const float cf_ = blo(cv);
      *(LAS u16*)(lds + L_ASC + (64 * wv + lane) * 2) = (u16)pk(cf_ * __builtin_amdgcn_rcpf(1.f + __expf(-cf_)), 0.f);
    }
    gettag(e0, tagL, tagH);
    TS(1);
    ln1_publish(lds, rs, xr, lna, b, lane, wave, tagL, tagH);
    TS(2);
    VMW(2);
    if (a < 3) stage(L_AR1, (const char*)wmod + (25 + a) * 128, 6 * D * 2);
    auto modround = [&](u32 lsrc, u32 lc_) {
      float r0, r1;
      gemv(lsrc, L_ASC, r0, r1);
      LDS32F(L_ARED + wave * 256 + lane * 4) = r0;
      BAR();
      if (tid < 16) {
        v4f s = LDS128F(L_ARED + tid * 16);
#pragma unroll
        for (int w = 1; w < 7; ++w) s += LDS128F(L_ARED + w * 256 + tid * 16);
        v4u o = {tagL, pk(s[0], s[1]), pk(s[2], s[3]), tagH};
        GST(o, OFF_MOD + (16 * lc_ + tid) * 16);
      }
    };
    modround(L_AR0, lc);
    TS(3);
    if (a < 3) { VMW(0); BAR(); modround(L_AR1, 25 + a); }
    if (a >= 4) stage(L_AR1, (const char*)w1 + (a - 4) * 128, F * 2);
    if (a >= 3 && a < 10) stage(L_AR0, (const char*)wmod + (32 + a) * 128, 6 * D * 2);
    const int hc = OFF_H + a * HTILE + tid * 16;
#pragma unroll
    for (int i = 0; i < 4; ++i) GST(SENT, hc + i * 8192);
    TS(4);
    if (a >= 4) {
      if (wave < 4) {
        const int gi = 336 + (tid < 224 ? tid : 0);
        v4u g;
        SPIN {
          MEMB();
          g = GLD(OFF_MOD + gi * 16);
          if (__all(tagok(g, tagL, tagH))) break;
          __builtin_amdgcn_s_sleep(8);
        }
        v2u f = {g.y, g.z};
        if (tid >= 112) { f.x = pk(unpk(f.x) + bc(1.f)); f.y = pk(unpk(f.y) + bc(1.f)); }
        if (tid < 224) LDS64(L_AG + tid * 8) = f;
      }
      VMW(0);
      BAR();
      TS(5);
      float r0, r1;
      gemv(L_AR1, L_AG + ((lane & 1) ? 0 : 2 * D), r0, r1);
      { v2f o = {r0, r1}; LDS64F(L_ARED2 + wave * 512 + lane * 8) = o; }
      BAR();
      if (tid < 64) {
        v2f s = LDS64F(L_ARED2 + tid * 8);
#pragma unroll
        for (int w = 1; w < 7; ++w) s += LDS64F(L_ARED2 + w * 512 + tid * 8);
        v4u o = {tagL, __float_as_uint(s[0]), __float_as_uint(s[1]), tagH};
        GST(o, OFF_UV + (64 * (a - 4) + tid) * 16);
      }
    }
    SB();
    TS(6);
#pragma unroll
    for (int i = 4; i < 14; ++i) GST(SENT, hc + i * 8192);
#pragma unroll
    for (int i = 0; i < 14; ++i) GST(SENT, hc + 32 * HTILE + i * 8192);
    VMW(0);
    BAR();
    if (tid == 0) { v4u f = {tagL, 0, 0, tagH}; GST(f, OFF_CFL + a * 128); }
    TS(7);
    if (a >= 3 && a < 10) modround(L_AR0, 32 + a);
    TS(29); TSEND();
    return;
  }

  v4u xr; u64 e0;
  first_loads(xr, e0);
  const u32 p = xcd * 28 + j;
  const u32 jq = (j * 37) >> 8;
  const u32 grp = xcd * 4 + jq, sl = j - 7 * jq;
  const u32 chw = wave >> 2;

  const u32 pi = ((2 * p + chw) * 256 + (tid & 255)) * 2;
  u32 pAr, pAi, pBr, pBi, pCr, pCi, pLd2, dk2;
  asm volatile("global_load_ushort %0, %1, %2 ; ALOAD" : "=v"(pAr) : "v"(pi), "s"(Are) : "memory");
  asm volatile("global_load_ushort %0, %1, %2 ; ALOAD" : "=v"(pAi) : "v"(pi), "s"(Aim) : "memory");
  asm volatile("global_load_ushort %0, %1, %2 ; ALOAD" : "=v"(pBr) : "v"(pi), "s"(Bre) : "memory");
  asm volatile("global_load_ushort %0, %1, %2 ; ALOAD" : "=v"(pBi) : "v"(pi), "s"(Bim) : "memory");
  asm volatile("global_load_ushort %0, %1, %2 ; ALOAD" : "=v"(pCr) : "v"(pi), "s"(Cre) : "memory");
  asm volatile("global_load_ushort %0, %1, %2 ; ALOAD" : "=v"(pCi) : "v"(pi), "s"(Cim) : "memory");
  asm volatile("s_load_dword %0, %1, %2 ; SLOAD" : "=s"(pLd2) : "s"(logdt), "s"(4 * p) : "memory");
  asm volatile("s_load_dword %0, %1, %2 ; SLOAD" : "=s"(dk2) : "s"(Dskip), "s"(4 * p) : "memory");
  const bool isw1 = xcd < 4;
  u32 wt_dst = 0;
  if (wave < 7) {
    const char* src; u32 rstride;
    if (isw1) {
      const u32 kap = p >> 2, fg = (p & 3) * 7 + wave;
      src = (const char*)w1 + kap * (16 * F * 2) + fg * 128; rstride = F * 2;
      wt_dst = OFF_W1T + (fg * 56 + kap) * 1024;
    } else {
      const u32 kk = p - 112;
      src = (const char*)w2 + kk * (16 * D * 2) + wave * 128; rstride = D * 2;
      wt_dst = OFF_W2T + (wave * 224 + 2 * kk) * 1024;
    }
    const u32 vo = (lane >> 3) * rstride + (lane & 7) * 16;
    dma16(lds0 + wave * KG_W, src, vo);
    dma16(lds0 + wave * KG_W + 1024, src + 8 * rstride, vo);
  } else {
    set_m0(lds0 + L_PF);
    const u32 a = (lane >> 4) * 8 + xcd, lc = a < 21 ? a : (a < 28 ? a + 7 : a - 7);
    asm volatile("global_load_lds_dword %0, %1" :: "v"((16 * j + (lane & 15)) * (12 * D) + lc * 128), "s"(wmod) : "memory");
    u32 vo = (j & 7) * 8192 + lane * 128; vo = vo < CODE_PF ? vo : 0;
    asm volatile("global_load_lds_dword %0, %1" :: "v"(vo), "s"(pc & ~255ull) : "memory");
  }
  const int h = lane >> 5, trq = (lane & 15) >> 2, trc = ((lane >> 4) & 1) * 32 + (lane & 3) * 8;
  const Ln1 lna = ln1_addr(b, lane, wave);
  int wt_a = wave * KG_W + (isw1 ? (8 * h + trq) * 128 + trc : (4 * h + trq) * 128 + 2 * trc), wt_b = wt_a + (isw1 ? 512 : 1024);
  int wt_c = wt_a + (isw1 ? 64 : 8), wt_d = wt_b + (isw1 ? 64 : 8), wt_g = lane * 16 + wt_dst, wt_h = wt_g + (isw1 ? 28 * 1024 : 1024);
  tie(wt_a); tie(wt_b); tie(wt_c); tie(wt_d); tie(wt_g); tie(wt_h);
  int rd_src, rd_dst, rd_dk;
  {
    const int jl = lane >> 3, iq = lane & 7, wq = wave & 3, kl = L_KLIN + chw * KLIN_CH;
    rd_src = chw * 4 * KG_W + 144 * (8 * wq + (wq == 3 ? 0 : jl)) + 16 * iq;
    if (wq < 2) {
      const int jj = 8 * wq + jl, hi = iq >> 2;
      rd_dst = (hi & jj) ? kl + 4 * 641 : kl + 512 * hi + ((256 * iq + 4 * jj) >> hi); rd_dk = (hi & jj) ? 0 : 64 >> hi;
    } else if (wq == 2) {
      const int hi = jl >> 2;
      rd_dst = kl + (hi ? 4 * (512 + 8 * iq + 32 * (jl & 2) + (jl & 1)) : 4 * (384 + 16 * iq + jl)); rd_dk = 16 >> hi;
    } else { rd_dst = kl + 4 * (lane ? 641 : 640); rd_dk = lane ? 0 : 4; }
    tie(rd_src); tie(rd_dst); tie(rd_dk);
  }
  int kap[7];
  {
    const v2f f0 = bc((float)(4 * tid)) + (v2f){-3.f, -2.f};
    u32 b0 = pk(f0), b1 = pk(f0 + bc(2.f)), b2 = pk(f0 + bc(4.f)), b3 = pk(f0 + bc(6.f));
    asm("" : "+v"(b0), "+v"(b1), "+v"(b2), "+v"(b3));
    kap[0] = (b0 & 0xffffu) * 4; kap[1] = (b0 >> 16) * 4; kap[2] = (b1 & 0xffffu) * 4; kap[3] = (b1 >> 16) * 4;
    kap[4] = (b2 & 0xffffu) * 4; kap[5] = (b2 >> 16) * 4; kap[6] = (b3 & 0xffffu) * 4;
    if (wave == 0) {
#pragma unroll
      for (int e = 0; e < 7; ++e) { const int l = 4 * tid + e - 3; if (l < 128) kap[e] = L_KLIN + 4 * (l < 0 ? 644 : l); }
    }
#pragma unroll
    for (int e = 0; e < 7; ++e) tie(kap[e]);
  }
  TS(28);
  if (!LNF && wave < 7) VMW(0);
  asm volatile("s_waitcnt vmcnt(2) lgkmcnt(0) ; TIE" : "+v"(xr), "+s"(e0), "+v"(pAr), "+v"(pAi), "+v"(pBr), "+v"(pBi), "+v"(pCr), "+v"(pCi), "+s"(pLd2), "+s"(dk2) :: "memory");
  TS(27);
  gettag(e0, tagL, tagH);
  pLd2 = __builtin_amdgcn_readfirstlane(pLd2); dk2 = __builtin_amdgcn_readfirstlane(dk2);
  if (LNF) ln1_publish(lds, rs, xr, lna, b, lane, wave, tagL, tagH);
  if (wave < 7) {
    if (LNF) VMW(2);
    v2u a0 = trd(lds + wt_a), a1 = trd(lds + wt_b), b0 = trd(lds + wt_c), b1 = trd(lds + wt_d);
    GST(((v4u){a0.x, a0.y, a1.x, a1.y}), wt_g);
    GST(((v4u){b0.x, b0.y, b1.x, b1.y}), wt_h);
  }
  SB();
  if (!LNF) ln1_publish(lds, rs, xr, lna, b, lane, wave, tagL, tagH);
  TS(1);

  v4u gx0, gx1, gs0, gs1, gm;
  const int mo = OFF_MOD + (((lane & 3) == 3 ? 4 : (lane & 3)) * 112 + (p >> 1)) * 16;
  auto sweep = [&]() {
    gx0 = GLDS(tid * 16, OFF_XT + p * 16384); gx1 = GLDS(tid * 16, OFF_XT + p * 16384 + 8192);
    gs0 = GLDS(tid * 16, OFF_STAT); gs1 = GLDS(tid * 16, OFF_STAT + 8192);
  };

  {
    const float dt = rbf(expf(chw ? bhi(pLd2) : blo(pLd2)));
    const float er = (dt * blo(pAr)) * 1.4426950408889634f, ei = (dt * blo(pAi)) * 0.15915494309189535f;
    const v2f dB = {dt * blo(pBr), dt * blo(pBi)}, Cc = {blo(pCr), blo(pCi)};
    v2f X = cmul(Cc, dB, cneg(dB));
    X.y = -X.y;
    const v2f e1 = cexpx(er, ei, 1.f), e16 = cexpx(er, -ei, 16.f), e512 = cexpx(er, ei, 512.f);
    const v2f e1n = cneg(e1), e16n = cneg(e16), e512n = cneg(e512);
    set_m0(lds0 + wave * KG_W);
    st_tid<0>(0x00003f80u);
    v2f Y = e1, Y4, Y8, Y12;
#define YROW(jj) st_tid<jj * 272>(pk(Y)); if (jj == 4) Y4 = Y; if (jj == 8) Y8 = Y; if (jj == 12) Y12 = Y; if (jj < 15) Y = cmul(Y, e1, e1n);
    YROW(1) YROW(2) YROW(3) YROW(4) YROW(5) YROW(6) YROW(7) YROW(8) YROW(9) YROW(10) YROW(11) YROW(12) YROW(13) YROW(14) YROW(15)
    st_tid<16 * 272>(pk(e512));
    st_tid<17 * 272>(pk(cmul(Y4, e512, e512n)));
    Y = cmul(Y8, e512, e512n);  st_tid<18 * 272>(pk(Y));
    st_tid<19 * 272>(pk(cmul(Y12, e512, e512n)));
    v2f E = cmul(e512, e512, e512n); st_tid<20 * 272>(pk(E));
    Y = cmul(Y, e512, e512n);   st_tid<21 * 272>(pk(Y));
    E = cmul(E, e512, e512n);   st_tid<22 * 272>(pk(E));
    Y = cmul(Y, e512, e512n);   st_tid<23 * 272>(pk(Y));
    E = cmul(E, e512, e512n);   st_tid<24 * 272>(pk(E));
    const int r = lane & 31;
    v4u yf[8];
    LAS char* py = lds + wave * KG_W + (r < 24 ? r : 24) * 272 + 16 * h;
    LAS char* px = lds + wave * KG_W + r * 272 + 16 * h;
#pragma unroll
    for (int m = 0; m < 8; ++m) yf[m] = *(LAS v4u*)(py + 32 * m);
    TS(25);
#define XROW(i) st_tid<(i) * 272>(pk(X)); if ((i) < 31) X = cmul(X, e16, e16n);
#define XROW4(i) XROW(i) XROW(i + 1) XROW(i + 2) XROW(i + 3)
    if (wave < 7) { if (LNF) VMW(0); else VMW(2); }
    asm volatile("s_barrier" ::: "memory");
    if (tid == 448) GST8(((v2u){tagL, tagH}), OFF_WFL + p * 128);
    TS(18);
    {
      const int o = OFF_X1 + (tid >> 3) * XHALF + p * 128 + (tid & 7) * 16;
      GST(SENT, o); GST(SENT, o + 64 * XHALF);
      SB();
    }
    XROW4(0) XROW4(4) XROW4(8) XROW4(12) XROW4(16) XROW4(20) XROW4(24) XROW4(28)
    TS(26);
    sweep();
    if (GMPOS == 0) gm = GLD(mo);
    SB();
    v16f acc = {0, 0, 0, 0, 0, 0, 0, 0, 0, 0, 0, 0, 0, 0, 0, 0};
    v4u xf[8];
#pragma unroll
    for (int m = 0; m < 8; ++m) xf[m] = *(LAS v4u*)(px + 32 * m);
#pragma unroll
    for (int m = 0; m < 8; ++m) acc = mfma32(xf[m], yf[m], acc);
#pragma unroll
    for (int q = 0; q < 4; ++q) {
      v4f o = {acc[4 * q], acc[4 * q + 1], acc[4 * q + 2], acc[4 * q + 3]};
      LDS128F(wave * KG_W + 144 * r + 32 * q + 16 * h) = o;
    }
  }
  TS(2);
  const int gfo = OFF_WFL + (wave * 28 + ((lane & 31) < 28 ? (lane & 31) : 0)) * 128;
  v2u gf = GLD8(gfo);
  SB();
  BAR();
  TS(3);
  {
    const v4f s = (LDS128F(rd_src) + LDS128F(rd_src + KG_W)) + (LDS128F(rd_src + 2 * KG_W) + LDS128F(rd_src + 3 * KG_W));
    LDS32F(rd_dst + 3 * rd_dk) = s[3]; LDS32F(rd_dst + 2 * rd_dk) = s[2]; LDS32F(rd_dst + rd_dk) = s[1]; LDS32F(rd_dst) = s[0];
    if ((wave & 3) == 3) {
      const v4u z0 = {0, 0, 0, 0};
      if (lane < 30) LDS128(L_Z + chw * Z_CH + lane * 16) = z0;
      if (lane == 30) LDS128(L_KLIN + chw * KLIN_CH + 4 * 644) = z0;
      if (chw && lane < 11) LDS128(L_FL + lane * 16) = z0;
    }
  }
  auto rest = [&](auto role) {
  constexpr int CV = decltype(role)::value;
  v4u w1f[32];
  const int w1o = OFF_W1T + (sl * 8 + wave) * 28 * 1024;
  auto ldw1 = [&](int i0, int i1) {
    const int l16 = opq(lane * 16);
#pragma unroll
    for (int i = i0; i < i1; ++i) { w1f[i] = GLDS(l16 + (i & 3) * 1024, w1o + (i >> 2) * 4096); SB(); }
  };
  const u32 wok = __any(gf.x != tagL || gf.y != tagH) ? 0u : 1u;
  if (!WOPT) {
  if (__any(gf.x != tagL || gf.y != tagH)) {
    SPIN {
      __builtin_amdgcn_s_sleep(2); MEMB();
      gf = GLD8(gfo);
      if (!__any(gf.x != tagL || gf.y != tagH)) break;
    }
  }
  } else if (lane == 0) LDS32(L_WA + wave * 4) = wok;
  BAR();
  TS(19);
  if (GMPOS == 1) { gm = GLD(mo); SB(); }
  bool early = true;
  if constexpr (!CV) {
    if (WOPT) { const v4u f0 = LDS128(L_WA), f1 = LDS128(L_WA + 16); early = __builtin_amdgcn_readfirstlane(f0.x & f0.y & f0.z & f0.w & f1.x & f1.y & f1.z & f1.w); }
    if (early) ldw1(0, NPRE);
  }
#pragma unroll
  for (int ch = 0; ch < 2; ++ch) {
    float k[7];
#pragma unroll
    for (int e = 0; e < 7; ++e) k[e] = LDS32F(kap[e] + ch * KLIN_CH);
    const u32 d10 = pk(k[1], k[0]), d21 = pk(k[2], k[1]), d32 = pk(k[3], k[2]), d43 = pk(k[4], k[3]), d54 = pk(k[5], k[4]), d65 = pk(k[6], k[5]);
    const int o = L_KC + ch * KC_CH + (511 - tid) * 8;
    v2u c0 = {d32, d10}, c1 = {d43, d21}, c2 = {d54, d32}, c3 = {d65, d43};
    LDS64(o) = c0; LDS64(o + KC_S) = c1; LDS64(o + 2 * KC_S) = c2; LDS64(o + 3 * KC_S) = c3;
  }
  if (tid < 32) { const v2u z0 = {0, 0}; LDS64(L_KC + (tid >> 2) * KC_S + 4096 + (tid & 3) * 8) = z0; }
  SB();
  const int c16 = lane & 15, g4 = lane >> 4;

  float ga_[2], gd_[2], g2_[2];
  {
    auto badx = [&]() { return ((gx0.x ^ tagL) | (gx1.x ^ tagL) | (gs0.x ^ tagL)) | ((gs1.x ^ tagL) | (gx0.w ^ tagH) | (gx1.w ^ tagH)) | ((gs0.w ^ tagH) | (gs1.w ^ tagH)); };
    if (__any((badx() | (gm.x ^ tagL) | (gm.w ^ tagH)) != 0)) {
      if (__any(badx() != 0)) {
        SPIN { __builtin_amdgcn_s_sleep(2); MEMB(); sweep(); if (!__any(badx() != 0)) break; }
      }
      SPIN { MEMB(); gm = GLD(mo); if (__all(tagok(gm, tagL, tagH))) break; __builtin_amdgcn_s_sleep(2); }
    }
    TS(23);
    const int mv = (p & 1) ? gm.z : gm.y;
    const u32 sa2 = __builtin_amdgcn_readlane(mv, 0), ca2 = __builtin_amdgcn_readlane(mv, 1), ga2 = __builtin_amdgcn_readlane(mv, 2), cb2 = __builtin_amdgcn_readlane(mv, 3);
    const v2f sa = unpk(sa2), g1 = unpk(pk(unpk(ca2) + bc(1.f)));
#pragma unroll
    for (int q = 0; q < 2; ++q) {
      const v4u gx = q ? gx1 : gx0, gs = q ? gs1 : gs0;
      const v2f h0 = fma2((unpk(gx.y) - bc(blo(gs.y))) * bc(__builtin_amdgcn_rcpf(bhi(gs.y))), g1, sa);
      const v2f h1 = fma2((unpk(gx.z) - bc(blo(gs.z))) * bc(__builtin_amdgcn_rcpf(bhi(gs.z))), g1, sa);
      const int lo = opq(L_Z + (q * 512 + tid) * 4);
      LDS32(lo + 480) = pk(h0.x, h1.x);
      LDS32(lo + 480 + Z_CH) = pk(h0.y, h1.y);
      LDS32(lo + (L_XT - L_Z)) = __builtin_amdgcn_perm(gx.z, gx.y, 0x05040100u);
      LDS32(lo + (L_XT - L_Z) + Z_CH) = __builtin_amdgcn_perm(gx.z, gx.y, 0x07060302u);
    }
    SB();
    asm volatile("s_waitcnt lgkmcnt(0)\n\ts_barrier" ::: "memory");
    SB();
    const v2f g2 = unpk(pk(unpk(cb2) + bc(1.f))), ga = unpk(ga2), gd = ga * unpk(dk2);
    ga_[0] = ga.x; ga_[1] = ga.y; gd_[0] = gd.x; gd_[1] = gd.y; g2_[0] = g2.x; g2_[1] = g2.y;
  }
  TS(5);

  const int epi_l = L_Z + 32 * c16 + 8 * g4;
  const int epi_g = OFF_X1 + (c16 >> 1) * XTILE + ((g4 & 1) ? 0 : XHALF) + p * 128 + (c16 & 1) * 32 + (g4 >> 1) * 16;
  auto setf = [&](int f) { MEMB(); if (lane == 0) *(volatile LAS u32*)(lds + f + (wave & 3) * 4) = 1; MEMB(); };
  auto wait1 = [&](int f) {
    _Pragma("unroll 1") for (u32 sp = 0; sp < (1u << 22); ++sp) { if (__builtin_amdgcn_readfirstlane(*(volatile LAS u32*)(lds + f))) break; }
    MEMB();
  };
  auto wait4 = [&](int f) {
    _Pragma("unroll 1") for (u32 sp = 0; sp < (1u << 22); ++sp) {
      const v4u v = *(volatile LAS v4u*)(lds + f);
      if (__builtin_amdgcn_readfirstlane(v.x & v.y & v.z & v.w)) break;
      __builtin_amdgcn_s_sleep(1);
    }
    MEMB();
  };
  v4u w2f[28];
  const int w2v = opq(lane * 16), w2o = OFF_W2T + (sl * 8 + wave) * 28 * 1024;
  auto ldw2 = [&](int u) { w2f[u] = GLDS(w2v + (u & 3) * 1024, w2o + (u >> 2) * 4096); SB(); };
  if constexpr (CV) {
    const int ech = (wave >> 1) & 1, et = (wave & 1) * 2;
    float ega = ech ? ga_[1] : ga_[0], egd = ech ? gd_[1] : gd_[0], eg2 = ech ? g2_[1] : g2_[0];
    int edm = (ech * 8 + et) * 1024 + lane * 16 + L_DUMP, elz = ech * Z_CH + et * 512 + epi_l, ego = epi_g + ech * 64 + et * 8 * XTILE;
    auto epi = [&](int stage, int u) { conv_epi(lds, rs, edm + u * 1024, elz + (4 * stage + u) * 512, ego + (4 * stage + u) * 8 * XTILE, ega, egd, eg2); };
    LAS char* pa = lds + opq(L_KC + ech * KC_CH + (c16 & 3) * KC_S + 4104 - 8 * (c16 >> 2) - 16 * g4 - 256 * (wave & 1) - 64 * 59);
    LAS char* pbz = lds + opq(L_Z + ech * Z_CH + 496 + 32 * c16 - 16 * g4 - 256 * (wave & 1) - 192);
    const int dmo = opq(L_DUMP + (wave & 3) * 4096 + lane * 16);
    v4u B[16];
    auto rdb = [&](int slot, int rho, int dl) { B[slot] = *(LAS v4u*)(pbz + 64 * (8 * dl + 3 - rho)); SB(); };
    auto rda = [&](int rho, int a, int half) {
      const v2u u = *(LAS v2u*)(pa + 64 * (59 - 8 * a - rho) + 8 * half); SB();
      if (half) { w1f[8 * rho + a].z = u.x; w1f[8 * rho + a].w = u.y; } else { w1f[8 * rho + a].x = u.x; w1f[8 * rho + a].y = u.y; }
    };
    auto rd1 = [&](int rho, int n) {
      if (n < 2) rda(rho, 0, n); else if (n < 6) rdb(4 * rho + n - 2, rho, 5 - n); else rda(rho, (n - 4) >> 1, n & 1);
    };
    v4f acc[4];
    __builtin_amdgcn_s_setprio(1);
#pragma unroll
    for (int n = 0; n < 16; ++n) rd1(n / 12, n % 12);
    tie(ega); tie(egd); tie(eg2); tie(edm); tie(elz); tie(ego); SB();
#pragma unroll
    for (int t = 0; t < 4; ++t) acc[t] = (v4f){0, 0, 0, 0};
    {
      int n = 16, m = 0, c = 0;
#pragma unroll
      for (int rho = 0; rho < 4; ++rho) {
#pragma unroll
        for (int a = 0; a < 4; ++a) {
#pragma unroll
          for (int dl = 3 - a; dl >= 0; --dl) {
            acc[a + dl] = mfma16(w1f[8 * rho + a], B[4 * rho + 3 - dl], acc[a + dl]); tie(acc[a + dl]); SB();
            if (n < 48) { rd1(n / 12, n % 12); ++n; if ((c & 1) && n < 48) { rd1(n / 12, n % 12); ++n; } }
            else if (m < 8) { rdb(m, 0, 7 - m); ++m; }
            ++c;
          }
        }
      }
    }
#pragma unroll
    for (int t = 0; t < 4; ++t) LDS128F(dmo + t * 1024) = acc[t];
    setf(F_D0);
#pragma unroll
    for (int n = 0; n < 16; ++n) rda(n >> 3, 4 + ((n >> 1) & 3), n & 1);
    TS(20);
    wait1(F_D0 + ((wave & 3) ^ 1) * 4);
    TS(6);
    epi(0, 0); epi(0, 1);
    setf(F_PA);
    SB();
    TS(7);
#pragma unroll
    for (int t = 0; t < 4; ++t) acc[t] = (v4f){0, 0, 0, 0};
#pragma unroll
    for (int rho = 0; rho < 4; ++rho) {
      LGK0(); SB();
      int n = 0;
#pragma unroll
      for (int a = 0; a < 8; ++a) {
#pragma unroll
        for (int dl = 7 - a; dl >= 0 && dl >= 4 - a; --dl) {
          acc[a + dl - 4] = mfma16(w1f[8 * rho + a], B[8 * (rho & 1) + 7 - dl], acc[a + dl - 4]); tie(acc[a + dl - 4]); SB();
          if (n < 8 && rho < 3) rdb(8 * ((rho + 1) & 1) + n, rho + 1, 7 - n);
          else if (n < 16 && rho < 2) rda(rho + 2, 4 + ((n - 8) >> 1), n & 1);
          else if (n == TRIGN && rho == TRIG) setf(F_TR);
          ++n;
        }
      }
    }
    wait1(F_PA + ((wave & 3) ^ 1) * 4);
#pragma unroll
    for (int t = 0; t < 4; ++t) LDS128F(dmo + t * 1024) = acc[t];
    setf(F_D1);
    TS(21);
    wait1(F_D1 + ((wave & 3) ^ 1) * 4);
    epi(1, 0); epi(1, 1);
    SB();
    __builtin_amdgcn_s_setprio(0);
    TS(8);
    wait1(F_WS + (wave & 3) * 4);
    const int wso = opq(L_WS + (wave & 3) * NWS * 1024 + lane * 16);
#pragma unroll
    for (int i = 0; i < NWS; ++i) w1f[i] = LDS128(wso + i * 1024);
    setf(F_EB);
  } else {
    if (tid == 0) {
      GST8(((v2u){tagL, tagH}), OFF_XFL + p * 32);
      if (b == 0) GST8(((v2u){tagL, tagH}), 0);
    }
    if (!early) {
      SPIN {
        MEMB();
        u32 bad = 0;
#pragma unroll
        for (int q = 0; q < 4; ++q) { const v2u g = GLD8(OFF_WFL + (q * 56 + (lane < 56 ? lane : 0)) * 128); bad |= (g.x ^ tagL) | (g.y ^ tagH); }
        if (!__any(bad != 0)) break;
        __builtin_amdgcn_s_sleep(2);
      }
      ldw1(0, NPRE);
    }
    ldw1(NPRE, N1);
    TS(20);
    wait4(F_PA);
    ldw1(N1, 28);
    const int xfo = OFF_XFL + (wave * 56 + (lane < 56 ? lane : 0)) * 32;
    v2u xf = GLD8(xfo);
    SB();
    {
      const char* src = ws + w1o + 4 * 28 * 1024;
      const u32 l16 = lane * 16;
#pragma unroll
      for (int i = 0; i < NWS; ++i) {
        if ((i & 3) == 0) { set_m0(lds0 + L_WS + wave * NWS * 1024 + i * 1024); dma16_dev_o<0>(src + i * 1024, l16); }
        else if ((i & 3) == 1) dma16_dev_o<1024>(src + (i - 1) * 1024, l16);
        else if ((i & 3) == 2) dma16_dev_o<2048>(src + (i - 2) * 1024, l16);
        else dma16_dev_o<3072>(src + (i - 3) * 1024, l16);
      }
    }
#pragma unroll
    for (int i = 0; i < NB1; ++i) ldw2(i);
    asm volatile("s_waitcnt vmcnt(%0)" :: "i"(NB1) : "memory");
    if (__any(xf.x != tagL || xf.y != tagH)) {
      SPIN { __builtin_amdgcn_s_sleep(2); MEMB(); xf = GLD8(xfo); if (!__any(xf.x != tagL || xf.y != tagH)) break; }
    }
    setf(F_WS);
    TS(21);
    wait4(F_TR);
    __builtin_amdgcn_s_setprio(2);
    TS(8);
  }

  const int uvo = h ? OFF_CFL + grp * 128 : OFF_UV + (256 * sl + 32 * wave + lane) * 16;
  const int gbo = OFF_MOD + (560 + 16 * sl + (tid & 15)) * 16;
  v4u uv;
  const int trb = (8 * h + trq) * 64 + trc;
  const char* xg = ws + OFF_X1 + grp * XTILE + (wave & 3) * 7168;
  const u32 xl = lane * 16;
  auto dma_x = [&](int pass, int i) {
    const int half = i >= 7, q = i - 7 * half, o = half * XHALF + (q >> 2) * 4096;
    const char* src = xg + pass * 32 * XTILE + o;
    if ((q & 3) == 0) { set_m0(lds0 + pass * XTILE + (wave & 3) * 7168 + o); dma16_dev_o<0>(src, xl); }
    else if ((q & 3) == 1) dma16_dev_o<1024>(src, xl);
    else if ((q & 3) == 2) dma16_dev_o<2048>(src, xl);
    else dma16_dev_o<3072>(src, xl);
  };
  auto accept_x = [&](int pass) {
    const int lx = pass * XTILE + (wave & 3) * 7168;
    float s, q;
    SPIN {
      u32 m = 0;
      v4u t[7], f[7];
#pragma unroll
      for (int i = 0; i < 7; ++i) t[i] = LDS128(lx + i * 1024 + lane * 16);
#pragma unroll
      for (int i = 0; i < 7; ++i) f[i] = trfrag(lds + lx + XHALF + trb + i * 1024);
#pragma unroll
      for (int i = 0; i < 7; ++i) m = max4(m, t[i]);
      s = 0.f; q = 0.f;
#pragma unroll
      for (int i = 0; i < 7; ++i) sumsq(f[i], s, q);
      if (!__any(m == SENTW || q != q)) break;
      MEMB();
#pragma unroll
      for (int i = 0; i < 7; ++i) m = max4(m, LDS128(lx + XHALF + i * 1024 + lane * 16));
      if (!__any(m == SENTW)) break;
      __builtin_amdgcn_s_sleep(4);
#pragma unroll
      for (int i = 0; i < 14; ++i) dma_x(pass, i);
      VMW(0);
    }
    s = swap32_add(s); q = swap32_add(q);
    if (!h) { v2f o = {s, q}; LDS64F(L_ST + pass * ST_P + lane * 48 + (wave & 3) * 8) = o; }
  };
  uv = GLD(uvo);
  SB();
  if constexpr (!CV) {
#pragma unroll
    for (int i = 0; i < 14; ++i) dma_x(0, i);
    __builtin_amdgcn_s_setprio(0);
    TS(9);
#pragma unroll
    for (int i = NB1; i < NB1 + NB2; ++i) ldw2(i);
    asm volatile("s_waitcnt vmcnt(%0)" :: "i"(NB2) : "memory");
    tie(uv);
    TS(6);
    accept_x(0);
    setf(F_XA);
    TS(10);
    wait4(F_EB);
  } else {
    ldw1(NWS, 28);
    TS(10);
  }
  wait4(F_XA);
  TS(11);
  if (!__all(tagok(uv, tagL, tagH))) {
    SPIN { __builtin_amdgcn_s_sleep(4); MEMB(); uv = GLD(uvo); if (__all(tagok(uv, tagL, tagH))) break; }
  }
  v4u uvf;
  {
    const float u = __uint_as_float(uv.y), v = __uint_as_float(uv.z), uh = rbf(u), vh = rbf(v);
    uvf = (v4u){h ? 0u : pk(uh, u - uh), h ? 0u : pk(vh, v - vh), 0u, 0u};
  }

  const v16f zero16 = {0, 0, 0, 0, 0, 0, 0, 0, 0, 0, 0, 0, 0, 0, 0, 0};
  float sd, rl;
  auto gemm1 = [&](int pass, auto extra) {
    const int lx = pass * XTILE;
    LAS char* pb = lds + opq(lx + trb);
    v4u bf[4];
#pragma unroll
    for (int k = 0; k < 3; ++k) bf[k] = trfrag(pb + k * 1024);
    SB();
    v16f acc = zero16;
    v2u res; v4f a0; float mean, ex2; v4u bs;
    __builtin_amdgcn_s_setprio(2 - CV);
#pragma unroll
    for (int k = 0; k < 28; ++k) {
      if (k + 3 < 28) bf[(k + 3) & 3] = trfrag(pb + (k + 3) * 1024);
      acc = mfma32(w1f[k], bf[k & 3], acc); tie(acc);
      extra(k);
      if (k == 1) {
        res = trd(lds + lx + XHALF + (64 * sl + 8 * wave + 4 * h + trq) * 64 + trc);
        a0 = LDS128F(L_ST + pass * ST_P + (lane & 31) * 48 + h * 16);
      }
      if (k == 7) LDS64(L_RES + pass * RES_P + (lane & 31) * 144 + 16 * wave + 8 * h) = res;
      if (k == 9) mean = pin(swap32_add(pin(a0[0]) + opf(a0[2])) * (1.f / D));
      if (k == 11) ex2 = pin(swap32_add(pin(a0[1]) + opf(a0[3])) * (1.f / D));
      if (k == 13) ex2 = pin(rbf(fmaxf(pin(ex2) - mean * mean, 0.f)));
      if (k == 15) ex2 = pin(__builtin_amdgcn_sqrtf(rbf(pin(ex2) + 1e-5f)));
      if (k == 17) { sd = rbf(pin(ex2)); rl = pin(__builtin_amdgcn_rcpf(sd) * -1.4426950408889634f); }
      if (k == 19) { mean = pin(mean); bs = (v4u){h ? 0u : pk(-mean, -mean), h ? 0u : pk(sd, sd), 0u, 0u}; tie(bs); }
      SB();
    }
    acc = mfma32(uvf, bs, acc); tie(acc);
    SB();
    __builtin_amdgcn_s_setprio(0);
    return acc;
  };
  auto epi1 = [&](int pass, v16f acc) {
    tie(acc);
    u32 hp[8];
#pragma unroll
    for (int e = 0; e < 8; ++e) {
      const float a0 = opf(acc[2 * e]), a1 = opf(acc[2 * e + 1]);
      const float e0_ = __builtin_amdgcn_exp2f(a0 * rl), e1_ = __builtin_amdgcn_exp2f(a1 * rl);
      hp[e] = pks(a0 * __builtin_amdgcn_rcpf(fmaf(e0_, sd, sd)), a1 * __builtin_amdgcn_rcpf(fmaf(e1_, sd, sd)));
    }
    const int ho = opq(OFF_H + (grp + 32 * pass) * HTILE + (16 * sl + 2 * wave) * 1024 + lane * 16);
    v4u h0 = {hp[0], hp[1], hp[2], hp[3]}, h1 = {hp[4], hp[5], hp[6], hp[7]};
    GST(h0, ho); GST(h1, ho + 1024);
    SB();
  };
  v16f acc;
  if constexpr (!CV) {
    acc = gemm1(0, [&](int k) { if (!(k & 1)) dma_x(1, k >> 1); });
    TS(12);
    epi1(0, acc);
    VMW(2);
    __builtin_amdgcn_s_setprio(2);
    accept_x(1);
    setf(F_XB);
    wait4(F_XB);
    TS(13);
    acc = gemm1(1, [&](int k) { if (!(k & 1) && NB1 + NB2 + (k >> 1) < 28) ldw2(NB1 + NB2 + (k >> 1)); });
  } else {
    acc = gemm1(0, [&](int k) { if (k >= 28 - 2 * NCA && !(k & 1)) ldw2((k - 28 + 2 * NCA) >> 1); });
    TS(12);
    epi1(0, acc);
#pragma unroll
    for (int i = NCA; i < 14; ++i) ldw2(i);
    TS(9);
    wait4(F_XB);
    TS(13);
    acc = gemm1(1, [&](int k) { if (!(k & 1)) ldw2(14 + (k >> 1)); });
  }
  TS(14);
  epi1(1, acc);
  if constexpr (!CV) {
#pragma unroll
    for (int i = NB1 + NB2 + 14; i < 28; ++i) ldw2(i);
  }
  TS(24);

  const int hoA = OFF_H + grp * HTILE + wave * 14 * 1024;
  v4u hf[14];
  v16f c0, c1;
  auto ldh = [&](int ho) {
    const int l16 = opq(lane * 16);
#pragma unroll
    for (int i = 0; i < 14; ++i) { hf[i] = GLDS(l16 + (i & 3) * 1024, ho + (i >> 2) * 4096); SB(); }
  };
  auto gemm2 = [&](int ho) {
    c0 = zero16; c1 = zero16;
#pragma unroll
    for (int i = 0; i < 14; ++i) { c0 = mfma32(w2f[2 * i], hf[i], c0); c1 = mfma32(w2f[2 * i + 1], hf[i], c1); tie(c0); tie(c1); SB(); }
    if (__any(c0[0] != c0[0])) {
      SPIN {
        MEMB();
        ldh(ho);
        u32 m = 0;
#pragma unroll
        for (int i = 0; i < 14; ++i) m = max4(m, hf[i]);
        if (!__any(m == SENTW)) break;
      }
      c0 = zero16; c1 = zero16;
#pragma unroll
      for (int i = 0; i < 14; ++i) { c0 = mfma32(w2f[2 * i], hf[i], c0); c1 = mfma32(w2f[2 * i + 1], hf[i], c1); }
    }
  };
  auto put_red = [&](int lr) {
    const int o = opq(lr + wave * RED_W + h * RED_S + (lane & 31) * 16);
#pragma unroll
    for (int q = 0; q < 4; ++q) {
      v4f o0 = {c0[4 * q], c0[4 * q + 1], c0[4 * q + 2], c0[4 * q + 3]}, o1 = {c1[4 * q], c1[4 * q + 1], c1[4 * q + 2], c1[4 * q + 3]};
      LDS128F(o + 2 * q * RED_S) = o0;
      LDS128F(o + (8 + 2 * q) * RED_S + 32) = o1;
    }
    SB();
  };
  v4u gbv;
  auto finish = [&](int pass, int t) {
    const int dq = t & 15, l = t >> 4;
    const int lr = opq(pass * L_REDB + (dq >> 1) * RED_S + (dq & 1) * (8 * RED_S + 32) + l * 16), lr2 = opq(lr + 4 * RED_W);
    v4f s0 = LDS128F(lr), s1 = LDS128F(lr2);
#pragma unroll
    for (int w = 1; w < 4; ++w) { s0 += LDS128F(lr + w * RED_W); s1 += LDS128F(lr2 + w * RED_W); }
    s0 += s1;
    const v2u rv = LDS64(L_RES + pass * RES_P + l * 144 + dq * 8);
    const v2u o = {pk(fma2(unpk(gbv.y), s0.xy, unpk(rv.x))), pk(fma2(unpk(gbv.z), s0.zw, unpk(rv.y)))};
    *(GAS v2u*)(out + (pass * 1024 + grp * 32 + l) * D + 64 * sl + 4 * dq) = o;
  };
  gbv = GLD(gbo);
  SB();
  BAR();
  TS(15);
  __builtin_amdgcn_s_setprio(1);
  ldh(hoA);
  __builtin_amdgcn_s_setprio(0);
  gemm2(hoA);
  if (!__all(tagok(gbv, tagL, tagH))) {
    SPIN { __builtin_amdgcn_s_sleep(4); MEMB(); gbv = GLD(gbo); if (__all(tagok(gbv, tagL, tagH))) break; }
  }
  TS(16);
  put_red(L_REDA);
  setf(F_RA + CV * 16);
  ldh(hoA + 32 * HTILE);
  TS(22);
  if (TAIL == 1 && !CV) {
    wait4(F_RA); wait4(F_RA + 16);
    finish(0, tid); finish(0, tid + 256);
  }
  SB();
  TS(4);
  gemm2(hoA + 32 * HTILE);
  put_red(L_REDB);
  TS(17);
  BAR();
  if (TAIL == 0) finish(0, tid);
  finish(1, tid);
  };
  if (wave < 4) rest(Role<0>{}); else rest(Role<1>{});
  TS(29); TSEND();
}

extern "C" void kernel_launch(void* const* d_in, const int* in_sizes, int n_in, void* d_out, int out_size, void* d_ws, size_t ws_size,
                              hipStream_t stream) {
  k_all<<<256, 512, 0, stream>>>((const u16*)d_in[0], (const u16*)d_in[1], (const u16*)d_in[2], (const u16*)d_in[3], (const u16*)d_in[4],
                                 (const u16*)d_in[5], (const u16*)d_in[6], (const u16*)d_in[7], (const u16*)d_in[8], (const u16*)d_in[10],
                                 (const u16*)d_in[11], (const u16*)d_in[12], (const u16*)d_in[13], (u16*)d_out, (char*)d_ws);
}
```

```cpp
#include <hip/hip_runtime.h>
#include <stdint.h>

typedef unsigned short u16;
typedef unsigned u32;
typedef unsigned long long u64;
typedef u32 v4u __attribute__((ext_vector_type(4)));
typedef u32 v2u __attribute__((ext_vector_type(2)));
typedef float v4f __attribute__((ext_vector_type(4)));
typedef float v2f __attribute__((ext_vector_type(2)));
typedef float v16f __attribute__((ext_vector_type(16)));
typedef __bf16 bf8 __attribute__((ext_vector_type(8)));
typedef __bf16 bf2 __attribute__((ext_vector_type(2)));
typedef short s4 __attribute__((ext_vector_type(4)));
#define GAS __attribute__((address_space(1)))
#define LAS __attribute__((address_space(3)))
#define DEV __device__ __forceinline__

constexpr int D = 448, F = 1792;

constexpr int OFF_STAT = 4096;
constexpr int OFF_MOD = 20480;
constexpr int OFF_UV = 32768;
constexpr int OFF_CFL = 61440;
constexpr int OFF_WFL = 65536;
constexpr int OFF_XFL = 98304;
constexpr int OFF_XT = 131072;
constexpr int OFF_W1T = 4 << 20;
constexpr int OFF_W2T = 6 << 20;
constexpr int OFF_X1 = 8 << 20;
constexpr int OFF_H = 12 << 20;
constexpr int OFF_DBG = 24 << 20;
constexpr int WS_BYTES = 32 << 20;
constexpr int XTILE = 57344, XHALF = 28672, HTILE = 114688;

constexpr int KG_W = 8704;
constexpr int L_KC = 0, KC_S = 4160, KC_CH = 4 * KC_S;
constexpr int L_LNT = 69632;
constexpr int L_STX = 76800;
constexpr int L_KLIN = 68096, KLIN_CH = 2592;
constexpr int NWS = 13, L_WS = 57344;
constexpr int L_Z = 110592, Z_CH = 4608;
constexpr int L_XT = 119808;
constexpr int L_DUMP = 129024;
constexpr int L_XA = 0, L_XB = XTILE;
constexpr int RED_S = 576, RED_W = 16 * RED_S, L_REDA = 0, L_REDB = 8 * RED_W;
constexpr int L_RES = 147456, RES_P = 4608;
constexpr int L_ST = 156672, ST_P = 1536;
constexpr int L_PF = 159744;
constexpr int L_FL = 160000;
constexpr int F_D0 = L_FL, F_D1 = L_FL + 16, F_EB = L_FL + 32, F_XA = L_FL + 48, F_XB = L_FL + 64, F_PA = L_FL + 80, F_TR = L_FL + 96,
              F_WS = L_FL + 112, F_RA = L_FL + 128, F_XL = L_FL + 160, F_S0 = L_FL + 176, F_S1 = L_FL + 192;
constexpr int L_WA = 160208;
constexpr int LDS_BYTES = 160272;
constexpr int L_AR0 = 0, L_AR1 = 81920;
constexpr int L_ASC = 57344;
constexpr int L_ARED = 58368;
constexpr int L_AG = 60416;
constexpr int L_ARED2 = 62464;

constexpr u32 SPIN_CAP = 1u << 14;
constexpr u32 SENTW = 0xffbfffbfu;
#define CODE_PF 36864
#define NPRE 14
#define NS1 12
#define PKEPI 1
#define NB2 6
#define NB1 6
#define NC1 21
#define NCA 14
#define LNF 0
#define GMPOS 1
#define WOPT 0
#define SWPOS 1
#define NHC 10
#define STAG 0
#define ABL 0
#define TAIL 2
#define TRIG 4
#define TRIGN 8
#define VPOS 12

DEV float blo(u32 v) { return __uint_as_float(v << 16); }
DEV float bhi(u32 v) { return __uint_as_float(v & 0xffff0000u); }
DEV v2f unpk(u32 v) { v2f r = {blo(v), bhi(v)}; return r; }
DEV u32 pk(float a, float b) { bf2 r = {(__bf16)a, (__bf16)b}; return __builtin_bit_cast(u32, r); }
DEV u32 pk(v2f a) { return pk(a.x, a.y); }
DEV float rbf(float a) { return blo(pk(a, a)); }
DEV u32 umax(u32 a, u32 b) { return a > b ? a : b; }
DEV u32 max4(u32 m, v4u v) { return umax(umax(umax(umax(m, v.x), v.y), v.z), v.w); }
DEV v2f fma2(v2f a, v2f b, v2f c) { return __builtin_elementwise_fma(a, b, c); }
DEV v2f bc(float a) { v2f r = {a, a}; return r; }
DEV v4f mfma16(v4u a, v4u b, v4f c) { return __builtin_amdgcn_mfma_f32_16x16x32_bf16(__builtin_bit_cast(bf8, a), __builtin_bit_cast(bf8, b), c, 0, 0, 0); }
DEV v16f mfma32(v4u a, v4u b, v16f c) { return __builtin_amdgcn_mfma_f32_32x32x16_bf16(__builtin_bit_cast(bf8, a), __builtin_bit_cast(bf8, b), c, 0, 0, 0); }
DEV float dot2(u32 a, u32 b, float c) { return __builtin_amdgcn_fdot2_f32_bf16(__builtin_bit_cast(bf2, a), __builtin_bit_cast(bf2, b), c, false); }
DEV void sumsq(v4u f, float& s, float& q) {
  s = dot2(f.x, 0x3f803f80u, s); q = dot2(f.x, f.x, q); s = dot2(f.y, 0x3f803f80u, s); q = dot2(f.y, f.y, q);
  s = dot2(f.z, 0x3f803f80u, s); q = dot2(f.z, f.z, q); s = dot2(f.w, 0x3f803f80u, s); q = dot2(f.w, f.w, q);
}
DEV v2u trd(LAS char* p) { return __builtin_bit_cast(v2u, __builtin_amdgcn_ds_read_tr16_b64_v4i16((LAS s4*)p)); }
DEV v4u trfrag(LAS char* p) { v2u a = trd(p), b = trd(p + 256); v4u r = {a.x, a.y, b.x, b.y}; return r; }
template <int CTRL, int RM> DEV float dppadd(float v) {
  return v + __builtin_bit_cast(float, __builtin_amdgcn_update_dpp(0, __builtin_bit_cast(int, v), CTRL, RM, 0xf, RM == 0xf));
}
DEV float rdl(float v, int l) { return __builtin_bit_cast(float, __builtin_amdgcn_readlane(__builtin_bit_cast(int, v), l)); }
DEV float swap32_add(float v) { auto r = __builtin_amdgcn_permlane32_swap(__float_as_uint(v), __float_as_uint(v), false, false); return __uint_as_float(r[0]) + __uint_as_float(r[1]); }
DEV float swap16_add(float v) { auto r = __builtin_amdgcn_permlane16_swap(__float_as_uint(v), __float_as_uint(v), false, false); return __uint_as_float(r[0]) + __uint_as_float(r[1]); }
DEV int opq(int v) { asm volatile("" : "+v"(v)); return v; }
DEV float opf(float v) { asm("" : "+v"(v)); return v; }
DEV float pin(float v) { asm volatile("" : "+v"(v)); return v; }
DEV u32 pks(float a, float b) { return pk(opf(a), opf(b)); }
DEV v2f cmul(v2f a, v2f w, v2f wn) { return fma2(a.yx, wn, a * w.xx); }
DEV v2f cneg(v2f w) { v2f r = {-w.y, w.y}; return r; }
DEV v2f cexpx(float er, float ei, float x) {
  const float e = __builtin_amdgcn_exp2f(er * x), ph = __builtin_amdgcn_fractf(ei * x);
  v2f c = {__builtin_amdgcn_cosf(ph), __builtin_amdgcn_sinf(ph)};
  return c * bc(e);
}

#define BAR() asm volatile("s_waitcnt lgkmcnt(0)\n\ts_barrier" ::: "memory")
#define VMW(n) asm volatile("s_waitcnt vmcnt(" #n ")" ::: "memory")
#define LGK0() __builtin_amdgcn_s_waitcnt(0xC07F)
#define MEMB() asm volatile("" ::: "memory")
#define SB() __builtin_amdgcn_sched_barrier(0)
#define CNT(i)
#define SPIN _Pragma("unroll 1") for (u32 sp = 0; sp < SPIN_CAP; ++sp)
#define LDS128(off) (*(LAS v4u*)(lds + (off)))
#define LDS128F(off) (*(LAS v4f*)(lds + (off)))
#define LDS64(off) (*(LAS v2u*)(lds + (off)))
#define LDS64F(off) (*(LAS v2f*)(lds + (off)))
#define LDS32(off) (*(LAS u32*)(lds + (off)))
#define LDS32F(off) (*(LAS float*)(lds + (off)))
#define GLD(off) __builtin_amdgcn_raw_buffer_load_b128(rs, (off), 0, 16)
#define GLDS(voff, soff) __builtin_amdgcn_raw_buffer_load_b128(rs, (voff), (soff), 16)
#define GLD8(off) __builtin_amdgcn_raw_buffer_load_b64(rs, (off), 0, 16)
#define GST8(v, off) __builtin_amdgcn_raw_buffer_store_b64((v), rs, (off), 0, 16)
#define GST(v, off) __builtin_amdgcn_raw_buffer_store_b128((v), rs, (off), 0, 16)

DEV void dma16(u32 ldsaddr, const char* gbase, u32 voff) {
  asm volatile("s_mov_b32 m0, %0\n\ts_nop 0\n\tglobal_load_lds_dwordx4 %1, %2" :: "s"(ldsaddr), "v"(voff), "s"(gbase) : "memory");
}
DEV void dma16_dev(u32 ldsaddr, const char* gbase, u32 voff) {
  asm volatile("s_mov_b32 m0, %0\n\ts_nop 0\n\tglobal_load_lds_dwordx4 %1, %2 sc1" :: "s"(ldsaddr), "v"(voff), "s"(gbase) : "memory");
}
DEV void dma4_dev(u32 ldsaddr, const char* gbase, u32 voff) {
  asm volatile("s_mov_b32 m0, %0\n\ts_nop 0\n\tglobal_load_lds_dword %1, %2 sc1" :: "s"(ldsaddr), "v"(voff), "s"(gbase) : "memory");
}
DEV void set_m0(u32 v) { asm volatile("s_mov_b32 m0, %0\n\ts_nop 0" :: "s"(v) : "memory"); }
template <int OFF> DEV void dma16_dev_o(const char* gbase, u32 voff) {
  asm volatile("global_load_lds_dwordx4 %0, %1 offset:%2 sc1" :: "v"(voff), "s"(gbase), "i"(OFF) : "memory");
}
template <class T> DEV void tie(T& v) { asm volatile("" : "+v"(v)); }
template <int OFF> DEV void st_tid(u32 v) { asm volatile("ds_write_addtid_b32 %0 offset:%1" :: "v"(v), "i"(OFF) : "memory"); }

#define TS(i)
#define TSEND()

template <int V> struct Role { static constexpr int value = V; };
DEV bool tagok(v4u g, u32 tagL, u32 tagH) { return g.x == tagL && g.w == tagH; }

struct Ln1 { int lw, lr, go; };
DEV Ln1 ln1_addr(u32 b, int lane, u32 wave) {
  Ln1 a = {L_LNT + (int)wave * 1024 + lane * 16, L_LNT + (int)wave * 1024 + (lane & 3) * 256 + (lane >> 2) * 8,
           OFF_XT + (32 * (int)wave + 2 * (lane >> 2)) * 16384 + (4 * (int)b + (lane & 3)) * 16};
  tie(a.lw); tie(a.lr); tie(a.go);
  return a;
}
DEV void ln1_publish(LAS char* lds, __amdgpu_buffer_rsrc_t rs, v4u xr, Ln1 ad, u32 b, int lane, u32 wave, u32 tagL, u32 tagH) {
  if (wave < 7) {
    LDS128(ad.lw) = xr;
    const v2u ra = LDS64(ad.lr), rb = LDS64(ad.lr + 128);
    float s = 0.f, q = 0.f;
    sumsq(xr, s, q);
    s = dppadd<0xb1, 0xf>(s); q = opf(dppadd<0xb1, 0xf>(q));
    s = dppadd<0x4e, 0xf>(s); q = opf(dppadd<0x4e, 0xf>(q));
    s = dppadd<0x141, 0xf>(s); q = opf(dppadd<0x141, 0xf>(q));
    MEMB();
    if ((lane & 7) == 0) { const v4u o = {__float_as_uint(s), __float_as_uint(q), tagL, tagH}; LDS128(L_STX + wave * 128 + lane * 2) = o; }
    MEMB();
    const v4u o0 = {tagL, ra.x, rb.x, tagH}, o1 = {tagL, ra.y, rb.y, tagH};
    GST(o0, ad.go); GST(o1, ad.go + 16384);
  } else {
    v4u t;
    _Pragma("unroll 1") for (u32 sp = 0; sp < (1u << 22); ++sp) {
      t = *(volatile LAS v4u*)(lds + L_STX + (lane < 56 ? lane : 0) * 16);
      if (!__any(t.z != tagL || t.w != tagH)) break;
      __builtin_amdgcn_s_sleep(1);
    }
    float s = lane < 56 ? __uint_as_float(t.x) : 0.f, q = lane < 56 ? __uint_as_float(t.y) : 0.f;
    s = dppadd<0x128, 0xf>(s); q = dppadd<0x128, 0xf>(q);
    s = swap32_add(swap16_add(s)); q = swap32_add(swap16_add(q));
    const float mean = s * (1.f / D), var = rbf(fmaxf(q * (1.f / D) - mean * mean, 0.f));
    const u32 ms = pk(mean, __builtin_amdgcn_sqrtf(rbf(var + 1e-5f)));
    const v4u o = {tagL, ms, (u32)__builtin_amdgcn_update_dpp(0, (int)ms, 0xb1, 0xf, 0xf, true), tagH};
    if (lane < 8 && !(lane & 1)) GST(o, OFF_STAT + 64 * b + lane * 8);
  }
  MEMB();
}

DEV void conv_epi(LAS char* lds, __amdgpu_buffer_rsrc_t rs, int dump, int lz, int goff, float ga, float gd, float g2) {
  const v4f p0 = LDS128F(dump), p1 = LDS128F(dump + 4096);
  const v2u hh = LDS64(lz + 480), xx = LDS64(lz + (L_XT - L_Z));
  u32 X0 = pk(fma2(bc(ga), p0.xy + p1.xy, fma2(bc(gd), unpk(hh.x), unpk(xx.x)))), X1 = pk(fma2(bc(ga), p0.zw + p1.zw, fma2(bc(gd), unpk(hh.y), unpk(xx.y))));
  asm("" : "+v"(X0), "+v"(X1));
  const u32 S0 = pk(unpk(X0) * bc(g2)), S1 = pk(unpk(X1) * bc(g2));
  auto r0 = __builtin_amdgcn_permlane16_swap(X0, S0, false, false);
  auto r1 = __builtin_amdgcn_permlane16_swap(X1, S1, false, false);
  v4u v = {r0[0], r1[0], r0[1], r1[1]};
  if (ABL & 64) tie(v); else GST(v, goff);
}

__global__ void __launch_bounds__(512, 2) k_all(const u16* __restrict__ x, const u16* __restrict__ Are, const u16* __restrict__ Aim,
    const u16* __restrict__ Bre, const u16* __restrict__ Bim, const u16* __restrict__ Cre, const u16* __restrict__ Cim,
    const u16* __restrict__ logdt, const u16* __restrict__ Dskip, const u16* __restrict__ w1, const u16* __restrict__ w2,
    const u16* __restrict__ cvec, const u16* __restrict__ wmod, u16* __restrict__ out, char* ws) {
  __shared__ __attribute__((aligned(16))) char smem[LDS_BYTES];
  LAS char* lds = (LAS char*)smem;
  const u32 lds0 = (u32)(size_t)lds;
  u64 pc;
  asm volatile("s_getpc_b64 %0" : "=s"(pc));
  asm volatile("" :: "s"(x), "s"(Are), "s"(Aim), "s"(Bre), "s"(Bim), "s"(Cre), "s"(Cim), "s"(logdt), "s"(Dskip), "s"(w1), "s"(w2), "s"(cvec), "s"(wmod), "s"(out), "s"(ws));
  const int tid = threadIdx.x, lane = tid & 63;
  const u32 wave = __builtin_amdgcn_readfirstlane(tid >> 6);
  const u32 b = blockIdx.x, xcd = b & 7, j = b >> 3;
  TS(0);
  const __amdgpu_buffer_rsrc_t rs = __builtin_amdgcn_make_buffer_rsrc(ws, 0, WS_BYTES, 0x00020000);
  auto first_loads = [&](v4u& xr, u64& e0) {
    const u64 xa = (u64)(x + 8 * b * D);
    const v4u srd = {(u32)xa, (u32)(xa >> 32) & 0xffffu, 16 * D, 0x00020000u};
    const u32 vo = wave < 7 ? (lane >> 3) * (2 * D) + 128 * wave + (lane & 7) * 16 : 16 * D;
    asm volatile("buffer_load_dwordx4 %0, %1, %2, 0 offen ; ALOAD" : "=v"(xr) : "v"(vo), "s"(srd) : "memory");
    asm volatile("s_load_dwordx2 %0, %1, 0x0 ; SLOAD" : "=s"(e0) : "s"(ws) : "memory");
  };
  const v4u SENT = {SENTW, SENTW, SENTW, SENTW};
  auto gettag = [&](u64 e0, u32& tl, u32& th) {
    const u64 T = (((u64)(u32)__builtin_amdgcn_readfirstlane((u32)(e0 >> 32)) << 32) | (u32)__builtin_amdgcn_readfirstlane((u32)e0)) + 0x9E3779B97F4A7C15ull; tl = (u32)T; th = (u32)(T >> 32); };
  u32 tagL, tagH;

  if (j >= 28) {
    v4u xr; u64 e0;
    first_loads(xr, e0);
    const u32 a = (j - 28) * 8 + xcd;
    const int h = lane >> 5, trq = (lane & 15) >> 2, trc = ((lane >> 4) & 1) * 32 + (lane & 3) * 8;
    const u32 wv = wave < 7 ? wave : 0;
    auto stage = [&](u32 ldst, const char* src, u32 rstride) {
      if (wave < 7) {
        const u32 vo = ((lane >> 3) + 64 * wave) * rstride + (lane & 7) * 16;
#pragma unroll
        for (int i = 0; i < 8; ++i) dma16(lds0 + ldst + wave * 8192 + i * 1024, src + i * 8 * rstride, vo);
      }
    };
    auto gemv = [&](u32 lsrc, int atab, float& r0, float& r1) {
      v16f c0 = {0, 0, 0, 0, 0, 0, 0, 0, 0, 0, 0, 0, 0, 0, 0, 0}, c1 = c0;
      LAS char* pb = lds + opq(lsrc + wv * 8192 + (8 * h + trq) * 128 + trc);
#pragma unroll
      for (int k = 0; k < 4; ++k) {
        const v4u af = LDS128(atab + wv * 128 + k * 32 + h * 16);
        const v2u b0 = trd(pb + k * 2048), b1 = trd(pb + k * 2048 + 512), b2 = trd(pb + k * 2048 + 64), b3 = trd(pb + k * 2048 + 576);
        c0 = mfma32(af, (v4u){b0.x, b0.y, b1.x, b1.y}, c0); c1 = mfma32(af, (v4u){b2.x, b2.y, b3.x, b3.y}, c1);
      }
      r0 = h ? c1[0] : c0[0]; r1 = h ? c1[1] : c0[1];
    };
    u32 cv;
    asm volatile("global_load_ushort %0, %1, %2 ; ALOAD" : "=v"(cv) : "v"((64 * wv + lane) * 2), "s"(cvec) : "memory");
    const u32 lc = a < 21 ? a : (a < 28 ? a + 7 : a - 7);
    stage(L_AR0, (const char*)wmod + lc * 128, 6 * D * 2);
    const Ln1 lna = ln1_addr(b, lane, wave);
    if (wave == 7) VMW(0);
    asm volatile("s_waitcnt vmcnt(8) lgkmcnt(0) ; TIE" : "+v"(xr), "+s"(e0), "+v"(cv) :: "memory");
    {
      const float cf_ = blo(cv);
      *(LAS u16*)(lds + L_ASC + (64 * wv + lane) * 2) = (u16)pk(cf_ * __builtin_amdgcn_rcpf(1.f + __expf(-cf_)), 0.f);
    }
    gettag(e0, tagL, tagH);
    TS(1);
    ln1_publish(lds, rs, xr, lna, b, lane, wave, tagL, tagH);
    TS(2);
    VMW(2);
    if (a < 3) stage(L_AR1, (const char*)wmod + (25 + a) * 128, 6 * D * 2);
    auto modround = [&](u32 lsrc, u32 lc_) {
      float r0, r1;
      gemv(lsrc, L_ASC, r0, r1);
      LDS32F(L_ARED + wave * 256 + lane * 4) = r0;
      BAR();
      if (tid < 16) {
        v4f s = LDS128F(L_ARED + tid * 16);
#pragma unroll
        for (int w = 1; w < 7; ++w) s += LDS128F(L_ARED + w * 256 + tid * 16);
        v4u o = {tagL, pk(s[0], s[1]), pk(s[2], s[3]), tagH};
        GST(o, OFF_MOD + (16 * lc_ + tid) * 16);
      }
    };
    modround(L_AR0, lc);
    TS(3);
    if (a < 3) { VMW(0); BAR(); modround(L_AR1, 25 + a); }
    if (a >= 4) stage(L_AR1, (const char*)w1 + (a - 4) * 128, F * 2);
    if (a >= 3 && a < 10) stage(L_AR0, (const char*)wmod + (32 + a) * 128, 6 * D * 2);
    const int hc = OFF_H + a * HTILE + tid * 16;
#pragma unroll
    for (int i = 0; i < NHC; ++i) GST(SENT, hc + (i / 14) * 32 * HTILE + (i % 14) * 8192);
    TS(4);
    if (a >= 4) {
      if (wave < 4) {
        const int gi = 336 + (tid < 224 ? tid : 0);
        v4u g;
        SPIN {
          MEMB();
          g = GLD(OFF_MOD + gi * 16);
          if (__all(tagok(g, tagL, tagH))) break;
          __builtin_amdgcn_s_sleep(8);
        }
        v2u f = {g.y, g.z};
        if (tid >= 112) { f.x = pk(unpk(f.x) + bc(1.f)); f.y = pk(unpk(f.y) + bc(1.f)); }
        if (tid < 224) LDS64(L_AG + tid * 8) = f;
      }
      VMW(0);
      BAR();
      TS(5);
      float r0, r1;
      gemv(L_AR1, L_AG + ((lane & 1) ? 0 : 2 * D), r0, r1);
      { v2f o = {r0, r1}; LDS64F(L_ARED2 + wave * 512 + lane * 8) = o; }
      BAR();
      if (tid < 64) {
        v2f s = LDS64F(L_ARED2 + tid * 8);
#pragma unroll
        for (int w = 1; w < 7; ++w) s += LDS64F(L_ARED2 + w * 512 + tid * 8);
        v4u o = {tagL, __float_as_uint(s[0]), __float_as_uint(s[1]), tagH};
        GST(o, OFF_UV + (64 * (a - 4) + tid) * 16);
      }
    }
    SB();
    TS(6);
#pragma unroll
    for (int i = NHC; i < 28; ++i) GST(SENT, hc + (i / 14) * 32 * HTILE + (i % 14) * 8192);
    VMW(0);
    BAR();
    if (tid == 0) GST8(((v2u){tagL, tagH}), OFF_CFL + a * 128);
    TS(7);
    if (a >= 3 && a < 10) modround(L_AR0, 32 + a);
    TS(29); TSEND();
    return;
  }

  v4u xr; u64 e0;
  first_loads(xr, e0);
  const u32 p = xcd * 28 + j;
  const u32 jq = (j * 37) >> 8;
  const u32 grp = xcd * 4 + jq, sl = j - 7 * jq;
  const u32 chw = wave >> 2;

  const u32 pi = ((2 * p + chw) * 256 + (tid & 255)) * 2;
  u32 pAr, pAi, pBr, pBi, pCr, pCi, pLd2, dk2;
  asm volatile("global_load_ushort %0, %1, %2 ; ALOAD" : "=v"(pAr) : "v"(pi), "s"(Are) : "memory");
  asm volatile("global_load_ushort %0, %1, %2 ; ALOAD" : "=v"(pAi) : "v"(pi), "s"(Aim) : "memory");
  asm volatile("global_load_ushort %0, %1, %2 ; ALOAD" : "=v"(pBr) : "v"(pi), "s"(Bre) : "memory");
  asm volatile("global_load_ushort %0, %1, %2 ; ALOAD" : "=v"(pBi) : "v"(pi), "s"(Bim) : "memory");
  asm volatile("global_load_ushort %0, %1, %2 ; ALOAD" : "=v"(pCr) : "v"(pi), "s"(Cre) : "memory");
  asm volatile("global_load_ushort %0, %1, %2 ; ALOAD" : "=v"(pCi) : "v"(pi), "s"(Cim) : "memory");
  asm volatile("s_load_dword %0, %1, %2 ; SLOAD" : "=s"(pLd2) : "s"(logdt), "s"(4 * p) : "memory");
  asm volatile("s_load_dword %0, %1, %2 ; SLOAD" : "=s"(dk2) : "s"(Dskip), "s"(4 * p) : "memory");
  const bool isw1 = xcd < 4;
  u32 wt_dst = 0;
  if (wave < 7) {
    const char* src; u32 rstride;
    if (isw1) {
      const u32 kap = p >> 2, fg = (p & 3) * 7 + wave;
      src = (const char*)w1 + kap * (16 * F * 2) + fg * 128; rstride = F * 2;
      wt_dst = OFF_W1T + (fg * 56 + kap) * 1024;
    } else {
      const u32 kk = p - 112;
      src = (const char*)w2 + kk * (16 * D * 2) + wave * 128; rstride = D * 2;
      wt_dst = OFF_W2T + (wave * 224 + 2 * kk) * 1024;
    }
    const u32 vo = (lane >> 3) * rstride + (lane & 7) * 16;
    dma16(lds0 + wave * KG_W, src, vo);
    dma16(lds0 + wave * KG_W + 1024, src + 8 * rstride, vo);
  } else {
    set_m0(lds0 + L_PF);
    const u32 a = (lane >> 4) * 8 + xcd, lc = a < 21 ? a : (a < 28 ? a + 7 : a - 7);
    asm volatile("global_load_lds_dword %0, %1" :: "v"((16 * j + (lane & 15)) * (12 * D) + lc * 128), "s"(wmod) : "memory");
    u32 vo = (j & 7) * 8192 + lane * 128; vo = vo < CODE_PF ? vo : 0;
    asm volatile("global_load_lds_dword %0, %1" :: "v"(vo), "s"(pc & ~255ull) : "memory");
  }
  const int h = lane >> 5, trq = (lane & 15) >> 2, trc = ((lane >> 4) & 1) * 32 + (lane & 3) * 8;
  const Ln1 lna = ln1_addr(b, lane, wave);
  int wt_a = wave * KG_W + (isw1 ? (8 * h + trq) * 128 + trc : (4 * h + trq) * 128 + 2 * trc), wt_b = wt_a + (isw1 ? 512 : 1024);
  int wt_c = wt_a + (isw1 ? 64 : 8), wt_d = wt_b + (isw1 ? 64 : 8), wt_g = lane * 16 + wt_dst, wt_h = wt_g + (isw1 ? 28 * 1024 : 1024);
  tie(wt_a); tie(wt_b); tie(wt_c); tie(wt_d); tie(wt_g); tie(wt_h);
  int rd_src, rd_dst, rd_dk;
  {
    const int jl = lane >> 3, iq = lane & 7, wq = wave & 3, kl = L_KLIN + chw * KLIN_CH;
    rd_src = chw * 4 * KG_W + 144 * (8 * wq + (wq == 3 ? 0 : jl)) + 16 * iq;
    if (wq < 2) {
      const int jj = 8 * wq + jl, hi = iq >> 2;
      rd_dst = (hi & jj) ? kl + 4 * 641 : kl + 512 * hi + ((256 * iq + 4 * jj) >> hi); rd_dk = (hi & jj) ? 0 : 64 >> hi;
    } else if (wq == 2) {
      const int hi = jl >> 2;
      rd_dst = kl + (hi ? 4 * (512 + 8 * iq + 32 * (jl & 2) + (jl & 1)) : 4 * (384 + 16 * iq + jl)); rd_dk = 16 >> hi;
    } else { rd_dst = kl + 4 * (lane ? 641 : 640); rd_dk = lane ? 0 : 4; }
    tie(rd_src); tie(rd_dst); tie(rd_dk);
  }
  int kap[7];
  {
    const v2f f0 = bc((float)(4 * tid)) + (v2f){-3.f, -2.f};
    u32 b0 = pk(f0), b1 = pk(f0 + bc(2.f)), b2 = pk(f0 + bc(4.f)), b3 = pk(f0 + bc(6.f));
    asm("" : "+v"(b0), "+v"(b1), "+v"(b2), "+v"(b3));
    kap[0] = (b0 & 0xffffu) * 4; kap[1] = (b0 >> 16) * 4; kap[2] = (b1 & 0xffffu) * 4; kap[3] = (b1 >> 16) * 4;
    kap[4] = (b2 & 0xffffu) * 4; kap[5] = (b2 >> 16) * 4; kap[6] = (b3 & 0xffffu) * 4;
    if (wave == 0) {
#pragma unroll
      for (int e = 0; e < 7; ++e) { const int l = 4 * tid + e - 3; if (l < 128) kap[e] = L_KLIN + 4 * (l < 0 ? 644 : l); }
    }
#pragma unroll
    for (int e = 0; e < 7; ++e) tie(kap[e]);
  }
  TS(28);
  if (!LNF && wave < 7) VMW(0);
  asm volatile("s_waitcnt vmcnt(2) lgkmcnt(0) ; TIE" : "+v"(xr), "+s"(e0), "+v"(pAr), "+v"(pAi), "+v"(pBr), "+v"(pBi), "+v"(pCr), "+v"(pCi), "+s"(pLd2), "+s"(dk2) :: "memory");
  TS(27);
  gettag(e0, tagL, tagH);
  pLd2 = __builtin_amdgcn_readfirstlane(pLd2); dk2 = __builtin_amdgcn_readfirstlane(dk2);
  if (LNF) ln1_publish(lds, rs, xr, lna, b, lane, wave, tagL, tagH);
  if (wave < 7) {
    if (LNF) VMW(2);
    v2u a0 = trd(lds + wt_a), a1 = trd(lds + wt_b), b0 = trd(lds + wt_c), b1 = trd(lds + wt_d);
    GST(((v4u){a0.x, a0.y, a1.x, a1.y}), wt_g);
    GST(((v4u){b0.x, b0.y, b1.x, b1.y}), wt_h);
  }
  SB();
  if (!LNF) ln1_publish(lds, rs, xr, lna, b, lane, wave, tagL, tagH);
  TS(1);

  v4u gx0, gx1, gs0, gs1, gm;
  const int mo = OFF_MOD + (((lane & 3) == 3 ? 4 : (lane & 3)) * 112 + (p >> 1)) * 16;
  auto sweep = [&]() {
    gx0 = GLDS(tid * 16, OFF_XT + p * 16384); gx1 = GLDS(tid * 16, OFF_XT + p * 16384 + 8192);
    gs0 = GLDS(tid * 16, OFF_STAT); gs1 = GLDS(tid * 16, OFF_STAT + 8192);
  };

  {
    const float dt = rbf(expf(chw ? bhi(pLd2) : blo(pLd2)));
    const float er = (dt * blo(pAr)) * 1.4426950408889634f, ei = (dt * blo(pAi)) * 0.15915494309189535f;
    const v2f dB = {dt * blo(pBr), dt * blo(pBi)}, Cc = {blo(pCr), blo(pCi)};
    v2f X = cmul(Cc, dB, cneg(dB));
    X.y = -X.y;
    const v2f e1 = cexpx(er, ei, 1.f), e16 = cexpx(er, -ei, 16.f), e512 = cexpx(er, ei, 512.f);
    const v2f e1n = cneg(e1), e16n = cneg(e16), e512n = cneg(e512);
    set_m0(lds0 + wave * KG_W);
    st_tid<0>(0x00003f80u);
    v2f Y = e1, Y4, Y8, Y12;
#define YROW(jj) st_tid<jj * 272>(pk(Y)); if (jj == 4) Y4 = Y; if (jj == 8) Y8 = Y; if (jj == 12) Y12 = Y; if (jj < 15) Y = cmul(Y, e1, e1n);
    YROW(1) YROW(2) YROW(3) YROW(4) YROW(5) YROW(6) YROW(7) YROW(8) YROW(9) YROW(10) YROW(11) YROW(12) YROW(13) YROW(14) YROW(15)
    st_tid<16 * 272>(pk(e512));
    st_tid<17 * 272>(pk(cmul(Y4, e512, e512n)));
    Y = cmul(Y8, e512, e512n);  st_tid<18 * 272>(pk(Y));
    st_tid<19 * 272>(pk(cmul(Y12, e512, e512n)));
    v2f E = cmul(e512, e512, e512n); st_tid<20 * 272>(pk(E));
    Y = cmul(Y, e512, e512n);   st_tid<21 * 272>(pk(Y));
    E = cmul(E, e512, e512n);   st_tid<22 * 272>(pk(E));
    Y = cmul(Y, e512, e512n);   st_tid<23 * 272>(pk(Y));
    E = cmul(E, e512, e512n);   st_tid<24 * 272>(pk(E));
    const int r = lane & 31;
    v4u yf[8];
    LAS char* py = lds + wave * KG_W + (r < 24 ? r : 24) * 272 + 16 * h;
    LAS char* px = lds + wave * KG_W + r * 272 + 16 * h;
#pragma unroll
    for (int m = 0; m < 8; ++m) yf[m] = *(LAS v4u*)(py + 32 * m);
    TS(25);
#define XROW(i) st_tid<(i) * 272>(pk(X)); if ((i) < 31) X = cmul(X, e16, e16n);
#define XROW4(i) XROW(i) XROW(i + 1) XROW(i + 2) XROW(i + 3)
    if (wave < 7) { if (LNF) VMW(0); else VMW(2); }
    asm volatile("s_barrier" ::: "memory");
    if (tid == 448) GST8(((v2u){tagL, tagH}), OFF_WFL + p * 128);
    TS(18);
    {
      const int o = OFF_X1 + (tid >> 3) * XHALF + p * 128 + (tid & 7) * 16;
      GST(SENT, o); GST(SENT, o + 64 * XHALF);
      SB();
    }
    XROW4(0) XROW4(4) XROW4(8) XROW4(12) XROW4(16) XROW4(20) XROW4(24) XROW4(28)
    TS(26);
    if (SWPOS == 0) sweep();
    SB();
    v16f acc = {0, 0, 0, 0, 0, 0, 0, 0, 0, 0, 0, 0, 0, 0, 0, 0};
    v4u xf[8];
#pragma unroll
    for (int m = 0; m < 8; ++m) xf[m] = *(LAS v4u*)(px + 32 * m);
#pragma unroll
    for (int m = 0; m < 8; ++m) acc = mfma32(xf[m], yf[m], acc);
#pragma unroll
    for (int q = 0; q < 4; ++q) {
      v4f o = {acc[4 * q], acc[4 * q + 1], acc[4 * q + 2], acc[4 * q + 3]};
      LDS128F(wave * KG_W + 144 * r + 32 * q + 16 * h) = o;
    }
  }
  TS(2);
  const int gfo = OFF_WFL + (wave * 28 + ((lane & 31) < 28 ? (lane & 31) : 0)) * 128;
  v2u gf = GLD8(gfo);
  SB();
  BAR();
  TS(3);
  if (SWPOS == 1) { sweep(); SB(); }
  {
    const v4f s = (LDS128F(rd_src) + LDS128F(rd_src + KG_W)) + (LDS128F(rd_src + 2 * KG_W) + LDS128F(rd_src + 3 * KG_W));
    LDS32F(rd_dst + 3 * rd_dk) = s[3]; LDS32F(rd_dst + 2 * rd_dk) = s[2]; LDS32F(rd_dst + rd_dk) = s[1]; LDS32F(rd_dst) = s[0];
    if ((wave & 3) == 3) {
      const v4u z0 = {0, 0, 0, 0};
      if (lane < 30) LDS128(L_Z + chw * Z_CH + lane * 16) = z0;
      if (lane == 30) LDS128(L_KLIN + chw * KLIN_CH + 4 * 644) = z0;
      if (chw && lane < 13) LDS128(L_FL + lane * 16) = z0;
    }
  }
  auto rest = [&](auto role) {
  constexpr int CV = decltype(role)::value;
  v4u w1f[32];
  const int w1o = OFF_W1T + (sl * 8 + wave) * 28 * 1024;
  auto ldw1 = [&](int i0, int i1) {
    const int l16 = opq(lane * 16);
#pragma unroll
    for (int i = i0; i < i1; ++i) { if (ABL & 4) { w1f[i] = (v4u){0, 0, 0, 0}; tie(w1f[i]); } else w1f[i] = GLDS(l16 + (i & 3) * 1024, w1o + (i >> 2) * 4096); SB(); }
  };
  const u32 wok = __any(gf.x != tagL || gf.y != tagH) ? 0u : 1u;
  if (!WOPT) {
  if (__any(gf.x != tagL || gf.y != tagH)) {
    SPIN {
      CNT(6); __builtin_amdgcn_s_sleep(2); MEMB();
      gf = GLD8(gfo);
      if (!__any(gf.x != tagL || gf.y != tagH)) break;
    }
  }
  } else if (lane == 0) LDS32(L_WA + wave * 4) = wok;
  BAR();
  TS(19);
  if (SWPOS == 2) sweep();
  gm = GLD(mo); SB();
  bool early = true;
  if constexpr (!CV) {
    if (WOPT) { const v4u f0 = LDS128(L_WA), f1 = LDS128(L_WA + 16); early = __builtin_amdgcn_readfirstlane(f0.x & f0.y & f0.z & f0.w & f1.x & f1.y & f1.z & f1.w); }
    if (early) ldw1(0, NPRE);
  }
#pragma unroll
  for (int ch = 0; ch < 2; ++ch) {
    float k[7];
#pragma unroll
    for (int e = 0; e < 7; ++e) k[e] = LDS32F(kap[e] + ch * KLIN_CH);
    const u32 d10 = pk(k[1], k[0]), d21 = pk(k[2], k[1]), d32 = pk(k[3], k[2]), d43 = pk(k[4], k[3]), d54 = pk(k[5], k[4]), d65 = pk(k[6], k[5]);
    const int o = L_KC + ch * KC_CH + (511 - tid) * 8;
    v2u c0 = {d32, d10}, c1 = {d43, d21}, c2 = {d54, d32}, c3 = {d65, d43};
    LDS64(o) = c0; LDS64(o + KC_S) = c1; LDS64(o + 2 * KC_S) = c2; LDS64(o + 3 * KC_S) = c3;
  }
  if (tid < 32) { const v2u z0 = {0, 0}; LDS64(L_KC + (tid >> 2) * KC_S + 4096 + (tid & 3) * 8) = z0; }
  SB();
  const int c16 = lane & 15, g4 = lane >> 4;

  float ga_[2], gd_[2], g2_[2];
  {
    auto badx = [&]() { return ((gx0.x ^ tagL) | (gx1.x ^ tagL) | (gs0.x ^ tagL)) | ((gs1.x ^ tagL) | (gx0.w ^ tagH) | (gx1.w ^ tagH)) | ((gs0.w ^ tagH) | (gs1.w ^ tagH)); };
    if (__any((badx() | (gm.x ^ tagL) | (gm.w ^ tagH)) != 0)) {
      if (__any(badx() != 0)) {
        SPIN { CNT(4); __builtin_amdgcn_s_sleep(2); MEMB(); sweep(); if (!__any(badx() != 0)) break; }
      }
      SPIN { CNT(5); MEMB(); gm = GLD(mo); if (__all(tagok(gm, tagL, tagH))) break; __builtin_amdgcn_s_sleep(2); }
    }
    TS(23);
    const int mv = (p & 1) ? gm.z : gm.y;
    const u32 sa2 = __builtin_amdgcn_readlane(mv, 0), ca2 = __builtin_amdgcn_readlane(mv, 1), ga2 = __builtin_amdgcn_readlane(mv, 2), cb2 = __builtin_amdgcn_readlane(mv, 3);
    const v2f sa = unpk(sa2), g1 = unpk(pk(unpk(ca2) + bc(1.f)));
#pragma unroll
    for (int q = 0; q < 2; ++q) {
      const v4u gx = q ? gx1 : gx0, gs = q ? gs1 : gs0;
      const v2f h0 = fma2((unpk(gx.y) - bc(blo(gs.y))) * bc(__builtin_amdgcn_rcpf(bhi(gs.y))), g1, sa);
      const v2f h1 = fma2((unpk(gx.z) - bc(blo(gs.z))) * bc(__builtin_amdgcn_rcpf(bhi(gs.z))), g1, sa);
      const int lo = opq(L_Z + (q * 512 + tid) * 4);
      LDS32(lo + 480) = pk(h0.x, h1.x);
      LDS32(lo + 480 + Z_CH) = pk(h0.y, h1.y);
      LDS32(lo + (L_XT - L_Z)) = __builtin_amdgcn_perm(gx.z, gx.y, 0x05040100u);
      LDS32(lo + (L_XT - L_Z) + Z_CH) = __builtin_amdgcn_perm(gx.z, gx.y, 0x07060302u);
    }
    SB();
    asm volatile("s_waitcnt lgkmcnt(0)\n\ts_barrier" ::: "memory");
    SB();
    const v2f g2 = unpk(pk(unpk(cb2) + bc(1.f))), ga = unpk(ga2), gd = ga * unpk(dk2);
    ga_[0] = ga.x; ga_[1] = ga.y; gd_[0] = gd.x; gd_[1] = gd.y; g2_[0] = g2.x; g2_[1] = g2.y;
  }
  TS(5);

  const int epi_l = L_Z + 32 * c16 + 8 * g4;
  const int epi_g = OFF_X1 + (c16 >> 1) * XTILE + ((g4 & 1) ? 0 : XHALF) + p * 128 + (c16 & 1) * 32 + (g4 >> 1) * 16;
  auto setf = [&](int f) { MEMB(); if (lane == 0) *(volatile LAS u32*)(lds + f + (wave & 3) * 4) = 1; MEMB(); };
  auto wait1 = [&](int f) {
    _Pragma("unroll 1") for (u32 sp = 0; sp < (1u << 22); ++sp) { if (__builtin_amdgcn_readfirstlane(*(volatile LAS u32*)(lds + f))) break; }
    MEMB();
  };
  auto wait1s = [&](int f) {
    _Pragma("unroll 1") for (u32 sp = 0; sp < (1u << 22); ++sp) { if (__builtin_amdgcn_readfirstlane(*(volatile LAS u32*)(lds + f))) break; __builtin_amdgcn_s_sleep(1); }
    MEMB();
  };
  auto wait4 = [&](int f) {
    _Pragma("unroll 1") for (u32 sp = 0; sp < (1u << 22); ++sp) {
      const v4u v = *(volatile LAS v4u*)(lds + f);
      if (__builtin_amdgcn_readfirstlane(v.x & v.y & v.z & v.w)) break;
      __builtin_amdgcn_s_sleep(1);
    }
    MEMB();
  };
  v4u w2f[28];
  const int w2v = opq(lane * 16), w2o = OFF_W2T + (sl * 8 + wave) * 28 * 1024;
  auto ldw2 = [&](int u) { if (ABL & 1) { w2f[u] = (v4u){0, 0, 0, 0}; tie(w2f[u]); } else w2f[u] = GLDS(w2v + (u & 3) * 1024, w2o + (u >> 2) * 4096); SB(); };
  if constexpr (CV) {
    const int ech = (wave >> 1) & 1, et = (wave & 1) * 2;
    float ega = ech ? ga_[1] : ga_[0], egd = ech ? gd_[1] : gd_[0], eg2 = ech ? g2_[1] : g2_[0];
    int edm = (ech * 8 + et) * 1024 + lane * 16 + L_DUMP, elz = ech * Z_CH + et * 512 + epi_l, ego = epi_g + ech * 64 + et * 8 * XTILE;
    auto epi = [&](int stage, int u) { conv_epi(lds, rs, edm + u * 1024, elz + (4 * stage + u) * 512, ego + (4 * stage + u) * 8 * XTILE, ega, egd, eg2); };
    LAS char* pa = lds + opq(L_KC + ech * KC_CH + (c16 & 3) * KC_S + 4104 - 8 * (c16 >> 2) - 16 * g4 - 256 * (wave & 1) - 64 * 59);
    LAS char* pbz = lds + opq(L_Z + ech * Z_CH + 496 + 32 * c16 - 16 * g4 - 256 * (wave & 1) - 192);
    const int dmo = opq(L_DUMP + (wave & 3) * 4096 + lane * 16);
    v4u B[16];
    auto rdb = [&](int slot, int rho, int dl) { B[slot] = *(LAS v4u*)(pbz + 64 * (8 * dl + 3 - rho)); SB(); };
    auto rda = [&](int rho, int a, int half) {
      const v2u u = *(LAS v2u*)(pa + 64 * (59 - 8 * a - rho) + 8 * half); SB();
      if (half) { w1f[8 * rho + a].z = u.x; w1f[8 * rho + a].w = u.y; } else { w1f[8 * rho + a].x = u.x; w1f[8 * rho + a].y = u.y; }
    };
    auto rd1 = [&](int rho, int n) {
      if (n < 2) rda(rho, 0, n); else if (n < 6) rdb(4 * rho + n - 2, rho, 5 - n); else rda(rho, (n - 4) >> 1, n & 1);
    };
    v4f acc[4];
    __builtin_amdgcn_s_setprio(1);
#pragma unroll
    for (int n = 0; n < 16; ++n) rd1(n / 12, n % 12);
    tie(ega); tie(egd); tie(eg2); tie(edm); tie(elz); tie(ego); SB();
#pragma unroll
    for (int t = 0; t < 4; ++t) acc[t] = (v4f){0, 0, 0, 0};
    {
      int n = 16, m = 0, c = 0;
#pragma unroll
      for (int rho = 0; rho < 4; ++rho) {
#pragma unroll
        for (int a = 0; a < 4; ++a) {
#pragma unroll
          for (int dl = 3 - a; dl >= 0; --dl) {
            acc[a + dl] = mfma16(w1f[8 * rho + a], B[4 * rho + 3 - dl], acc[a + dl]); tie(acc[a + dl]); SB();
            if (n < 48) { rd1(n / 12, n % 12); ++n; if ((c & 1) && n < 48) { rd1(n / 12, n % 12); ++n; } }
            else if (m < 8) { rdb(m, 0, 7 - m); ++m; }
            ++c;
          }
        }
      }
    }
#pragma unroll
    for (int t = 0; t < 4; ++t) LDS128F(dmo + t * 1024) = acc[t];
    setf(F_D0);
#pragma unroll
    for (int n = 0; n < 16; ++n) rda(n >> 3, 4 + ((n >> 1) & 3), n & 1);
    TS(20);
    wait1(F_D0 + ((wave & 3) ^ 1) * 4);
    TS(6);
    epi(0, 0); epi(0, 1);
    setf(F_PA);
    SB();
    TS(7);
#pragma unroll
    for (int t = 0; t < 4; ++t) acc[t] = (v4f){0, 0, 0, 0};
#pragma unroll
    for (int rho = 0; rho < 4; ++rho) {
      LGK0(); SB();
      int n = 0;
#pragma unroll
      for (int a = 0; a < 8; ++a) {
#pragma unroll
        for (int dl = 7 - a; dl >= 0 && dl >= 4 - a; --dl) {
          acc[a + dl - 4] = mfma16(w1f[8 * rho + a], B[8 * (rho & 1) + 7 - dl], acc[a + dl - 4]); tie(acc[a + dl - 4]); SB();
          if (n < 8 && rho < 3) rdb(8 * ((rho + 1) & 1) + n, rho + 1, 7 - n);
          else if (n < 16 && rho < 2) rda(rho + 2, 4 + ((n - 8) >> 1), n & 1);
          else if (n == TRIGN && rho == TRIG) setf(F_TR);
          ++n;
        }
      }
    }
    wait1(F_PA + ((wave & 3) ^ 1) * 4);
#pragma unroll
    for (int t = 0; t < 4; ++t) LDS128F(dmo + t * 1024) = acc[t];
    setf(F_D1);
    if (TRIG == 4) setf(F_TR);
    TS(21);
    wait1(F_D1 + ((wave & 3) ^ 1) * 4);
    epi(1, 0);
    if (TRIG == 5) setf(F_TR);
    epi(1, 1);
    SB();
    __builtin_amdgcn_s_setprio(0);
    TS(8);
    wait1(F_WS + (wave & 3) * 4);
    const int wso = opq(L_WS + (wave & 3) * NWS * 1024 + lane * 16);
#pragma unroll
    for (int i = 0; i < NWS; ++i) w1f[i] = LDS128(wso + i * 1024);
    setf(F_EB);
  } else {
    if (tid == 0) {
      GST8(((v2u){tagL, tagH}), OFF_XFL + p * 32);
      if (b == 0) GST8(((v2u){tagL, tagH}), 0);
    }
    if (!early) {
      SPIN {
        MEMB();
        u32 bad = 0;
#pragma unroll
        for (int q = 0; q < 4; ++q) { const v2u g = GLD8(OFF_WFL + (q * 56 + (lane < 56 ? lane : 0)) * 128); bad |= (g.x ^ tagL) | (g.y ^ tagH); }
        if (!__any(bad != 0)) break;
        __builtin_amdgcn_s_sleep(2);
      }
      ldw1(0, NPRE);
    }
    ldw1(NPRE, 28);
    auto park = [&](int i) {
      if (ABL & 4) return;
      const char* src = ws + w1o + 4 * 28 * 1024;
      const u32 l16 = lane * 16;
      if ((i & 3) == 0) { set_m0(lds0 + L_WS + wave * NWS * 1024 + i * 1024); dma16_dev_o<0>(src + i * 1024, l16); }
      else if ((i & 3) == 1) dma16_dev_o<1024>(src + (i - 1) * 1024, l16);
      else if ((i & 3) == 2) dma16_dev_o<2048>(src + (i - 2) * 1024, l16);
      else dma16_dev_o<3072>(src + (i - 3) * 1024, l16);
    };
#pragma unroll
    for (int i = 0; i < NS1; ++i) park(i);
    TS(20);
    wait4(F_PA);
#pragma unroll
    for (int i = NS1; i < NWS; ++i) park(i);
    const int xfo = OFF_XFL + (wave * 56 + (lane < 56 ? lane : 0)) * 32;
    v2u xf = GLD8(xfo);
    SB();
#pragma unroll
    for (int i = 0; i < NB1; ++i) ldw2(i);
    if (__any(xf.x != tagL || xf.y != tagH)) {
      SPIN { CNT(7); __builtin_amdgcn_s_sleep(2); MEMB(); xf = GLD8(xfo); if (!__any(xf.x != tagL || xf.y != tagH)) break; }
    }
    asm volatile("s_waitcnt vmcnt(%0)" :: "i"(NB1) : "memory");
    setf(F_WS);
    TS(21);
    wait4(F_TR);
    __builtin_amdgcn_s_setprio(2);
    TS(8);
  }

  const int uvo = OFF_UV + (256 * sl + 32 * wave + (lane & 31)) * 16;
  const int gbo = OFF_MOD + (560 + 16 * sl + (tid & 15)) * 16;
  v4u uv;
  const int trb = (8 * h + trq) * 64 + trc;
  const char* xg = ws + OFF_X1 + grp * XTILE + (wave & 3) * 7168;
  const u32 xl = lane * 16;
  auto dma_x = [&](int pass, int i) {
    if (ABL & 32) return;
    const int half = i >= 7, q = i - 7 * half, o = half * XHALF + (q >> 2) * 4096;
    const char* src = xg + pass * 32 * XTILE + o;
    if ((q & 3) == 0) { set_m0(lds0 + pass * XTILE + (wave & 3) * 7168 + o); dma16_dev_o<0>(src, xl); }
    else if ((q & 3) == 1) dma16_dev_o<1024>(src, xl);
    else if ((q & 3) == 2) dma16_dev_o<2048>(src, xl);
    else dma16_dev_o<3072>(src, xl);
  };
  auto accept_x = [&](int pass) {
    const int lx = pass * XTILE + (wave & 3) * 7168;
    float s, q;
    SPIN {
      u32 m = 0;
      v4u t[7], f[7];
#pragma unroll
      for (int i = 0; i < 7; ++i) t[i] = LDS128(lx + i * 1024 + lane * 16);
#pragma unroll
      for (int i = 0; i < 7; ++i) f[i] = trfrag(lds + lx + XHALF + trb + i * 1024);
#pragma unroll
      for (int i = 0; i < 7; ++i) m = max4(m, t[i]);
      s = 0.f; q = 0.f;
#pragma unroll
      for (int i = 0; i < 7; ++i) sumsq(f[i], s, q);
      if (ABL || !__any(m == SENTW || q != q)) break;
      MEMB();
#pragma unroll
      for (int i = 0; i < 7; ++i) m = max4(m, LDS128(lx + XHALF + i * 1024 + lane * 16));
      if (!__any(m == SENTW)) break;
      CNT(pass);
      __builtin_amdgcn_s_sleep(4);
#pragma unroll
      for (int i = 0; i < 14; ++i) dma_x(pass, i);
      VMW(0);
    }
    s = swap32_add(s); q = swap32_add(q);
    if (!h) { v2f o = {s, q}; LDS64F(L_ST + pass * ST_P + lane * 48 + (wave & 3) * 8) = o; }
  };
  uv = GLD(uvo);
  SB();
  if constexpr (!CV) {
#pragma unroll
    for (int i = 0; i < 14; ++i) dma_x(0, i);
    __builtin_amdgcn_s_setprio(0);
    TS(9);
#pragma unroll
    for (int i = NB1; i < NB1 + NB2; ++i) ldw2(i);
    asm volatile("s_waitcnt vmcnt(%0)" :: "i"(NB2) : "memory");
    tie(uv);
    TS(6);
    accept_x(0);
    setf(F_XA);
    TS(10);
    wait4(F_EB);
  } else {
    ldw1(NWS, 28);
    TS(10);
  }
  wait4(F_XA);
  TS(11);
  if (!__all(tagok(uv, tagL, tagH))) {
    SPIN { CNT(8); __builtin_amdgcn_s_sleep(4); MEMB(); uv = GLD(uvo); if (__all(tagok(uv, tagL, tagH))) break; }
  }
  v4u uvf;
  {
    const float u = __uint_as_float(uv.y), v = __uint_as_float(uv.z), uh = rbf(u), vh = rbf(v);
    uvf = (v4u){h ? 0u : pk(uh, u - uh), h ? 0u : pk(vh, v - vh), 0u, 0u};
  }

  const v16f zero16 = {0, 0, 0, 0, 0, 0, 0, 0, 0, 0, 0, 0, 0, 0, 0, 0};
  float sd, rl;
  auto gemm1 = [&](int pass, auto extra) {
    const int lx = pass * XTILE;
    LAS char* pb = lds + opq(lx + trb);
    v4u bf[4];
#pragma unroll
    for (int k = 0; k < 3; ++k) bf[k] = trfrag(pb + k * 1024);
    SB();
    v16f acc = zero16;
    v2u res; v4f a0; float mean, ex2; v4u bs;
    __builtin_amdgcn_s_setprio(2 - CV);
#pragma unroll
    for (int k = 0; k < 28; ++k) {
      if (k + 3 < 28) bf[(k + 3) & 3] = trfrag(pb + (k + 3) * 1024);
      acc = mfma32(w1f[k], bf[k & 3], acc); tie(acc);
      extra(k);
      if (k == 1) {
        res = trd(lds + lx + XHALF + (64 * sl + 8 * wave + 4 * h + trq) * 64 + trc);
        a0 = LDS128F(L_ST + pass * ST_P + (lane & 31) * 48 + h * 16);
      }
      if (k == 7) LDS64(L_RES + pass * RES_P + (lane & 31) * 144 + 16 * wave + 8 * h) = res;
      if (k == 9) mean = pin(swap32_add(pin(a0[0]) + opf(a0[2])) * (1.f / D));
      if (k == 11) ex2 = pin(swap32_add(pin(a0[1]) + opf(a0[3])) * (1.f / D));
      if (k == 13) ex2 = pin(rbf(fmaxf(pin(ex2) - mean * mean, 0.f)));
      if (k == 15) ex2 = pin(__builtin_amdgcn_sqrtf(rbf(pin(ex2) + 1e-5f)));
      if (k == 17) { sd = rbf(pin(ex2)); rl = pin(__builtin_amdgcn_rcpf(sd) * -1.4426950408889634f); }
      if (k == 19) { mean = pin(mean); bs = (v4u){h ? 0u : pk(-mean, -mean), h ? 0u : pk(sd, sd), 0u, 0u}; tie(bs); }
      SB();
    }
    acc = mfma32(uvf, bs, acc); tie(acc);
    SB();
    __builtin_amdgcn_s_setprio(0);
    return acc;
  };
  auto epi1 = [&](int pass, v16f acc, auto pre) {
    tie(acc);
    u32 hp[8];
#pragma unroll
    for (int e = 0; e < 8; ++e) {
      const float a0 = opf(acc[2 * e]), a1 = opf(acc[2 * e + 1]);
      const float e0_ = __builtin_amdgcn_exp2f(a0 * rl), e1_ = __builtin_amdgcn_exp2f(a1 * rl);
      hp[e] = pks(a0 * __builtin_amdgcn_rcpf(fmaf(e0_, sd, sd)), a1 * __builtin_amdgcn_rcpf(fmaf(e1_, sd, sd)));
    }
    const int ho = opq(OFF_H + (grp + 32 * pass) * HTILE + (16 * sl + 2 * wave) * 1024 + lane * 16);
    pre();
    v4u h0 = {hp[0], hp[1], hp[2], hp[3]}, h1 = {hp[4], hp[5], hp[6], hp[7]};
    if (ABL & 16) { tie(h0); tie(h1); } else { GST(h0, ho); GST(h1, ho + 1024); }
    SB();
  };
  v2u cf = GLD8(OFF_CFL + grp * 128);
  SB();
  auto h_marked = [&]() {
    if (__any(cf.x != tagL || cf.y != tagH)) {
      SPIN { CNT(10); MEMB(); cf = GLD8(OFF_CFL + grp * 128); if (!__any(cf.x != tagL || cf.y != tagH)) break; __builtin_amdgcn_s_sleep(4); }
    }
  };
  v16f acc;
  if constexpr (!CV) {
    acc = gemm1(0, [&](int k) { if (!(k & 1)) dma_x(1, k >> 1); if (STAG && k == 25) setf(F_S0); });
    TS(12);
    epi1(0, acc, h_marked);
    if (ABL & 16) VMW(0); else VMW(2);
    __builtin_amdgcn_s_setprio(2);
    accept_x(1);
    setf(F_XB);
    wait4(F_XB);
    TS(13);
    acc = gemm1(1, [&](int k) { if (!(k & 1) && NB1 + NB2 + (k >> 1) < 28) ldw2(NB1 + NB2 + (k >> 1)); if (STAG && k == 25) setf(F_S1); });
  } else {
    if (STAG) wait1s(F_S0 + (wave & 3) * 4);
    acc = gemm1(0, [&](int k) { if (k >= 28 - 2 * NCA && !(k & 1)) ldw2((k - 28 + 2 * NCA) >> 1); });
    TS(12);
    epi1(0, acc, h_marked);
#pragma unroll
    for (int i = NCA; i < 14; ++i) ldw2(i);
    TS(9);
    wait4(F_XB);
    if (STAG) wait1s(F_S1 + (wave & 3) * 4);
    TS(13);
    acc = gemm1(1, [&](int k) { if (!(k & 1)) ldw2(14 + (k >> 1)); });
  }
  TS(14);
  epi1(1, acc, [] {});
  if constexpr (!CV) {
#pragma unroll
    for (int i = NB1 + NB2 + 14; i < 28; ++i) ldw2(i);
  }
  TS(24);

  const int hoA = OFF_H + grp * HTILE + wave * 14 * 1024;
  v4u hf[14];
  v16f c0, c1;
  auto ldh = [&](int ho) {
    const int l16 = opq(lane * 16);
#pragma unroll
    for (int i = 0; i < 14; ++i) { if (ABL & 2) { hf[i] = (v4u){0, 0, 0, 0}; tie(hf[i]); } else hf[i] = GLDS(l16 + (i & 3) * 1024, ho + (i >> 2) * 4096); SB(); }
  };
  auto gemm2 = [&](int ho) {
    c0 = zero16; c1 = zero16;
#pragma unroll
    for (int i = 0; i < 14; ++i) { c0 = mfma32(w2f[2 * i], hf[i], c0); c1 = mfma32(w2f[2 * i + 1], hf[i], c1); tie(c0); tie(c1); SB(); }
    if (!ABL && __any(c0[0] != c0[0])) {
      SPIN {
        MEMB();
        CNT(ho == hoA ? 2 : 3);
        ldh(ho);
        u32 m = 0;
#pragma unroll
        for (int i = 0; i < 14; ++i) m = max4(m, hf[i]);
        if (!__any(m == SENTW)) break;
      }
      c0 = zero16; c1 = zero16;
#pragma unroll
      for (int i = 0; i < 14; ++i) { c0 = mfma32(w2f[2 * i], hf[i], c0); c1 = mfma32(w2f[2 * i + 1], hf[i], c1); }
    }
  };
  auto put_red = [&](int lr) {
    const int o = opq(lr + wave * RED_W + h * RED_S + (lane & 31) * 16);
#pragma unroll
    for (int q = 0; q < 4; ++q) {
      v4f o0 = {c0[4 * q], c0[4 * q + 1], c0[4 * q + 2], c0[4 * q + 3]}, o1 = {c1[4 * q], c1[4 * q + 1], c1[4 * q + 2], c1[4 * q + 3]};
      LDS128F(o + 2 * q * RED_S) = o0;
      LDS128F(o + (8 + 2 * q) * RED_S + 32) = o1;
    }
    SB();
  };
  v4u gbv;
  auto finish = [&](int pass, int t) {
    const int dq = t & 15, l = t >> 4;
    const int lr = opq(pass * L_REDB + (dq >> 1) * RED_S + (dq & 1) * (8 * RED_S + 32) + l * 16), lr2 = opq(lr + 4 * RED_W);
    v4f s0 = LDS128F(lr), s1 = LDS128F(lr2);
#pragma unroll
    for (int w = 1; w < 4; ++w) { s0 += LDS128F(lr + w * RED_W); s1 += LDS128F(lr2 + w * RED_W); }
    s0 += s1;
    const v2u rv = LDS64(L_RES + pass * RES_P + l * 144 + dq * 8);
    const v2u o = {pk(fma2(unpk(gbv.y), s0.xy, unpk(rv.x))), pk(fma2(unpk(gbv.z), s0.zw, unpk(rv.y)))};
    *(GAS v2u*)(out + (pass * 1024 + grp * 32 + l) * D + 64 * sl + 4 * dq) = o;
  };
  gbv = GLD(gbo);
  SB();
  BAR();
  TS(15);
  __builtin_amdgcn_s_setprio(1);
  ldh(hoA);
  __builtin_amdgcn_s_setprio(0);
  gemm2(hoA);
  if (!__all(tagok(gbv, tagL, tagH))) {
    SPIN { CNT(9); __builtin_amdgcn_s_sleep(4); MEMB(); gbv = GLD(gbo); if (__all(tagok(gbv, tagL, tagH))) break; }
  }
  TS(16);
  put_red(L_REDA);
  setf(F_RA + CV * 16);
  ldh(hoA + 32 * HTILE);
  TS(22);
  if (TAIL == 1 && !CV) {
    wait4(F_RA); wait4(F_RA + 16);
    finish(0, tid); finish(0, tid + 256);
  }
  SB();
  TS(4);
  gemm2(hoA + 32 * HTILE);
  put_red(L_REDB);
  TS(17);
  if (TAIL == 2 && !CV) { wait4(F_RA); wait4(F_RA + 16); finish(0, tid); finish(0, tid + 256); }
  BAR();
  if (TAIL == 0) finish(0, tid);
  finish(1, tid);
  };
  if (wave < 4) rest(Role<0>{}); else rest(Role<1>{});
  TS(29); TSEND();
}

extern "C" void kernel_launch(void* const* d_in, const int* in_sizes, int n_in, void* d_out, int out_size, void* d_ws, size_t ws_size,
                              hipStream_t stream) {
  k_all<<<256, 512, 0, stream>>>((const u16*)d_in[0], (const u16*)d_in[1], (const u16*)d_in[2], (const u16*)d_in[3], (const u16*)d_in[4],
                                 (const u16*)d_in[5], (const u16*)d_in[6], (const u16*)d_in[7], (const u16*)d_in[8], (const u16*)d_in[10],
                                 (const u16*)d_in[11], (const u16*)d_in[12], (const u16*)d_in[13], (u16*)d_out, (char*)d_ws);
}
```

```cpp
#include <hip/hip_runtime.h>
#include <stdint.h>

typedef unsigned short u16;
typedef unsigned u32;
typedef unsigned long long u64;
typedef u32 v4u __attribute__((ext_vector_type(4)));
typedef u32 v2u __attribute__((ext_vector_type(2)));
typedef float v4f __attribute__((ext_vector_type(4)));
typedef float v2f __attribute__((ext_vector_type(2)));
typedef float v16f __attribute__((ext_vector_type(16)));
typedef __bf16 bf8 __attribute__((ext_vector_type(8)));
typedef __bf16 bf2 __attribute__((ext_vector_type(2)));
typedef short s4 __attribute__((ext_vector_type(4)));
#define GAS __attribute__((address_space(1)))
#define LAS __attribute__((address_space(3)))
#define DEV __device__ __forceinline__

constexpr int D = 448, F = 1792;

constexpr int OFF_STAT = 4096;
constexpr int OFF_MOD = 20480;
constexpr int OFF_UV = 32768;
constexpr int OFF_CFL = 61440;
constexpr int OFF_WFL = 65536;
constexpr int OFF_XFL = 98304;
constexpr int OFF_XT = 131072;
constexpr int OFF_W1T = 4 << 20;
constexpr int OFF_W2T = 6 << 20;
constexpr int OFF_X1 = 8 << 20;
constexpr int OFF_H = 12 << 20;
constexpr int OFF_DBG = 24 << 20;
constexpr int WS_BYTES = 32 << 20;
constexpr int XTILE = 57344, XHALF = 28672, HTILE = 114688;

constexpr int KG_W = 8704;
constexpr int L_KC = 0, KC_S = 4160, KC_CH = 4 * KC_S;
constexpr int L_LNT = 69632;
constexpr int L_IX = 81920;
constexpr int L_STX = 76800;
constexpr int L_KLIN = 70224, KLIN_CH = 2688;
constexpr int NWS = 13, L_WS = 57344;
constexpr int L_Z = 110592, Z_CH = 4608;
constexpr int L_XT = 119808;
constexpr int L_DUMP = 129024;
constexpr int L_XA = 0, L_XB = XTILE;
constexpr int RED_S = 576, RED_W = 16 * RED_S, L_REDA = 0, L_REDB = 8 * RED_W;
constexpr int L_RES = 147456, RES_P = 4608;
constexpr int L_ST = 156672, ST_P = 1536;
constexpr int L_PF = 159744;
constexpr int L_FL = 160000;
constexpr int F_D0 = L_FL, F_D1 = L_FL + 16, F_EB = L_FL + 32, F_XA = L_FL + 48, F_XB = L_FL + 64, F_PA = L_FL + 80, F_TR = L_FL + 96,
              F_WS = L_FL + 112, F_RA = L_FL + 128, F_XL = L_FL + 160, F_S0 = L_FL + 176, F_S1 = L_FL + 192,
              F_G1 = L_FL + 208, F_P = L_FL + 224, F_R = L_FL + 256, F_K = L_FL + 272, F_H = L_FL + 288, F_W = L_FL + 304;
constexpr int L_WA = 160320;
constexpr int LDS_BYTES = 160384;
constexpr int L_AR0 = 0, L_AR1 = 81920;
constexpr int L_ASC = 57344;
constexpr int L_ARED = 58368;
constexpr int L_AG = 60416;
constexpr int L_ARED2 = 62464;

constexpr u32 SPIN_CAP = 1u << 14;
constexpr u32 SENTW = 0xffbfffbfu;
#define CODE_PF 36864
#define NPRE 14
#define NS1 8
#define N1 28
#define SDLY 0
#define PKEPI 1
#define NB2 6
#define NB1 4
#define NC1 21
#define NCA 14
#define LNF 0
#define GMPOS 1
#define WOPT 0
#define SWPOS 1
#define NHC 7
#define STAG 0
#define NHX 3
#define WSOFF 0
#define XAP 0
#define XBK 0
#define RFL 0
#define NHE 14
#define CLRPOS 1
#define N0 4
#define SWE 0
#define IXD 0
#define CPR 0
#define ABL 0
#define TAIL 2
#define TRIG 4
#define TRIGN 8
#define VPOS 12

DEV float blo(u32 v) { return __uint_as_float(v << 16); }
DEV float bhi(u32 v) { return __uint_as_float(v & 0xffff0000u); }
DEV v2f unpk(u32 v) { v2f r = {blo(v), bhi(v)}; return r; }
DEV u32 pk(float a, float b) { bf2 r = {(__bf16)a, (__bf16)b}; return __builtin_bit_cast(u32, r); }
DEV u32 pk(v2f a) { return pk(a.x, a.y); }
DEV float rbf(float a) { return blo(pk(a, a)); }
DEV u32 umax(u32 a, u32 b) { return a > b ? a : b; }
DEV u32 max4(u32 m, v4u v) { return umax(umax(umax(umax(m, v.x), v.y), v.z), v.w); }
DEV v2f fma2(v2f a, v2f b, v2f c) { return __builtin_elementwise_fma(a, b, c); }
DEV v2f bc(float a) { v2f r = {a, a}; return r; }
DEV v4f mfma16(v4u a, v4u b, v4f c) { return __builtin_amdgcn_mfma_f32_16x16x32_bf16(__builtin_bit_cast(bf8, a), __builtin_bit_cast(bf8, b), c, 0, 0, 0); }
DEV v16f mfma32(v4u a, v4u b, v16f c) { return __builtin_amdgcn_mfma_f32_32x32x16_bf16(__builtin_bit_cast(bf8, a), __builtin_bit_cast(bf8, b), c, 0, 0, 0); }
DEV float dot2(u32 a, u32 b, float c) { return __builtin_amdgcn_fdot2_f32_bf16(__builtin_bit_cast(bf2, a), __builtin_bit_cast(bf2, b), c, false); }
DEV void sumsq(v4u f, float& s, float& q) {
  s = dot2(f.x, 0x3f803f80u, s); q = dot2(f.x, f.x, q); s = dot2(f.y, 0x3f803f80u, s); q = dot2(f.y, f.y, q);
  s = dot2(f.z, 0x3f803f80u, s); q = dot2(f.z, f.z, q); s = dot2(f.w, 0x3f803f80u, s); q = dot2(f.w, f.w, q);
}
DEV v2u trd(LAS char* p) { return __builtin_bit_cast(v2u, __builtin_amdgcn_ds_read_tr16_b64_v4i16((LAS s4*)p)); }
DEV v4u trfrag(LAS char* p) { v2u a = trd(p), b = trd(p + 256); v4u r = {a.x, a.y, b.x, b.y}; return r; }
template <int CTRL, int RM> DEV float dppadd(float v) {
  return v + __builtin_bit_cast(float, __builtin_amdgcn_update_dpp(0, __builtin_bit_cast(int, v), CTRL, RM, 0xf, RM == 0xf));
}
DEV float rdl(float v, int l) { return __builtin_bit_cast(float, __builtin_amdgcn_readlane(__builtin_bit_cast(int, v), l)); }
DEV float swap32_add(float v) { auto r = __builtin_amdgcn_permlane32_swap(__float_as_uint(v), __float_as_uint(v), false, false); return __uint_as_float(r[0]) + __uint_as_float(r[1]); }
DEV float swap16_add(float v) { auto r = __builtin_amdgcn_permlane16_swap(__float_as_uint(v), __float_as_uint(v), false, false); return __uint_as_float(r[0]) + __uint_as_float(r[1]); }
DEV int opq(int v) { asm volatile("" : "+v"(v)); return v; }
DEV float opf(float v) { asm("" : "+v"(v)); return v; }
DEV float pin(float v) { asm volatile("" : "+v"(v)); return v; }
DEV u32 pks(float a, float b) { return pk(opf(a), opf(b)); }
DEV v2f cmul(v2f a, v2f w, v2f wn) { return fma2(a.yx, wn, a * w.xx); }
DEV v2f cneg(v2f w) { v2f r = {-w.y, w.y}; return r; }
DEV v2f cexpx(float er, float ei, float x) {
  const float e = __builtin_amdgcn_exp2f(er * x), ph = __builtin_amdgcn_fractf(ei * x);
  v2f c = {__builtin_amdgcn_cosf(ph), __builtin_amdgcn_sinf(ph)};
  return c * bc(e);
}

#define BAR() asm volatile("s_waitcnt lgkmcnt(0)\n\ts_barrier" ::: "memory")
#define VMW(n) asm volatile("s_waitcnt vmcnt(" #n ")" ::: "memory")
#define LGK0() __builtin_amdgcn_s_waitcnt(0xC07F)
#define MEMB() asm volatile("" ::: "memory")
#define SB() __builtin_amdgcn_sched_barrier(0)
#define CNT(i)
#define SPIN _Pragma("unroll 1") for (u32 sp = 0; sp < SPIN_CAP; ++sp)
#define LDS128(off) (*(LAS v4u*)(lds + (off)))
#define LDS128F(off) (*(LAS v4f*)(lds + (off)))
#define LDS64(off) (*(LAS v2u*)(lds + (off)))
#define LDS64F(off) (*(LAS v2f*)(lds + (off)))
#define LDS32(off) (*(LAS u32*)(lds + (off)))
#define LDS32F(off) (*(LAS float*)(lds + (off)))
#define GLD(off) __builtin_amdgcn_raw_buffer_load_b128(rs, (off), 0, 16)
#define GLDS(voff, soff) __builtin_amdgcn_raw_buffer_load_b128(rs, (voff), (soff), 16)
#define GLD8(off) __builtin_amdgcn_raw_buffer_load_b64(rs, (off), 0, 16)
#define GST8(v, off) __builtin_amdgcn_raw_buffer_store_b64((v), rs, (off), 0, 16)
#define GST(v, off) __builtin_amdgcn_raw_buffer_store_b128((v), rs, (off), 0, 16)

DEV void dma16(u32 ldsaddr, const char* gbase, u32 voff) {
  asm volatile("s_mov_b32 m0, %0\n\ts_nop 0\n\tglobal_load_lds_dwordx4 %1, %2" :: "s"(ldsaddr), "v"(voff), "s"(gbase) : "memory");
}
DEV void dma16_dev(u32 ldsaddr, const char* gbase, u32 voff) {
  asm volatile("s_mov_b32 m0, %0\n\ts_nop 0\n\tglobal_load_lds_dwordx4 %1, %2 sc1" :: "s"(ldsaddr), "v"(voff), "s"(gbase) : "memory");
}
DEV void dma4_dev(u32 ldsaddr, const char* gbase, u32 voff) {
  asm volatile("s_mov_b32 m0, %0\n\ts_nop 0\n\tglobal_load_lds_dword %1, %2 sc1" :: "s"(ldsaddr), "v"(voff), "s"(gbase) : "memory");
}
DEV void set_m0(u32 v) { asm volatile("s_mov_b32 m0, %0\n\ts_nop 0" :: "s"(v) : "memory"); }
template <int OFF> DEV void dma16_dev_o(const char* gbase, u32 voff) {
  asm volatile("global_load_lds_dwordx4 %0, %1 offset:%2 sc1" :: "v"(voff), "s"(gbase), "i"(OFF) : "memory");
}
template <int OFF> DEV void dma16_o(const char* gbase, u32 voff) {
  asm volatile("global_load_lds_dwordx4 %0, %1 offset:%2" :: "v"(voff), "s"(gbase), "i"(OFF) : "memory");
}
template <class T> DEV void tie(T& v) { asm volatile("" : "+v"(v)); }
template <int OFF> DEV void st_tid(u32 v) { asm volatile("ds_write_addtid_b32 %0 offset:%1" :: "v"(v), "i"(OFF) : "memory"); }

#define TS(i)
#define TSEND()

template <int V> struct Role { static constexpr int value = V; };
constexpr int kpos(int k) { return L_KLIN + 4 * (k + (k >> 5)); }
struct IxTab { int v[4][4][64][4]; };
constexpr int tap_entry(int l) {
  if (l < 0) return 644;
  if (l < 256) return l;
  const int sh = l < 512 ? 1 : (l < 1024 ? 2 : 3), half = 1 << (sh - 1), rem = l & ((1 << sh) - 1);
  int q = l >> sh;
  if (rem > half || (rem == half && (q & 1))) ++q;
  return 128 * sh + q;
}
constexpr IxTab make_ix() {
  IxTab t = {};
  for (int wq = 0; wq < 4; ++wq)
    for (int lane = 0; lane < 64; ++lane) {
      const int jl = lane >> 3, iq = lane & 7;
      int k0 = 0, dk = 0;
      if (wq < 2) {
        const int jj = 8 * wq + jl, hi = iq >> 2;
        k0 = (hi & jj) ? 641 : 128 * hi + ((64 * iq + jj) >> hi); dk = (hi & jj) ? 0 : 16 >> hi;
      } else if (wq == 2) {
        const int hi = jl >> 2;
        k0 = hi ? 512 + 8 * iq + 32 * (jl & 2) + (jl & 1) : 384 + 16 * iq + jl; dk = 4 >> hi;
      } else { k0 = lane ? 641 : 640; dk = lane ? 0 : 1; }
      for (int e = 0; e < 4; ++e) t.v[wq][0][lane][e] = kpos(k0 + e * dk);
      t.v[wq][1][lane][0] = 144 * (8 * wq + (wq == 3 ? 0 : jl)) + 16 * iq;
      for (int e = 0; e < 11; ++e) t.v[wq][1 + (e + 1) / 4][lane][(e + 1) & 3] = kpos(tap_entry(8 * (64 * wq + lane) + e - 3));
    }
  return t;
}
__device__ const IxTab ixtab = make_ix();
DEV bool tagok(v4u g, u32 tagL, u32 tagH) { return g.x == tagL && g.w == tagH; }

struct Ln1 { int lw, lr, go; };
DEV Ln1 ln1_addr(u32 b, int lane, u32 wave) {
  Ln1 a = {L_LNT + (int)wave * 1024 + lane * 16, L_LNT + (int)wave * 1024 + (lane & 3) * 256 + (lane >> 2) * 8,
           OFF_XT + (32 * (int)wave + 2 * (lane >> 2)) * 16384 + (4 * (int)b + (lane & 3)) * 16};
  tie(a.lw); tie(a.lr); tie(a.go);
  return a;
}
DEV void ln1_publish(LAS char* lds, __amdgpu_buffer_rsrc_t rs, v4u xr, Ln1 ad, u32 b, int lane, u32 wave, u32 tagL, u32 tagH) {
  if (wave < 7) {
    LDS128(ad.lw) = xr;
    const v2u ra = LDS64(ad.lr), rb = LDS64(ad.lr + 128);
    float s = 0.f, q = 0.f;
    sumsq(xr, s, q);
    s = dppadd<0xb1, 0xf>(s); q = opf(dppadd<0xb1, 0xf>(q));
    s = dppadd<0x4e, 0xf>(s); q = opf(dppadd<0x4e, 0xf>(q));
    s = dppadd<0x141, 0xf>(s); q = opf(dppadd<0x141, 0xf>(q));
    MEMB();
    if ((lane & 7) == 0) { const v4u o = {__float_as_uint(s), __float_as_uint(q), tagL, tagH}; LDS128(L_STX + wave * 128 + lane * 2) = o; }
    MEMB();
    const v4u o0 = {tagL, ra.x, rb.x, tagH}, o1 = {tagL, ra.y, rb.y, tagH};
    GST(o0, ad.go); GST(o1, ad.go + 16384);
  } else {
    v4u t;
    _Pragma("unroll 1") for (u32 sp = 0; sp < (1u << 22); ++sp) {
      t = *(volatile LAS v4u*)(lds + L_STX + (lane < 56 ? lane : 0) * 16);
      if (!__any(t.z != tagL || t.w != tagH)) break;
      __builtin_amdgcn_s_sleep(1);
    }
    float s = lane < 56 ? __uint_as_float(t.x) : 0.f, q = lane < 56 ? __uint_as_float(t.y) : 0.f;
    s = dppadd<0x128, 0xf>(s); q = dppadd<0x128, 0xf>(q);
    s = swap32_add(swap16_add(s)); q = swap32_add(swap16_add(q));
    const float mean = s * (1.f / D), var = rbf(fmaxf(q * (1.f / D) - mean * mean, 0.f));
    const u32 ms = pk(mean, __builtin_amdgcn_sqrtf(rbf(var + 1e-5f)));
    const v4u o = {tagL, ms, (u32)__builtin_amdgcn_update_dpp(0, (int)ms, 0xb1, 0xf, 0xf, true), tagH};
    if (lane < 8 && !(lane & 1)) GST(o, OFF_STAT + 64 * b + lane * 8);
  }
  MEMB();
}

DEV void conv_epi(LAS char* lds, __amdgpu_buffer_rsrc_t rs, int dump, int lz, int goff, float ga, float gd, float g2) {
  const v4f p0 = LDS128F(dump), p1 = LDS128F(dump + 4096);
  const v2u hh = LDS64(lz + 480), xx = LDS64(lz + (L_XT - L_Z));
  u32 X0 = pk(fma2(bc(ga), p0.xy + p1.xy, fma2(bc(gd), unpk(hh.x), unpk(xx.x)))), X1 = pk(fma2(bc(ga), p0.zw + p1.zw, fma2(bc(gd), unpk(hh.y), unpk(xx.y))));
  asm("" : "+v"(X0), "+v"(X1));
  const u32 S0 = pk(unpk(X0) * bc(g2)), S1 = pk(unpk(X1) * bc(g2));
  auto r0 = __builtin_amdgcn_permlane16_swap(X0, S0, false, false);
  auto r1 = __builtin_amdgcn_permlane16_swap(X1, S1, false, false);
  v4u v = {r0[0], r1[0], r0[1], r1[1]};
  if (ABL & 64) tie(v); else GST(v, goff);
}

__global__ void __launch_bounds__(512, 2) k_all(const u16* __restrict__ x, const u16* __restrict__ Are, const u16* __restrict__ Aim,
    const u16* __restrict__ Bre, const u16* __restrict__ Bim, const u16* __restrict__ Cre, const u16* __restrict__ Cim,
    const u16* __restrict__ logdt, const u16* __restrict__ Dskip, const u16* __restrict__ w1, const u16* __restrict__ w2,
    const u16* __restrict__ cvec, const u16* __restrict__ wmod, u16* __restrict__ out, char* ws) {
  __shared__ __attribute__((aligned(16))) char smem[LDS_BYTES];
  LAS char* lds = (LAS char*)smem;
  const u32 lds0 = (u32)(size_t)lds;
  u64 pc;
  asm volatile("s_getpc_b64 %0" : "=s"(pc));
  asm volatile("" :: "s"(x), "s"(Are), "s"(Aim), "s"(Bre), "s"(Bim), "s"(Cre), "s"(Cim), "s"(logdt), "s"(Dskip), "s"(w1), "s"(w2), "s"(cvec), "s"(wmod), "s"(out), "s"(ws));
  const int tid = threadIdx.x, lane = tid & 63;
  const u32 wave = __builtin_amdgcn_readfirstlane(tid >> 6);
  const u32 b = blockIdx.x, xcd = b & 7, j = b >> 3;
  TS(0);
  const __amdgpu_buffer_rsrc_t rs = __builtin_amdgcn_make_buffer_rsrc(ws, 0, WS_BYTES, 0x00020000);
  auto first_loads = [&](v4u& xr, u64& e0) {
    const u64 xa = (u64)(x + 8 * b * D);
    const v4u srd = {(u32)xa, (u32)(xa >> 32) & 0xffffu, 16 * D, 0x00020000u};
    const u32 vo = wave < 7 ? (lane >> 3) * (2 * D) + 128 * wave + (lane & 7) * 16 : 16 * D;
    asm volatile("buffer_load_dwordx4 %0, %1, %2, 0 offen ; ALOAD" : "=v"(xr) : "v"(vo), "s"(srd) : "memory");
    asm volatile("s_load_dwordx2 %0, %1, 0x0 ; SLOAD" : "=s"(e0) : "s"(ws) : "memory");
  };
  const v4u SENT = {SENTW, SENTW, SENTW, SENTW};
  auto gettag = [&](u64 e0, u32& tl, u32& th) {
    const u64 T = (((u64)(u32)__builtin_amdgcn_readfirstlane((u32)(e0 >> 32)) << 32) | (u32)__builtin_amdgcn_readfirstlane((u32)e0)) + 0x9E3779B97F4A7C15ull; tl = (u32)T; th = (u32)(T >> 32); };
  u32 tagL, tagH;

  if (j >= 28) {
    v4u xr; u64 e0;
    first_loads(xr, e0);
    const u32 a = (j - 28) * 8 + xcd;
    const int h = lane >> 5, trq = (lane & 15) >> 2, trc = ((lane >> 4) & 1) * 32 + (lane & 3) * 8;
    const u32 wv = wave < 7 ? wave : 0;
    auto stage = [&](u32 ldst, const char* src, u32 rstride) {
      if (wave < 7) {
        const u32 vo = ((lane >> 3) + 64 * wave) * rstride + (lane & 7) * 16;
#pragma unroll
        for (int i = 0; i < 8; ++i) dma16(lds0 + ldst + wave * 8192 + i * 1024, src + i * 8 * rstride, vo);
      }
    };
    auto gemv = [&](u32 lsrc, int atab, float& r0, float& r1) {
      v16f c0 = {0, 0, 0, 0, 0, 0, 0, 0, 0, 0, 0, 0, 0, 0, 0, 0}, c1 = c0;
      LAS char* pb = lds + opq(lsrc + wv * 8192 + (8 * h + trq) * 128 + trc);
#pragma unroll
      for (int k = 0; k < 4; ++k) {
        const v4u af = LDS128(atab + wv * 128 + k * 32 + h * 16);
        const v2u b0 = trd(pb + k * 2048), b1 = trd(pb + k * 2048 + 512), b2 = trd(pb + k * 2048 + 64), b3 = trd(pb + k * 2048 + 576);
        c0 = mfma32(af, (v4u){b0.x, b0.y, b1.x, b1.y}, c0); c1 = mfma32(af, (v4u){b2.x, b2.y, b3.x, b3.y}, c1);
      }
      r0 = h ? c1[0] : c0[0]; r1 = h ? c1[1] : c0[1];
    };
    u32 cv;
    asm volatile("global_load_ushort %0, %1, %2 ; ALOAD" : "=v"(cv) : "v"((64 * wv + lane) * 2), "s"(cvec) : "memory");
    const u32 lc = a < 21 ? a : (a < 28 ? a + 7 : a - 7);
    stage(L_AR0, (const char*)wmod + lc * 128, 6 * D * 2);
    const Ln1 lna = ln1_addr(b, lane, wave);
    if (wave == 7) VMW(0);
    asm volatile("s_waitcnt vmcnt(8) lgkmcnt(0) ; TIE" : "+v"(xr), "+s"(e0), "+v"(cv) :: "memory");
    {
      const float cf_ = blo(cv);
      *(LAS u16*)(lds + L_ASC + (64 * wv + lane) * 2) = (u16)pk(cf_ * __builtin_amdgcn_rcpf(1.f + __expf(-cf_)), 0.f);
    }
    gettag(e0, tagL, tagH);
    TS(1);
    ln1_publish(lds, rs, xr, lna, b, lane, wave, tagL, tagH);
    TS(2);
    VMW(2);
    if (a < 3) stage(L_AR1, (const char*)wmod + (25 + a) * 128, 6 * D * 2);
    auto modround = [&](u32 lsrc, u32 lc_) {
      float r0, r1;
      gemv(lsrc, L_ASC, r0, r1);
      LDS32F(L_ARED + wave * 256 + lane * 4) = r0;
      BAR();
      if (tid < 16) {
        v4f s = LDS128F(L_ARED + tid * 16);
#pragma unroll
        for (int w = 1; w < 7; ++w) s += LDS128F(L_ARED + w * 256 + tid * 16);
        v4u o = {tagL, pk(s[0], s[1]), pk(s[2], s[3]), tagH};
        GST(o, OFF_MOD + (16 * lc_ + tid) * 16);
      }
    };
    modround(L_AR0, lc);
    TS(3);
    if (a < 3) { VMW(0); BAR(); modround(L_AR1, 25 + a); }
    if (a >= 4) stage(L_AR1, (const char*)w1 + (a - 4) * 128, F * 2);
    if (a >= 3 && a < 10) stage(L_AR0, (const char*)wmod + (32 + a) * 128, 6 * D * 2);
    const int hc = OFF_H + a * HTILE + tid * 16;
#pragma unroll
    for (int i = 0; i < NHC; ++i) GST(SENT, hc + (i / 14) * 32 * HTILE + (i % 14) * 8192);
    TS(4);
    if (a >= 4) {
      if (wave < 4) {
        const int gi = 336 + (tid < 224 ? tid : 0);
        v4u g;
        SPIN {
          MEMB();
          g = GLD(OFF_MOD + gi * 16);
          if (__all(tagok(g, tagL, tagH))) break;
          __builtin_amdgcn_s_sleep(8);
        }
        v2u f = {g.y, g.z};
        if (tid >= 112) { f.x = pk(unpk(f.x) + bc(1.f)); f.y = pk(unpk(f.y) + bc(1.f)); }
        if (tid < 224) LDS64(L_AG + tid * 8) = f;
      }
      VMW(0);
      BAR();
      TS(5);
      float r0, r1;
      gemv(L_AR1, L_AG + ((lane & 1) ? 0 : 2 * D), r0, r1);
      { v2f o = {r0, r1}; LDS64F(L_ARED2 + wave * 512 + lane * 8) = o; }
      BAR();
      if (tid < 64) {
        v2f s = LDS64F(L_ARED2 + tid * 8);
#pragma unroll
        for (int w = 1; w < 7; ++w) s += LDS64F(L_ARED2 + w * 512 + tid * 8);
        v4u o = {tagL, __float_as_uint(s[0]), __float_as_uint(s[1]), tagH};
        GST(o, OFF_UV + (64 * (a - 4) + tid) * 16);
      }
    }
    SB();
    TS(6);
#pragma unroll
    for (int i = NHC; i < 14 + NHX; ++i) GST(SENT, hc + (i / 14) * 32 * HTILE + (i % 14) * 8192);
    asm volatile("s_waitcnt vmcnt(%0)" :: "i"(NHX) : "memory");
    BAR();
    if (tid == 0) GST8(((v2u){tagL, tagH}), OFF_CFL + a * 128);
    TS(8);
#pragma unroll
    for (int i = 14 + NHX; i < 28; ++i) GST(SENT, hc + 32 * HTILE + (i % 14) * 8192);
    VMW(0);
    BAR();
    if (tid == 0) GST8(((v2u){tagL, tagH}), OFF_CFL + a * 128 + 64);
    TS(7);
    if (a >= 3 && a < 10) modround(L_AR0, 32 + a);
    TS(29); TSEND();
    return;
  }

  v4u xr; u64 e0;
  first_loads(xr, e0);
  const u32 p = xcd * 28 + j;
  const u32 jq = (j * 37) >> 8;
  const u32 grp = xcd * 4 + jq, sl = j - 7 * jq;
  const u32 chw = wave >> 2;

  const u32 pi = ((2 * p + chw) * 256 + (tid & 255)) * 2;
  u32 pAr, pAi, pBr, pBi, pCr, pCi, pLd2, dk2;
  asm volatile("global_load_ushort %0, %1, %2 ; ALOAD" : "=v"(pAr) : "v"(pi), "s"(Are) : "memory");
  asm volatile("global_load_ushort %0, %1, %2 ; ALOAD" : "=v"(pAi) : "v"(pi), "s"(Aim) : "memory");
  asm volatile("global_load_ushort %0, %1, %2 ; ALOAD" : "=v"(pBr) : "v"(pi), "s"(Bre) : "memory");
  asm volatile("global_load_ushort %0, %1, %2 ; ALOAD" : "=v"(pBi) : "v"(pi), "s"(Bim) : "memory");
  asm volatile("global_load_ushort %0, %1, %2 ; ALOAD" : "=v"(pCr) : "v"(pi), "s"(Cre) : "memory");
  asm volatile("global_load_ushort %0, %1, %2 ; ALOAD" : "=v"(pCi) : "v"(pi), "s"(Cim) : "memory");
  asm volatile("s_load_dword %0, %1, %2 ; SLOAD" : "=s"(pLd2) : "s"(logdt), "s"(4 * p) : "memory");
  asm volatile("s_load_dword %0, %1, %2 ; SLOAD" : "=s"(dk2) : "s"(Dskip), "s"(4 * p) : "memory");
  const bool isw1 = xcd < 4;
  u32 wt_dst = 0;
  if (wave < 7) {
    const char* src; u32 rstride;
    if (isw1) {
      const u32 kap = p >> 2, fg = (p & 3) * 7 + wave;
      src = (const char*)w1 + kap * (16 * F * 2) + fg * 128; rstride = F * 2;
      wt_dst = OFF_W1T + (fg * 56 + kap) * 1024;
    } else {
      const u32 kk = p - 112;
      src = (const char*)w2 + kk * (16 * D * 2) + wave * 128; rstride = D * 2;
      wt_dst = OFF_W2T + (wave * 224 + 2 * kk) * 1024;
    }
    const u32 vo = (lane >> 3) * rstride + (lane & 7) * 16;
    dma16(lds0 + wave * KG_W, src, vo);
    dma16(lds0 + wave * KG_W + 1024, src + 8 * rstride, vo);
  } else {
    set_m0(lds0 + L_PF);
    const u32 a = (lane >> 4) * 8 + xcd, lc = a < 21 ? a : (a < 28 ? a + 7 : a - 7);
    asm volatile("global_load_lds_dword %0, %1" :: "v"((16 * j + (lane & 15)) * (12 * D) + lc * 128), "s"(wmod) : "memory");
    u32 vo = (j & 7) * 8192 + lane * 128; vo = vo < CODE_PF ? vo : 0;
    asm volatile("global_load_lds_dword %0, %1" :: "v"(vo), "s"(pc & ~255ull) : "memory");
  }
  if (IXD == 0)
  if (wave < 4) {
    const char* src = (const char*)&ixtab + wave * 4096;
    set_m0(lds0 + L_IX + wave * 4096);
    dma16_o<0>(src, lane * 16); dma16_o<1024>(src, lane * 16); dma16_o<2048>(src, lane * 16); dma16_o<3072>(src, lane * 16);
  }
  const int h = lane >> 5, trq = (lane & 15) >> 2, trc = ((lane >> 4) & 1) * 32 + (lane & 3) * 8;
  const Ln1 lna = ln1_addr(b, lane, wave);
  int wt_a = wave * KG_W + (isw1 ? (8 * h + trq) * 128 + trc : (4 * h + trq) * 128 + 2 * trc), wt_b = wt_a + (isw1 ? 512 : 1024);
  int wt_c = wt_a + (isw1 ? 64 : 8), wt_d = wt_b + (isw1 ? 64 : 8), wt_g = lane * 16 + wt_dst, wt_h = wt_g + (isw1 ? 28 * 1024 : 1024);
  tie(wt_a); tie(wt_b); tie(wt_c); tie(wt_d); tie(wt_g); tie(wt_h);
  if (wave == 7 && lane < 20) LDS128(L_FL + lane * 16) = (v4u){0, 0, 0, 0};
  TS(28);
  if (!LNF && wave < 7) VMW(0);
  asm volatile("s_waitcnt vmcnt(2) lgkmcnt(0) ; TIE" : "+v"(xr), "+s"(e0), "+v"(pAr), "+v"(pAi), "+v"(pBr), "+v"(pBi), "+v"(pCr), "+v"(pCi), "+s"(pLd2), "+s"(dk2) :: "memory");
  TS(27);
  gettag(e0, tagL, tagH);
  pLd2 = __builtin_amdgcn_readfirstlane(pLd2); dk2 = __builtin_amdgcn_readfirstlane(dk2);
  if (LNF) ln1_publish(lds, rs, xr, lna, b, lane, wave, tagL, tagH);
  if (wave < 7) {
    if (LNF) VMW(2);
    v2u a0 = trd(lds + wt_a), a1 = trd(lds + wt_b), b0 = trd(lds + wt_c), b1 = trd(lds + wt_d);
    GST(((v4u){a0.x, a0.y, a1.x, a1.y}), wt_g);
    GST(((v4u){b0.x, b0.y, b1.x, b1.y}), wt_h);
  }
  SB();
  if (!LNF) ln1_publish(lds, rs, xr, lna, b, lane, wave, tagL, tagH);
  if (CLRPOS == 0)
    {
      const int o = OFF_X1 + (tid >> 3) * XHALF + p * 128 + (tid & 7) * 16;
      GST(SENT, o); GST(SENT, o + 64 * XHALF);
      SB();
    }
  TS(1);

    v4u gx[4], gs[4];
    auto sweep = [&]() {
#pragma unroll
      for (int q = 0; q < 4; ++q) { gx[q] = GLDS(tid * 16, OFF_XT + p * 16384 + q * 4096); gs[q] = GLDS(tid * 16, OFF_STAT + q * 4096); SB(); }
    };

  {
    const float dt = rbf(expf(chw ? bhi(pLd2) : blo(pLd2)));
    const float er = (dt * blo(pAr)) * 1.4426950408889634f, ei = (dt * blo(pAi)) * 0.15915494309189535f;
    const v2f dB = {dt * blo(pBr), dt * blo(pBi)}, Cc = {blo(pCr), blo(pCi)};
    v2f X = cmul(Cc, dB, cneg(dB));
    X.y = -X.y;
    const v2f e1 = cexpx(er, ei, 1.f), e16 = cexpx(er, -ei, 16.f), e512 = cexpx(er, ei, 512.f);
    const v2f e1n = cneg(e1), e16n = cneg(e16), e512n = cneg(e512);
    set_m0(lds0 + wave * KG_W);
    st_tid<0>(0x00003f80u);
    v2f Y = e1, Y4, Y8, Y12;
#define YROW(jj) st_tid<jj * 272>(pk(Y)); if (jj == 4) Y4 = Y; if (jj == 8) Y8 = Y; if (jj == 12) Y12 = Y; if (jj < 15) Y = cmul(Y, e1, e1n);
    YROW(1) YROW(2) YROW(3) YROW(4) YROW(5) YROW(6) YROW(7) YROW(8) YROW(9) YROW(10) YROW(11) YROW(12) YROW(13) YROW(14) YROW(15)
    st_tid<16 * 272>(pk(e512));
    st_tid<17 * 272>(pk(cmul(Y4, e512, e512n)));
    Y = cmul(Y8, e512, e512n);  st_tid<18 * 272>(pk(Y));
    st_tid<19 * 272>(pk(cmul(Y12, e512, e512n)));
    v2f E = cmul(e512, e512, e512n); st_tid<20 * 272>(pk(E));
    Y = cmul(Y, e512, e512n);   st_tid<21 * 272>(pk(Y));
    E = cmul(E, e512, e512n);   st_tid<22 * 272>(pk(E));
    Y = cmul(Y, e512, e512n);   st_tid<23 * 272>(pk(Y));
    E = cmul(E, e512, e512n);   st_tid<24 * 272>(pk(E));
    const int r = lane & 31;
    v4u yf[8];
    LAS char* py = lds + wave * KG_W + (r < 24 ? r : 24) * 272 + 16 * h;
    LAS char* px = lds + wave * KG_W + r * 272 + 16 * h;
#pragma unroll
    for (int m = 0; m < 8; ++m) yf[m] = *(LAS v4u*)(py + 32 * m);
    TS(25);
#define XROW(i) st_tid<(i) * 272>(pk(X)); if ((i) < 31) X = cmul(X, e16, e16n);
#define XROW4(i) XROW(i) XROW(i + 1) XROW(i + 2) XROW(i + 3)
    if (wave < 7) VMW(2);
    asm volatile("s_barrier" ::: "memory");
    if (tid == 448) GST8(((v2u){tagL, tagH}), OFF_WFL + p * 128);
    TS(18);
    if (IXD == 1) {
  if (wave < 4) {
    const char* src = (const char*)&ixtab + wave * 4096;
    set_m0(lds0 + L_IX + wave * 4096);
    dma16_o<0>(src, lane * 16); dma16_o<1024>(src, lane * 16); dma16_o<2048>(src, lane * 16); dma16_o<3072>(src, lane * 16);
  }
    set_m0(lds0 + wave * KG_W);
    }
    if (CLRPOS == 1)
    {
      const int o = OFF_X1 + (tid >> 3) * XHALF + p * 128 + (tid & 7) * 16;
      GST(SENT, o); GST(SENT, o + 64 * XHALF);
      SB();
    }
    XROW4(0) XROW4(4) XROW4(8) XROW4(12) XROW4(16) XROW4(20) XROW4(24) XROW4(28)
    TS(26);
    if (SWE == 1 && wave < 4) sweep();
    v16f acc = {0, 0, 0, 0, 0, 0, 0, 0, 0, 0, 0, 0, 0, 0, 0, 0};
    v4u xf[8];
#pragma unroll
    for (int m = 0; m < 8; ++m) xf[m] = *(LAS v4u*)(px + 32 * m);
#pragma unroll
    for (int m = 0; m < 8; ++m) acc = mfma32(xf[m], yf[m], acc);
    tie(acc);
    if (SWE == 2 && wave < 4) sweep();
#pragma unroll
    for (int q = 0; q < 4; ++q) {
      v4f o = {acc[4 * q], acc[4 * q + 1], acc[4 * q + 2], acc[4 * q + 3]};
      LDS128F(wave * KG_W + 144 * r + 32 * q + 16 * h) = o;
    }
  }
  TS(2);
  auto setf = [&](int f) { MEMB(); if (lane == 0) *(volatile LAS u32*)(lds + f + (wave & 3) * 4) = 1; MEMB(); };
  auto wait1 = [&](int f) {
    _Pragma("unroll 1") for (u32 sp = 0; sp < (1u << 22); ++sp) { if (__builtin_amdgcn_readfirstlane(*(volatile LAS u32*)(lds + f))) break; }
    MEMB();
  };
  auto wait1s = [&](int f) {
    _Pragma("unroll 1") for (u32 sp = 0; sp < (1u << 22); ++sp) { if (__builtin_amdgcn_readfirstlane(*(volatile LAS u32*)(lds + f))) break; __builtin_amdgcn_s_sleep(1); }
    MEMB();
  };
  auto wait4n = [&](int f) {
    _Pragma("unroll 1") for (u32 sp = 0; sp < (1u << 22); ++sp) {
      const v4u v = *(volatile LAS v4u*)(lds + f);
      if (__builtin_amdgcn_readfirstlane(v.x & v.y & v.z & v.w)) break;
    }
    MEMB();
  };
  auto wait8n = [&](int f, int g) {
    _Pragma("unroll 1") for (u32 sp = 0; sp < (1u << 22); ++sp) {
      const v4u v = *(volatile LAS v4u*)(lds + f), u = *(volatile LAS v4u*)(lds + g);
      if (__builtin_amdgcn_readfirstlane(v.x & v.y & v.z & v.w & u.x & u.y & u.z & u.w)) break;
    }
    MEMB();
  };
  auto wait4 = [&](int f) {
    _Pragma("unroll 1") for (u32 sp = 0; sp < (1u << 22); ++sp) {
      const v4u v = *(volatile LAS v4u*)(lds + f);
      if (__builtin_amdgcn_readfirstlane(v.x & v.y & v.z & v.w)) break;
      __builtin_amdgcn_s_sleep(1);
    }
    MEMB();
  };
  if (IXD == 1 && wave < 4) VMW(2);
  setf(F_P + chw * 16);
  auto rest = [&](auto role) {
  constexpr int CV = decltype(role)::value;
  v4u w1f[32];
  const int w1o = OFF_W1T + (sl * 8 + wave) * 28 * 1024;
  auto ldw1 = [&](int i0, int i1) {
    const int l16 = opq(lane * 16);
#pragma unroll
    for (int i = i0; i < i1; ++i) { if (ABL & 4) { w1f[i] = (v4u){0, 0, 0, 0}; tie(w1f[i]); } else w1f[i] = GLDS(l16 + (i & 3) * 1024, w1o + (i >> 2) * 4096); SB(); }
  };
  const int c16 = lane & 15, g4 = lane >> 4;
  const int mo = OFF_MOD + (((lane & 3) == 3 ? 4 : (lane & 3)) * 112 + (p >> 1)) * 16;
  v4u gm;
  auto mod2 = [&](int i) { return (u32)__builtin_amdgcn_readlane((p & 1) ? gm.z : gm.y, i); };
  auto mod_ready = [&]() {
    if (!__all(tagok(gm, tagL, tagH))) {
      SPIN { CNT(5); MEMB(); gm = GLD(mo); if (__all(tagok(gm, tagL, tagH))) break; __builtin_amdgcn_s_sleep(2); }
    }
  };
  const int ech = (wave >> 1) & 1, et = (wave & 1) * 2;
  int edm, elz, ego, pao, pbo, dmo;
  float ega, egd, eg2;
  if constexpr (CV) {
    __builtin_amdgcn_s_setprio(CPR);
    wait4n(F_P);
    const int ixo = opq(L_IX + (wave & 3) * 4096 + lane * 16);
    const v4u t0 = LDS128(ixo), t1 = LDS128(ixo + 1024), t2 = LDS128(ixo + 2048), t3 = LDS128(ixo + 3072);
    const int rd_src = t1.x, rd_d[4] = {(int)t0.x, (int)t0.y, (int)t0.z, (int)t0.w};
    const int kap[11] = {(int)t1.y, (int)t1.z, (int)t1.w, (int)t2.x, (int)t2.y, (int)t2.z, (int)t2.w, (int)t3.x, (int)t3.y, (int)t3.z, (int)t3.w};
    wait4n(F_P + 16);
    TS(3);
#pragma unroll
    for (int ch = 0; ch < 2; ++ch) {
      const int o = rd_src + ch * 4 * KG_W;
      const v4f s = (LDS128F(o) + LDS128F(o + KG_W)) + (LDS128F(o + 2 * KG_W) + LDS128F(o + 3 * KG_W));
#pragma unroll
      for (int e = 3; e >= 0; --e) LDS32F(rd_d[e] + ch * KLIN_CH) = s[e];
    }
    if (wave == 7 && lane < 2) LDS32(kpos(644) + lane * KLIN_CH) = 0;
    setf(F_R);
    gm = GLD(mo);
    {
  const int epi_l = L_Z + 32 * c16 + 8 * g4;
  const int epi_g = OFF_X1 + (c16 >> 1) * XTILE + ((g4 & 1) ? 0 : XHALF) + p * 128 + (c16 & 1) * 32 + (g4 >> 1) * 16;
      edm = (ech * 8 + et) * 1024 + lane * 16 + L_DUMP; elz = ech * Z_CH + et * 512 + epi_l; ego = epi_g + ech * 64 + et * 8 * XTILE;
      pao = L_KC + ech * KC_CH + (c16 & 3) * KC_S + 4104 - 8 * (c16 >> 2) - 16 * g4 - 256 * (wave & 1) - 64 * 59;
      pbo = L_Z + ech * Z_CH + 496 + 32 * c16 - 16 * g4 - 256 * (wave & 1) - 192;
      dmo = L_DUMP + (wave & 3) * 4096 + lane * 16;
      tie(edm); tie(elz); tie(ego); tie(pao); tie(pbo); tie(dmo);
    }
    wait4n(F_R);
    TS(19);
    const int ko = opq(L_KC + (255 - (tid & 255)) * 16);
#pragma unroll
    for (int ch = 0; ch < 2; ++ch) {
      float k[11];
#pragma unroll
      for (int e = 0; e < 11; ++e) k[e] = LDS32F(kap[e] + ch * KLIN_CH);
      u32 d[10];
#pragma unroll
      for (int e = 0; e < 10; ++e) d[e] = pk(k[e + 1], k[e]);
#pragma unroll
      for (int c = 0; c < 4; ++c) LDS128(ko + ch * KC_CH + c * KC_S) = (v4u){d[c + 6], d[c + 4], d[c + 2], d[c]};
    }
    if (wave == 7 && lane < 16) LDS128(L_KC + (lane >> 1) * KC_S + 4096 + (lane & 1) * 16) = (v4u){0, 0, 0, 0};
    mod_ready();
    setf(F_K);
    {
      const u32 esh = ech ? 0 : 16;
      ega = bhi(mod2(2) << esh); egd = ega * bhi(dk2 << esh); eg2 = rbf(1.f + bhi(mod2(3) << esh));
      tie(ega); tie(egd); tie(eg2);
    }
    TS(23);
    wait8n(F_K, F_H);
    SB();
  } else {
    if (SDLY) __builtin_amdgcn_s_sleep(SDLY);
    if (SWE == 0) sweep();
    gm = GLD(mo);
    const int gfo = OFF_WFL + (wave * 56 + (lane < 56 ? lane : 0)) * 128;
    v2u gf;
    SB();
    if (lane < 15) LDS128(L_Z + (wave >> 1) * Z_CH + (wave & 1) * 240 + lane * 16) = (v4u){0, 0, 0, 0};
    TS(3);
    auto badx = [&]() {
      u32 bad = 0;
#pragma unroll
      for (int q = 0; q < 4; ++q) bad |= (gx[q].x ^ tagL) | (gs[q].x ^ tagL) | (gx[q].w ^ tagH) | (gs[q].w ^ tagH);
      return bad;
    };
    if (__any(badx() != 0)) {
      SPIN { CNT(4); __builtin_amdgcn_s_sleep(2); MEMB(); sweep(); if (!__any(badx() != 0)) break; }
    }
    mod_ready();
    gf = GLD8(gfo);
    SB();
    TS(19);
    const v2f sa = unpk(mod2(0)), g1 = unpk(pk(unpk(mod2(1)) + bc(1.f)));
    const int lo = opq(L_Z + tid * 4);
#pragma unroll
    for (int q = 0; q < 4; ++q) {
      const v2f h0 = fma2((unpk(gx[q].y) - bc(blo(gs[q].y))) * bc(__builtin_amdgcn_rcpf(bhi(gs[q].y))), g1, sa);
      const v2f h1 = fma2((unpk(gx[q].z) - bc(blo(gs[q].z))) * bc(__builtin_amdgcn_rcpf(bhi(gs[q].z))), g1, sa);
      LDS32(lo + q * 1024 + 480) = pk(h0.x, h1.x);
      LDS32(lo + q * 1024 + 480 + Z_CH) = pk(h0.y, h1.y);
      LDS32(lo + q * 1024 + (L_XT - L_Z)) = __builtin_amdgcn_perm(gx[q].z, gx[q].y, 0x05040100u);
      LDS32(lo + q * 1024 + (L_XT - L_Z) + Z_CH) = __builtin_amdgcn_perm(gx[q].z, gx[q].y, 0x07060302u);
    }
    setf(F_H);
    TS(23);
    if (__any(gf.x != tagL || gf.y != tagH)) {
      SPIN { CNT(6); MEMB(); gf = GLD8(gfo); if (!__any(gf.x != tagL || gf.y != tagH)) break; __builtin_amdgcn_s_sleep(2); }
    }
    setf(F_W);
    wait4n(F_W);
  }
  TS(5);

  v4u w2f[28];
  const int w2v = opq(lane * 16), w2o = OFF_W2T + (sl * 8 + wave) * 28 * 1024;
  auto ldw2 = [&](int u) { if (ABL & 1) { w2f[u] = (v4u){0, 0, 0, 0}; tie(w2f[u]); } else w2f[u] = GLDS(w2v + (u & 3) * 1024, w2o + (u >> 2) * 4096); SB(); };
  if constexpr (CV) {
    auto epi = [&](int stage, int u) { conv_epi(lds, rs, edm + u * 1024, elz + (4 * stage + u) * 512, ego + (4 * stage + u) * 8 * XTILE, ega, egd, eg2); };
    LAS char* pa = lds + pao;
    LAS char* pbz = lds + pbo;
    v4u B[16];
    auto rdb = [&](int slot, int rho, int dl) { B[slot] = *(LAS v4u*)(pbz + 64 * (8 * dl + 3 - rho)); SB(); };
    auto rda = [&](int rho, int a, int half) {
      const v2u u = *(LAS v2u*)(pa + 64 * (59 - 8 * a - rho) + 8 * half); SB();
      if (half) { w1f[8 * rho + a].z = u.x; w1f[8 * rho + a].w = u.y; } else { w1f[8 * rho + a].x = u.x; w1f[8 * rho + a].y = u.y; }
    };
    auto rd1 = [&](int rho, int n) {
      if (n < 2) rda(rho, 0, n); else if (n < 6) rdb(4 * rho + n - 2, rho, 5 - n); else rda(rho, (n - 4) >> 1, n & 1);
    };
    v4f acc[4];
    __builtin_amdgcn_s_setprio(1);
#pragma unroll
    for (int n = 0; n < 16; ++n) rd1(n / 12, n % 12);
#pragma unroll
    for (int t = 0; t < 4; ++t) acc[t] = (v4f){0, 0, 0, 0};
    {
      int n = 16, m = 0, c = 0;
#pragma unroll
      for (int rho = 0; rho < 4; ++rho) {
#pragma unroll
        for (int a = 0; a < 4; ++a) {
#pragma unroll
          for (int dl = 3 - a; dl >= 0; --dl) {
            acc[a + dl] = mfma16(w1f[8 * rho + a], B[4 * rho + 3 - dl], acc[a + dl]); tie(acc[a + dl]); SB();
            if (n < 48) { rd1(n / 12, n % 12); ++n; if ((c & 1) && n < 48) { rd1(n / 12, n % 12); ++n; } }
            else if (m < 8) { rdb(m, 0, 7 - m); ++m; }
            ++c;
          }
        }
      }
    }
#pragma unroll
    for (int t = 0; t < 4; ++t) LDS128F(dmo + t * 1024) = acc[t];
    setf(F_D0);
#pragma unroll
    for (int n = 0; n < 16; ++n) rda(n >> 3, 4 + ((n >> 1) & 3), n & 1);
    TS(20);
    wait1(F_D0 + ((wave & 3) ^ 1) * 4);
    TS(6);
    epi(0, 0); epi(0, 1);
    setf(F_PA);
    SB();
    TS(7);
#pragma unroll
    for (int t = 0; t < 4; ++t) acc[t] = (v4f){0, 0, 0, 0};
#pragma unroll
    for (int rho = 0; rho < 4; ++rho) {
      LGK0(); SB();
      int n = 0;
#pragma unroll
      for (int a = 0; a < 8; ++a) {
#pragma unroll
        for (int dl = 7 - a; dl >= 0 && dl >= 4 - a; --dl) {
          acc[a + dl - 4] = mfma16(w1f[8 * rho + a], B[8 * (rho & 1) + 7 - dl], acc[a + dl - 4]); tie(acc[a + dl - 4]); SB();
          if (n < 8 && rho < 3) rdb(8 * ((rho + 1) & 1) + n, rho + 1, 7 - n);
          else if (n < 16 && rho < 2) rda(rho + 2, 4 + ((n - 8) >> 1), n & 1);
          else if (n == TRIGN && rho == TRIG) setf(F_TR);
          ++n;
        }
      }
    }
    wait1(F_PA + ((wave & 3) ^ 1) * 4);
#pragma unroll
    for (int t = 0; t < 4; ++t) LDS128F(dmo + t * 1024) = acc[t];
    setf(F_D1);
    if (TRIG == 4) setf(F_TR);
    TS(21);
    wait1(F_D1 + ((wave & 3) ^ 1) * 4);
    epi(1, 0);
    if (TRIG == 5) setf(F_TR);
    epi(1, 1);
    SB();
    __builtin_amdgcn_s_setprio(0);
    TS(8);
    wait1(F_WS + (wave & 3) * 4);
    const int wso = opq(L_WS + (wave & 3) * NWS * 1024 + lane * 16);
#pragma unroll
    for (int i = 0; i < NWS; ++i) w1f[i] = LDS128(wso + i * 1024);
    setf(F_EB);
  } else {
    ldw1(0, N0);
    if (wave == 0) {
      wait8n(F_K, F_H);
      if (lane == 0) {
        GST8(((v2u){tagL, tagH}), OFF_XFL + p * 32);
        if (b == 0) GST8(((v2u){tagL, tagH}), 0);
      }
      SB();
    }
    ldw1(N0, N1);
    auto park = [&](int i) {
      if (ABL & 4) return;
      const char* src = ws + w1o + 4 * 28 * 1024;
      const u32 l16 = lane * 16;
      if ((i & 3) == 0) { set_m0(lds0 + L_WS + wave * NWS * 1024 + i * 1024); dma16_dev_o<0>(src + i * 1024, l16); }
      else if ((i & 3) == 1) dma16_dev_o<1024>(src + (i - 1) * 1024, l16);
      else if ((i & 3) == 2) dma16_dev_o<2048>(src + (i - 2) * 1024, l16);
      else dma16_dev_o<3072>(src + (i - 3) * 1024, l16);
    };
    if (NS1) wait4(F_K);
#pragma unroll
    for (int i = 0; i < NS1; ++i) park(i);
    TS(20);
    wait4(F_PA);
    ldw1(N1, 28);
#pragma unroll
    for (int i = NS1; i < NWS; ++i) park(i);
    const int xfo = OFF_XFL + (wave * 56 + (lane < 56 ? lane : 0)) * 32;
    v2u xf = GLD8(xfo);
    SB();
#pragma unroll
    for (int i = 0; i < NB1; ++i) ldw2(i);
    if (__any(xf.x != tagL || xf.y != tagH)) {
      SPIN { CNT(7); __builtin_amdgcn_s_sleep(2); MEMB(); xf = GLD8(xfo); if (!__any(xf.x != tagL || xf.y != tagH)) break; }
    }
    asm volatile("s_waitcnt vmcnt(%0)" :: "i"(NB1) : "memory");
    setf(F_WS);
    TS(21);
    wait4(F_TR);
    __builtin_amdgcn_s_setprio(XAP);
    TS(8);
  }

  const int uvo = OFF_UV + (256 * sl + 32 * wave + (lane & 31)) * 16;
  const int gbo = OFF_MOD + (560 + 16 * sl + (tid & 15)) * 16;
  v4u uv;
  const int trb = (8 * h + trq) * 64 + trc;
  const char* xg = ws + OFF_X1 + grp * XTILE + (wave & 3) * 7168;
  const u32 xl = lane * 16;
  auto dma_x = [&](int pass, int i) {
    if (ABL & 32) return;
    const int half = i >= 7, q = i - 7 * half, o = half * XHALF + (q >> 2) * 4096;
    const char* src = xg + pass * 32 * XTILE + o;
    if ((q & 3) == 0) { set_m0(lds0 + pass * XTILE + (wave & 3) * 7168 + o); dma16_dev_o<0>(src, xl); }
    else if ((q & 3) == 1) dma16_dev_o<1024>(src, xl);
    else if ((q & 3) == 2) dma16_dev_o<2048>(src, xl);
    else dma16_dev_o<3072>(src, xl);
  };
  auto accept_x = [&](int pass) {
    const int lx = pass * XTILE + (wave & 3) * 7168;
    float s, q;
    SPIN {
      u32 m = 0;
      v4u t[7], f[7];
#pragma unroll
      for (int i = 0; i < 7; ++i) t[i] = LDS128(lx + i * 1024 + lane * 16);
#pragma unroll
      for (int i = 0; i < 7; ++i) f[i] = trfrag(lds + lx + XHALF + trb + i * 1024);
#pragma unroll
      for (int i = 0; i < 7; ++i) m = max4(m, t[i]);
      s = 0.f; q = 0.f;
#pragma unroll
      for (int i = 0; i < 7; ++i) sumsq(f[i], s, q);
      if (ABL || !__any(m == SENTW || q != q)) break;
      MEMB();
#pragma unroll
      for (int i = 0; i < 7; ++i) m = max4(m, LDS128(lx + XHALF + i * 1024 + lane * 16));
      if (!__any(m == SENTW)) break;
      CNT(pass);
      __builtin_amdgcn_s_sleep(4);
#pragma unroll
      for (int i = 0; i < 14; ++i) dma_x(pass, i);
      VMW(0);
    }
    s = swap32_add(s); q = swap32_add(q);
    if (!h) { v2f o = {s, q}; LDS64F(L_ST + pass * ST_P + lane * 48 + (wave & 3) * 8) = o; }
  };
  uv = GLD(uvo);
  SB();
  if constexpr (!CV) {
#pragma unroll
    for (int i = 0; i < 14; ++i) dma_x(0, i);
    __builtin_amdgcn_s_setprio(0);
    TS(9);
#pragma unroll
    for (int i = NB1; i < NB1 + NB2; ++i) ldw2(i);
    asm volatile("s_waitcnt vmcnt(%0)" :: "i"(NB2) : "memory");
    tie(uv);
    TS(6);
    accept_x(0);
    setf(F_XA);
    TS(10);
    wait4(F_EB);
  } else {
    ldw1(NWS, 28);
    TS(10);
  }
  wait4(F_XA);
  TS(11);
  if (!__all(tagok(uv, tagL, tagH))) {
    SPIN { CNT(8); __builtin_amdgcn_s_sleep(4); MEMB(); uv = GLD(uvo); if (__all(tagok(uv, tagL, tagH))) break; }
  }
  v4u uvf;
  {
    const float u = __uint_as_float(uv.y), v = __uint_as_float(uv.z), uh = rbf(u), vh = rbf(v);
    uvf = (v4u){h ? 0u : pk(uh, u - uh), h ? 0u : pk(vh, v - vh), 0u, 0u};
  }

  const v16f zero16 = {0, 0, 0, 0, 0, 0, 0, 0, 0, 0, 0, 0, 0, 0, 0, 0};
  float sd, rl;
  auto gemm1 = [&](int pass, auto extra) {
    const int lx = pass * XTILE;
    LAS char* pb = lds + opq(lx + trb);
    v4u bf[4];
#pragma unroll
    for (int k = 0; k < 3; ++k) bf[k] = trfrag(pb + k * 1024);
    SB();
    v16f acc = zero16;
    v2u res; v4f a0; float mean, ex2; v4u bs;
    __builtin_amdgcn_s_setprio(2 - CV);
#pragma unroll
    for (int k = 0; k < 28; ++k) {
      if (k + 3 < 28) bf[(k + 3) & 3] = trfrag(pb + (k + 3) * 1024);
      acc = mfma32(w1f[k], bf[k & 3], acc); tie(acc);
      extra(k);
      if (k == 1) {
        res = trd(lds + lx + XHALF + (64 * sl + 8 * wave + 4 * h + trq) * 64 + trc);
        a0 = LDS128F(L_ST + pass * ST_P + (lane & 31) * 48 + h * 16);
      }
      if (k == 7) LDS64(L_RES + pass * RES_P + (lane & 31) * 144 + 16 * wave + 8 * h) = res;
      if (k == 9) mean = pin(swap32_add(pin(a0[0]) + opf(a0[2])) * (1.f / D));
      if (k == 11) ex2 = pin(swap32_add(pin(a0[1]) + opf(a0[3])) * (1.f / D));
      if (k == 13) ex2 = pin(rbf(fmaxf(pin(ex2) - mean * mean, 0.f)));
      if (k == 15) ex2 = pin(__builtin_amdgcn_sqrtf(rbf(pin(ex2) + 1e-5f)));
      if (k == 17) { sd = rbf(pin(ex2)); rl = pin(__builtin_amdgcn_rcpf(sd) * -1.4426950408889634f); }
      if (k == 19) { mean = pin(mean); bs = (v4u){h ? 0u : pk(-mean, -mean), h ? 0u : pk(sd, sd), 0u, 0u}; tie(bs); }
      SB();
    }
    acc = mfma32(uvf, bs, acc); tie(acc);
    SB();
    __builtin_amdgcn_s_setprio(0);
    return acc;
  };
  auto epi1 = [&](int pass, v16f acc, auto pre) {
    tie(acc);
    u32 hp[8];
#pragma unroll
    for (int e = 0; e < 8; ++e) {
      const float a0 = opf(acc[2 * e]), a1 = opf(acc[2 * e + 1]);
      const float e0_ = __builtin_amdgcn_exp2f(a0 * rl), e1_ = __builtin_amdgcn_exp2f(a1 * rl);
      hp[e] = pks(a0 * __builtin_amdgcn_rcpf(fmaf(e0_, sd, sd)), a1 * __builtin_amdgcn_rcpf(fmaf(e1_, sd, sd)));
    }
    const int ho = opq(OFF_H + (grp + 32 * pass) * HTILE + (16 * sl + 2 * wave) * 1024 + lane * 16);
    pre();
    v4u h0 = {hp[0], hp[1], hp[2], hp[3]}, h1 = {hp[4], hp[5], hp[6], hp[7]};
    if (ABL & 16) { tie(h0); tie(h1); } else { GST(h0, ho); GST(h1, ho + 1024); }
    SB();
  };
  v2u cf = GLD8(OFF_CFL + grp * 128);
  SB();
  auto h_marked = [&](int pass) {
    if (__any(cf.x != tagL || cf.y != tagH)) {
      SPIN { CNT(10 + pass); MEMB(); cf = GLD8(OFF_CFL + grp * 128 + pass * 64); if (!__any(cf.x != tagL || cf.y != tagH)) break; __builtin_amdgcn_s_sleep(4); }
    }
  };
  v16f acc;
  if constexpr (!CV) {
    acc = gemm1(0, [&](int k) { if (XBK == 0 ? !(k & 1) : k >= XBK && k < XBK + 14) dma_x(1, XBK == 0 ? k >> 1 : k - XBK); if (STAG && k == 25) setf(F_S0); });
    TS(12);
    epi1(0, acc, [&] { h_marked(0); });
    if (ABL & 16) VMW(0); else VMW(2);
    __builtin_amdgcn_s_setprio(2);
    accept_x(1);
    setf(F_XB);
    wait4(F_XB);
    cf = GLD8(OFF_CFL + grp * 128 + 64);
    SB();
    TS(13);
    acc = gemm1(1, [&](int k) { if (!(k & 1) && NB1 + NB2 + (k >> 1) < 28) ldw2(NB1 + NB2 + (k >> 1)); if (STAG && k == 25) setf(F_S1); });
  } else {
    if (STAG) wait1s(F_S0 + (wave & 3) * 4);
    acc = gemm1(0, [&](int k) { if (k >= 28 - 2 * NCA && !(k & 1)) ldw2((k - 28 + 2 * NCA) >> 1); });
    TS(12);
    epi1(0, acc, [&] { h_marked(0); });
#pragma unroll
    for (int i = NCA; i < 14; ++i) ldw2(i);
    TS(9);
    wait4(F_XB);
    cf = GLD8(OFF_CFL + grp * 128 + 64);
    SB();
    if (STAG) wait1s(F_S1 + (wave & 3) * 4);
    TS(13);
    acc = gemm1(1, [&](int k) { if (!(k & 1)) ldw2(14 + (k >> 1)); });
  }
  if (RFL && CV) setf(F_G1);
  TS(14);
  epi1(1, acc, [&] { h_marked(1); });
  if constexpr (!CV) {
#pragma unroll
    for (int i = NB1 + NB2 + 14; i < 28; ++i) ldw2(i);
  }
  TS(24);

  const int hoA = OFF_H + grp * HTILE + wave * 14 * 1024;
  v4u hf[14];
  v16f c0, c1;
  auto ldh = [&](int ho, int i0 = 0, int i1 = 14) {
    const int l16 = opq(lane * 16);
#pragma unroll
    for (int i = i0; i < i1; ++i) { if (ABL & 2) { hf[i] = (v4u){0, 0, 0, 0}; tie(hf[i]); } else hf[i] = GLDS(l16 + (i & 3) * 1024, ho + (i >> 2) * 4096); SB(); }
  };
  auto gemm2 = [&](int ho) {
    c0 = zero16; c1 = zero16;
#pragma unroll
    for (int i = 0; i < 14; ++i) { c0 = mfma32(w2f[2 * i], hf[i], c0); c1 = mfma32(w2f[2 * i + 1], hf[i], c1); tie(c0); tie(c1); SB(); }
    if (!ABL && __any(c0[0] != c0[0])) {
      SPIN {
        MEMB();
        CNT(ho == hoA ? 2 : 3);
        ldh(ho);
        u32 m = 0;
#pragma unroll
        for (int i = 0; i < 14; ++i) m = max4(m, hf[i]);
        if (!__any(m == SENTW)) break;
      }
      c0 = zero16; c1 = zero16;
#pragma unroll
      for (int i = 0; i < 14; ++i) { c0 = mfma32(w2f[2 * i], hf[i], c0); c1 = mfma32(w2f[2 * i + 1], hf[i], c1); }
    }
  };
  auto put_red = [&](int lr) {
    const int o = opq(lr + wave * RED_W + h * RED_S + (lane & 31) * 16);
#pragma unroll
    for (int q = 0; q < 4; ++q) {
      v4f o0 = {c0[4 * q], c0[4 * q + 1], c0[4 * q + 2], c0[4 * q + 3]}, o1 = {c1[4 * q], c1[4 * q + 1], c1[4 * q + 2], c1[4 * q + 3]};
      LDS128F(o + 2 * q * RED_S) = o0;
      LDS128F(o + (8 + 2 * q) * RED_S + 32) = o1;
    }
    SB();
  };
  v4u gbv;
  auto finish = [&](int pass, int t) {
    const int dq = t & 15, l = t >> 4;
    const int lr = opq(pass * L_REDB + (dq >> 1) * RED_S + (dq & 1) * (8 * RED_S + 32) + l * 16), lr2 = opq(lr + 4 * RED_W);
    v4f s0 = LDS128F(lr), s1 = LDS128F(lr2);
#pragma unroll
    for (int w = 1; w < 4; ++w) { s0 += LDS128F(lr + w * RED_W); s1 += LDS128F(lr2 + w * RED_W); }
    s0 += s1;
    const v2u rv = LDS64(L_RES + pass * RES_P + l * 144 + dq * 8);
    const v2u o = {pk(fma2(unpk(gbv.y), s0.xy, unpk(rv.x))), pk(fma2(unpk(gbv.z), s0.zw, unpk(rv.y)))};
    *(GAS v2u*)(out + (pass * 1024 + grp * 32 + l) * D + 64 * sl + 4 * dq) = o;
  };
  gbv = GLD(gbo);
  SB();
  if (RFL && !CV) { wait1s(F_G1 + (wave & 3) * 4); ldh(hoA, 0, NHE); }
  BAR();
  TS(15);
  __builtin_amdgcn_s_setprio(1);
  ldh(hoA, RFL && !CV ? NHE : 0, 14);
  __builtin_amdgcn_s_setprio(0);
  gemm2(hoA);
  if (!__all(tagok(gbv, tagL, tagH))) {
    SPIN { CNT(9); __builtin_amdgcn_s_sleep(4); MEMB(); gbv = GLD(gbo); if (__all(tagok(gbv, tagL, tagH))) break; }
  }
  TS(16);
  put_red(L_REDA);
  setf(F_RA + CV * 16);
  ldh(hoA + 32 * HTILE);
  TS(22);
  if (TAIL == 1 && !CV) {
    wait4(F_RA); wait4(F_RA + 16);
    finish(0, tid); finish(0, tid + 256);
  }
  SB();
  TS(4);
  gemm2(hoA + 32 * HTILE);
  put_red(L_REDB);
  TS(17);
  if (TAIL == 2 && !CV) { wait4(F_RA); wait4(F_RA + 16); finish(0, tid); finish(0, tid + 256); }
  BAR();
  if (TAIL == 0) finish(0, tid);
  finish(1, tid);
  };
  if (wave >= 4) rest(Role<1>{}); else rest(Role<0>{});
  TS(29); TSEND();
}

extern "C" void kernel_launch(void* const* d_in, const int* in_sizes, int n_in, void* d_out, int out_size, void* d_ws, size_t ws_size,
                              hipStream_t stream) {
  k_all<<<256, 512, 0, stream>>>((const u16*)d_in[0], (const u16*)d_in[1], (const u16*)d_in[2], (const u16*)d_in[3], (const u16*)d_in[4],
                                 (const u16*)d_in[5], (const u16*)d_in[6], (const u16*)d_in[7], (const u16*)d_in[8], (const u16*)d_in[10],
                                 (const u16*)d_in[11], (const u16*)d_in[12], (const u16*)d_in[13], (u16*)d_out, (char*)d_ws + WSOFF);
}
```
